```python
import torch

def _softmax(x, axis=-1):
    return torch.softmax(x, dim=axis)
def _gelu(x):
    return torch.nn.functional.gelu(x, approximate="tanh")
def _silu(x):
    return torch.nn.functional.silu(x)
def _sigmoid(x):
    return torch.sigmoid(x)
def _layernorm(x, axis=-1, eps=1e-5):
    m = x.mean(dim=axis, keepdim=True)
    v = x.var(dim=axis, keepdim=True, unbiased=False)
    return (x - m) / torch.sqrt(v + eps)
def _scatter_add(tgt, idx, src, axis=0):
    return tgt.clone().index_add_(axis, idx, src)
def _segment_sum(src, seg, n):
    out = torch.zeros((n,) + src.shape[1:], dtype=src.dtype)
    return out.index_add_(0, seg, src)
def _pad(x, pw):
    flat = tuple(v for pair in reversed(pw) for v in pair)
    return torch.nn.functional.pad(x, flat)

kernel_name = 'concat_heads__ssm_conv_s4__spectral_gfnet__prompt'

def setup_inputs():

    _w = (lambda a: (lambda t: t.to(torch.bfloat16) if t.is_floating_point() else t)(torch.as_tensor(a)))
    def _A_setup():
        B, L, D, N = 1, 2048, 1024, 128
        A_re = -(lambda _x: torch.exp(torch.as_tensor(_x)))((lambda *s: torch.randn(*s, dtype=torch.bfloat16) * 3.2)(D, N))
        A_im = torch.pi * (lambda *s: torch.randn(*s, dtype=torch.bfloat16) * 0.05)(D, N)
        B_re = (lambda *s: torch.randn(*s, dtype=torch.bfloat16) * 0.1)(D, N)
        B_im = (lambda *s: torch.randn(*s, dtype=torch.bfloat16) * 0.1)(D, N)
        C_re = (lambda *s: torch.randn(*s, dtype=torch.bfloat16) * 0.1)(D, N)
        C_im = (lambda *s: torch.randn(*s, dtype=torch.bfloat16) * 0.1)(D, N)
        log_dt = (lambda *s: torch.randn(*s, dtype=torch.bfloat16) * 0.05)(D) * 0.5 - 4.0
        Dskip = (lambda *s: torch.randn(*s, dtype=torch.bfloat16) * 0.05)(D)
        return {
            "u": _w((lambda *s: torch.randn(*s, dtype=torch.bfloat16) * 0.05)(B, L, D)),
            "A_re": _w(A_re),
            "A_im": _w(A_im),
            "B_re": _w(B_re),
            "B_im": _w(B_im),
            "C_re": _w(C_re),
            "C_im": _w(C_im),
            "log_dt": _w(log_dt),
            "Dskip": _w(Dskip),
        }
    _A_in = _A_setup()

    def _B_setup():
        B, S, D = 1, 2048, 1024
        Df = D // 2 + 1
        return {
            "x": _w((lambda *s: torch.randn(*s, dtype=torch.bfloat16) * 0.05)(B, S, D)),
            "w_r": _w((lambda *s: torch.randn(*s, dtype=torch.bfloat16) * 0.8)(S, Df) * 0.02),
            "w_i": _w((lambda *s: torch.randn(*s, dtype=torch.bfloat16) * 0.8)(S, Df) * 0.02),
        }
    _B_in = _B_setup()

    return {
        "A_u": _A_in["u"],
        "A_A_re": _A_in["A_re"],
        "A_A_im": _A_in["A_im"],
        "A_B_re": _A_in["B_re"],
        "A_B_im": _A_in["B_im"],
        "A_C_re": _A_in["C_re"],
        "A_C_im": _A_in["C_im"],
        "A_log_dt": _A_in["log_dt"],
        "A_Dskip": _A_in["Dskip"],
        "B_x": _B_in["x"],
        "B_w_r": _B_in["w_r"],
        "B_w_i": _B_in["w_i"],
    }

def reference(A_u, A_A_re, A_A_im, A_B_re, A_B_im, A_C_re, A_C_im, A_log_dt, A_Dskip, B_x, B_w_r, B_w_i):
    def _A_ref(u, A_re, A_im, B_re, B_im, C_re, C_im, log_dt, Dskip):
        Bsz, L, D = u.shape
        dt = torch.exp(log_dt)[:, None]
        A = A_re + 1j * A_im
        Bc = B_re + 1j * B_im
        Cc = C_re + 1j * C_im
        logdA = dt * A
        dB = dt * Bc
        l = (lambda x, dt: x.to(dt))(torch.arange(L), A_re.dtype)
        P = torch.exp(logdA[:, :, None] * l[None, None, :])
        K = torch.einsum("dn,dnl,dn->dl", Cc, P, dB).real
        L2 = 2 * L
        Kp = _pad(K, ((0, 0), (0, L)))
        up = (lambda x, *d: x.permute(*d))(u, 0, 2, 1)
        up = _pad(up, ((0, 0), (0, 0), (0, L)))
        Kf = (lambda x, axes=(-2,-1): torch.fft.rfft2(x.float(), dim=axes))(Kp[:, None, :], axes=(-2, -1))
        Uf = (lambda x, axes=(-2,-1): torch.fft.rfft2(x.float(), dim=axes))(up[:, :, None, :], axes=(-2, -1))
        Yf = Uf * Kf[None, :, :, :]
        y = (lambda x, s, axes=(-2,-1): torch.fft.irfft2(x, s=s, dim=axes).to(torch.bfloat16))(Yf, (1, L2), axes=(-2, -1))[:, :, 0, :L]
        y = (lambda x, *d: x.permute(*d))(y, 0, 2, 1) + u * Dskip[None, None, :]
        return y

    def _B_ref(x, w_r, w_i):
        B, S, D = x.shape
        X = (lambda x, axes=(-2,-1): torch.fft.rfft2(x.float(), dim=axes))(x, axes=(-2, -1))
        W = w_r + 1j * w_i
        X = X * W
        y = (lambda x, s, axes=(-2,-1): torch.fft.irfft2(x, s=s, dim=axes).to(torch.bfloat16))(X, (S, D), axes=(-2, -1))
        return y

    a = _A_ref(A_u, A_A_re, A_A_im, A_B_re, A_B_im, A_C_re, A_C_im, A_log_dt, A_Dskip)
    b = _B_ref(B_x, B_w_r, B_w_i)
    return torch.concatenate([a, b], axis=-1)
```

```cpp
#include <hip/hip_runtime.h>
#include <hip/hip_bf16.h>
#include <type_traits>

#define SEQ 2048
#define DCH 1024
#define NST 128
#define DF 513

typedef float2 cf;
typedef unsigned int u32;
typedef unsigned short u16;
using f32x16 = __attribute__((ext_vector_type(16))) float;

template <int I, int N, class F>
__device__ __forceinline__ void static_for(F&& f) {
  if constexpr (I < N) {
    f(std::integral_constant<int, I>{});
    static_for<I + 1, N>(f);
  }
}

__device__ __forceinline__ cf cmk(float x, float y) { cf r; r.x = x; r.y = y; return r; }
__device__ __forceinline__ cf cadd(cf a, cf b) { return cmk(a.x + b.x, a.y + b.y); }
__device__ __forceinline__ cf csub(cf a, cf b) { return cmk(a.x - b.x, a.y - b.y); }
__device__ __forceinline__ cf cmul(cf a, cf b) {
  return cmk(fmaf(a.x, b.x, -(a.y * b.y)), fmaf(a.x, b.y, a.y * b.x));
}
__device__ __forceinline__ cf cmulc(cf a, cf b) {
  return cmk(fmaf(a.x, b.x, a.y * b.y), fmaf(a.y, b.x, -(a.x * b.y)));
}
__device__ __forceinline__ cf csqr(cf a) { return cmk(fmaf(a.x, a.x, -(a.y * a.y)), 2.0f * a.x * a.y); }

__device__ __forceinline__ float bf2f(u32 h) { return __uint_as_float(h << 16); }
__device__ __forceinline__ u32 f2bf(float f) {
  __hip_bfloat16 b = __float2bfloat16(f);
  return (u32)(*reinterpret_cast<u16*>(&b));
}
__device__ __forceinline__ float bfround(float f) { return bf2f(f2bf(f)); }

__device__ __forceinline__ void wave_sync() {
  __builtin_amdgcn_fence(__ATOMIC_SEQ_CST, "wavefront");
  __builtin_amdgcn_wave_barrier();
  __builtin_amdgcn_fence(__ATOMIC_SEQ_CST, "wavefront");
}

constexpr double COS64_TAB[17] = {
    1.0, 0.99518472667219688624, 0.98078528040323044913, 0.95694033573220886494,
    0.92387953251128675613, 0.88192126434835502971, 0.83146961230254523708,
    0.77301045336273696081, 0.70710678118654752440, 0.63439328416364549822,
    0.55557023301960222474, 0.47139673682599764856, 0.38268343236508977173,
    0.29028467725446236764, 0.19509032201612826785, 0.09801714032956060199, 0.0};
constexpr double cos64(int j) {
  j = ((j % 64) + 64) % 64;
  if (j > 32) j = 64 - j;
  return (j <= 16) ? COS64_TAB[j] : -COS64_TAB[32 - j];
}
constexpr double sin64(int j) { return cos64(j - 16); }

template <int J, int DIR>
__device__ __forceinline__ cf tw64(cf a) {
  constexpr int j = ((J % 64) + 64) % 64;
  if constexpr (j == 0) {
    return a;
  } else if constexpr (j == 32) {
    return cmk(-a.x, -a.y);
  } else if constexpr (j == 16) {
    return (DIR > 0) ? cmk(-a.y, a.x) : cmk(a.y, -a.x);
  } else if constexpr (j == 48) {
    return (DIR > 0) ? cmk(a.y, -a.x) : cmk(-a.y, a.x);
  } else if constexpr (j % 16 == 8) {
    constexpr float h = 0.70710678118654752440f;
    constexpr float c = (float)cos64(j);
    constexpr float s = (float)(DIR * sin64(j));
    constexpr float cs = (c > 0 ? 1.f : -1.f), ss = (s > 0 ? 1.f : -1.f);
    float re = cs * a.x - ss * a.y;
    float im = ss * a.x + cs * a.y;
    return cmk(re * h, im * h);
  } else {
    constexpr float c = (float)cos64(j);
    constexpr float s = (float)(DIR * sin64(j));
    return cmk(fmaf(a.x, c, -(a.y * s)), fmaf(a.x, s, a.y * c));
  }
}

template <int N, int DIR, int S>
__device__ __forceinline__ void fft_rec(const cf* in, cf* out) {
  if constexpr (N == 1) {
    out[0] = in[0];
  } else if constexpr (N == 2) {
    cf a = in[0], b = in[S];
    out[0] = cadd(a, b);
    out[1] = csub(a, b);
  } else {
    constexpr int Q = N / 4;
    cf t0[Q], t1[Q], t2[Q], t3[Q];
    fft_rec<Q, DIR, 4 * S>(in, t0);
    fft_rec<Q, DIR, 4 * S>(in + S, t1);
    fft_rec<Q, DIR, 4 * S>(in + 2 * S, t2);
    fft_rec<Q, DIR, 4 * S>(in + 3 * S, t3);
    static_for<0, Q>([&](auto ic) {
      constexpr int k = decltype(ic)::value;
      constexpr int M = 64 / N;
      cf a = t0[k];
      cf b = tw64<k * M, DIR>(t1[k]);
      cf c = tw64<2 * k * M, DIR>(t2[k]);
      cf d = tw64<3 * k * M, DIR>(t3[k]);
      cf s0 = cadd(a, c), s1 = csub(a, c), s2 = cadd(b, d), s3 = csub(b, d);
      cf s3r = (DIR > 0) ? cmk(-s3.y, s3.x) : cmk(s3.y, -s3.x);
      out[k] = cadd(s0, s2);
      out[k + Q] = cadd(s1, s3r);
      out[k + 2 * Q] = csub(s0, s2);
      out[k + 3 * Q] = csub(s1, s3r);
    });
  }
}

template <int N, int DIR>
__device__ __forceinline__ void fft_reg(cf* v) {
  cf o[N];
  fft_rec<N, DIR, 1>(v, o);
#pragma unroll
  for (int i = 0; i < N; ++i) v[i] = o[i];
}

template <int DIR>
__device__ __forceinline__ void fft32_halfzero(cf* v) {
  cf a[16], b[16], oa[16], ob[16];
  static_for<0, 16>([&](auto ic) {
    constexpr int n = decltype(ic)::value;
    a[n] = v[n];
    b[n] = tw64<2 * n, DIR>(v[n]);
  });
  fft_rec<16, DIR, 1>(a, oa);
  fft_rec<16, DIR, 1>(b, ob);
#pragma unroll
  for (int k = 0; k < 16; ++k) {
    v[2 * k] = oa[k];
    v[2 * k + 1] = ob[k];
  }
}

struct TwBase { cf w[5]; };

__device__ __forceinline__ TwBase make_twbase(int n, int logN) {
  TwBase b;
#pragma unroll
  for (int t = 0; t < 5; ++t) {
    int e = (n << t) & ((1 << logN) - 1);
    float s, c;
    sincospif(-2.0f * (float)e / (float)(1 << logN), &s, &c);
    b.w[t] = cmk(c, s);
  }
  return b;
}

template <bool CONJ>
__device__ __forceinline__ void apply_tw32(cf* v, const TwBase& b) {
  cf p[32];
  p[1] = b.w[0];
  p[2] = b.w[1];
  p[4] = b.w[2];
  p[8] = b.w[3];
  p[16] = b.w[4];
  static_for<3, 32>([&](auto ic) {
    constexpr int k = decltype(ic)::value;
    constexpr int top = (k >= 16) ? 16 : (k >= 8) ? 8 : (k >= 4) ? 4 : 2;
    if constexpr (k != top) p[k] = cmul(p[k - top], p[top]);
  });
#pragma unroll
  for (int k = 1; k < 32; ++k) v[k] = CONJ ? cmulc(v[k], p[k]) : cmul(v[k], p[k]);
}

__device__ __forceinline__ void swap32(cf& a, cf& b) {
  auto r0 = __builtin_amdgcn_permlane32_swap(__float_as_uint(a.x), __float_as_uint(b.x), false, false);
  auto r1 = __builtin_amdgcn_permlane32_swap(__float_as_uint(a.y), __float_as_uint(b.y), false, false);
  a.x = __uint_as_float(r0[0]);
  b.x = __uint_as_float(r0[1]);
  a.y = __uint_as_float(r1[0]);
  b.y = __uint_as_float(r1[1]);
}

#define FP 74
#define FO 40
#define IP 82
#define IO 48
#define XBUF (32 * IP)

__device__ __forceinline__ void fft2048_fwd_p2(cf* v, cf* buf, int lane) {
  {
    int pos = (lane & 1) * FO + (lane >> 1);
#pragma unroll
    for (int k1 = 0; k1 < 32; ++k1) buf[k1 * FP + pos] = v[k1];
  }
  wave_sync();
  {
    int k1 = lane & 31, h = lane >> 5;
    const float4* p = reinterpret_cast<const float4*>(buf + k1 * FP + h * FO);
#pragma unroll
    for (int m = 0; m < 16; ++m) {
      float4 t = p[m];
      v[2 * m] = cmk(t.x, t.y);
      v[2 * m + 1] = cmk(t.z, t.w);
    }
  }
  wave_sync();
  fft_reg<32, -1>(v);
  bool hi = lane >= 32;
  static_for<0, 16>([&](auto ic) {
    constexpr int j = decltype(ic)::value;
    cf a = v[j], b = v[j + 16];
    swap32(a, b);
    cf br = cmk(hi ? b.y : b.x, hi ? -b.x : b.y);
    br = tw64<j, -1>(br);
    v[j] = cadd(a, br);
    v[j + 16] = csub(a, br);
  });
}

__device__ __forceinline__ void fft2048_inv_p1(cf* v, cf* buf, int lane) {
  bool hi = lane >= 32;
  static_for<0, 16>([&](auto ic) {
    constexpr int j = decltype(ic)::value;
    cf a = cadd(v[j], v[j + 16]);
    cf d = csub(v[j], v[j + 16]);
    cf dr = cmk(hi ? -d.y : d.x, hi ? d.x : d.y);
    dr = tw64<j, 1>(dr);
    swap32(a, dr);
    v[j] = a;
    v[j + 16] = dr;
  });
  fft_reg<32, 1>(v);
  {
    int k1 = lane & 31, h = lane >> 5;
    float4* p = reinterpret_cast<float4*>(buf + k1 * IP + h * IO);
#pragma unroll
    for (int m = 0; m < 16; ++m) p[m] = make_float4(v[2 * m].x, v[2 * m].y, v[2 * m + 1].x, v[2 * m + 1].y);
  }
  wave_sync();
  {
    int pos = (lane & 1) * IO + (lane >> 1);
#pragma unroll
    for (int k1 = 0; k1 < 32; ++k1) v[k1] = buf[k1 * IP + pos];
  }
  wave_sync();
}

__device__ __forceinline__ void mirror2048(const cf* v, cf* m, cf* buf, int lane) {
  int k1 = lane & 31, h = lane >> 5;
  int kb = k1 + 512 * h;
#pragma unroll
  for (int r = 0; r < 32; ++r) {
    int k = kb + 32 * (r & 15) + 1024 * (r >> 4);
    buf[k] = v[r];
  }
  wave_sync();
#pragma unroll
  for (int r = 0; r < 32; ++r) {
    int k = kb + 32 * (r & 15) + 1024 * (r >> 4);
    m[r] = buf[(2048 - k) & 2047];
  }
  wave_sync();
}

#define KC_PITCH 648
#define KREC 132

__device__ __forceinline__ cf cexp_scaled(float ar, float ai, float sc) {
  float e = expf(ar * sc);
  float s, c;
  sincosf(ai * sc, &s, &c);
  return cmk(e * c, e * s);
}

__device__ __forceinline__ void kgen_wave(const u16* __restrict__ Are, const u16* __restrict__ Aim,
                                          const u16* __restrict__ Bre, const u16* __restrict__ Bim,
                                          const u16* __restrict__ Cre, const u16* __restrict__ Cim,
                                          const u16* __restrict__ logdt, float* __restrict__ Kc,
                                          float* lds, int d, int lane) {
  float dt = bfround(expf(bf2f(logdt[d])));
  f32x16 acc;
#pragma unroll
  for (int i = 0; i < 16; ++i) acc[i] = 0.f;
  for (int round = 0; round < 2; ++round) {
    int n = round * 64 + lane;
    int idx = d * NST + n;
    float ar = dt * bf2f(Are[idx]), ai = dt * bf2f(Aim[idx]);
    cf dB = cmk(dt * bf2f(Bre[idx]), dt * bf2f(Bim[idx]));
    cf C = cmk(bf2f(Cre[idx]), bf2f(Cim[idx]));
    cf c0 = cmul(C, dB);
    cf e[12];
    e[0] = cexp_scaled(ar, ai, 1.0f);
    e[4] = cexp_scaled(ar, ai, 16.0f);
    e[8] = cexp_scaled(ar, ai, 256.0f);
    e[1] = csqr(e[0]); e[2] = csqr(e[1]); e[3] = csqr(e[2]);
    e[5] = csqr(e[4]); e[6] = csqr(e[5]); e[7] = csqr(e[6]);
    e[9] = csqr(e[8]); e[10] = csqr(e[9]); e[11] = csqr(e[10]);
    cf G[32], E[32];
    G[0] = c0;
    G[1] = cmul(c0, e[0]);
    G[2] = cmul(c0, e[1]);
    G[3] = cmul(G[1], e[1]);
    G[4] = cmul(c0, e[2]);
    G[5] = cmul(G[1], e[2]);
    G[6] = cmul(G[2], e[2]);
    G[7] = cmul(G[3], e[2]);
    G[8] = cmul(c0, e[8]);
    G[9] = cmul(G[2], e[8]);
    G[10] = cmul(G[4], e[8]);
    G[11] = cmul(G[6], e[8]);
    G[12] = cmul(c0, e[9]);
    G[13] = cmul(G[4], e[9]);
    G[14] = cmul(G[8], e[9]);
    G[15] = cmul(G[10], e[9]);
    G[16] = cmul(c0, e[10]);
    G[17] = cmul(G[8], e[10]);
    G[18] = cmul(G[12], e[10]);
    G[19] = cmul(G[14], e[10]);
    G[20] = cmul(c0, e[11]);
#pragma unroll
    for (int i = 21; i < 32; ++i) G[i] = cmk(0.f, 0.f);
    E[0] = cmk(1.f, 0.f);
    E[1] = e[3]; E[2] = e[4]; E[4] = e[5]; E[8] = e[6]; E[16] = e[7];
    static_for<3, 32>([&](auto ic) {
      constexpr int k = decltype(ic)::value;
      constexpr int top = (k >= 16) ? 16 : (k >= 8) ? 8 : (k >= 4) ? 4 : 2;
      if constexpr (k != top) E[k] = cmul(E[k - top], E[top]);
    });
    float4* rec = reinterpret_cast<float4*>(lds + lane * KREC);
#pragma unroll
    for (int l2 = 0; l2 < 16; ++l2) {
      rec[l2] = make_float4(G[2 * l2].x, E[2 * l2].x, G[2 * l2 + 1].x, E[2 * l2 + 1].x);
      rec[16 + l2] = make_float4(-G[2 * l2].y, E[2 * l2].y, -G[2 * l2 + 1].y, E[2 * l2 + 1].y);
    }
    wave_sync();
#pragma unroll 8
    for (int nn = 0; nn < 64; ++nn) {
      float2 ab = *reinterpret_cast<const float2*>(lds + nn * KREC + 2 * lane);
      acc = __builtin_amdgcn_mfma_f32_32x32x2f32(ab.x, ab.y, acc, 0, 0, 0);
    }
    wave_sync();
  }
  int col = lane & 31, hh = lane >> 5;
  float* dst = Kc + (size_t)d * KC_PITCH;
#pragma unroll
  for (int r = 0; r < 16; ++r) {
    int row = (r & 3) + 8 * (r >> 2) + 4 * hh;
    int p = -1;
    if (row < 8) p = row + 8 * col;
    else if (row < 12) p = 256 + (row - 8) + 4 * col;
    else if (row < 16) p = 384 + ((row - 12) & 1) + 64 * ((row - 12) >> 1) + 2 * col;
    else if (row < 20) p = 512 + 32 * (row - 16) + col;
    else if (row == 20 && col == 0) p = 640;
    if (p >= 0) dst[p] = acc[r];
  }
}

__global__ __launch_bounds__(128) void k_kgen(const u16* Are, const u16* Aim, const u16* Bre, const u16* Bim,
                                              const u16* Cre, const u16* Cim, const u16* logdt, float* Kc) {
  __shared__ __attribute__((aligned(16))) float lds[2 * 64 * KREC];
  int wave = threadIdx.x >> 6, lane = threadIdx.x & 63;
  int d = blockIdx.x * 2 + wave;
  kgen_wave(Are, Aim, Bre, Bim, Cre, Cim, logdt, Kc, lds + wave * 64 * KREC, d, lane);
}

__global__ __launch_bounds__(256) void k_wt(const u16* __restrict__ wr, const u16* __restrict__ wi, u32* __restrict__ WT) {
  __shared__ u32 tile[64][65];
  int bk = blockIdx.x % 9, bs = blockIdx.x / 9;
  int k0 = bk * 64, s0 = bs * 64;
  int tx = threadIdx.x & 63, ty = threadIdx.x >> 6;
  for (int r = ty; r < 64; r += 4) {
    int k = k0 + tx, s = s0 + r;
    u32 val = 0;
    if (k < DF) val = (u32)wr[s * DF + k] | ((u32)wi[s * DF + k] << 16);
    tile[r][tx] = val;
  }
  __syncthreads();
  for (int r = ty; r < 64; r += 4) {
    int k = k0 + r, s = s0 + tx;
    if (k < DF) WT[(size_t)k * SEQ + s] = tile[tx][r];
  }
}

#define RP 18
#define RRS (32 * RP)
#define ZP 513

__global__ __launch_bounds__(256) void k_brow(const u16* __restrict__ x, cf* __restrict__ X1T) {
  __shared__ __attribute__((aligned(16))) cf sm[16 * RRS + 260];
  cf* tw = sm + 16 * RRS;
  int t = threadIdx.x, wave = t >> 6, lane = t & 63;
  int s0 = blockIdx.x * 16;
  for (int k = t; k <= 256; k += 256) {
    float s, c;
    sincospif(-(float)k / 512.0f, &s, &c);
    tw[k] = cmk(c, s);
  }
  int rl = lane >> 4, n2 = lane & 15;
  int row = wave * 4 + rl;
  cf v[32];
  const u32* xr = reinterpret_cast<const u32*>(x + (size_t)(s0 + row) * DCH);
#pragma unroll
  for (int n1 = 0; n1 < 32; ++n1) {
    u32 w = xr[16 * n1 + n2];
    v[n1] = cmk(bf2f(w & 0xffffu), bf2f(w >> 16));
  }
  TwBase tb = make_twbase(n2, 9);
  fft_reg<32, -1>(v);
  apply_tw32<false>(v, tb);
  cf* rb = sm + row * RRS;
#pragma unroll
  for (int k1 = 0; k1 < 32; ++k1) rb[k1 * RP + n2] = v[k1];
  wave_sync();
#pragma unroll
  for (int tt = 0; tt < 2; ++tt) {
    const float4* p = reinterpret_cast<const float4*>(rb + (n2 + 16 * tt) * RP);
#pragma unroll
    for (int m = 0; m < 8; ++m) {
      float4 q = p[m];
      v[16 * tt + 2 * m] = cmk(q.x, q.y);
      v[16 * tt + 2 * m + 1] = cmk(q.z, q.w);
    }
  }
  fft_reg<16, -1>(v);
  fft_reg<16, -1>(v + 16);
  __syncthreads();
  cf* zs = sm + row * ZP;
#pragma unroll
  for (int tt = 0; tt < 2; ++tt)
#pragma unroll
    for (int k2 = 0; k2 < 16; ++k2) zs[n2 + 16 * tt + 32 * k2] = v[16 * tt + k2];
  __syncthreads();
  int r = t & 15, kk = t >> 4;
  const cf* zr = sm + r * ZP;
  for (int k = kk; k <= 256; k += 16) {
    cf a = zr[k];
    cf b = zr[(512 - k) & 511];
    if (k == 0) {
      X1T[(size_t)0 * SEQ + s0 + r] = cmk(a.x + a.y, a.x - a.y);
    } else {
      cf w = tw[k];
      cf ev = cmk(0.5f * (a.x + b.x), 0.5f * (a.y - b.y));
      cf od = cmk(0.5f * (a.y + b.y), -0.5f * (a.x - b.x));
      cf wo = cmul(od, w);
      X1T[(size_t)k * SEQ + s0 + r] = cadd(ev, wo);
      if (k != 256) {
        X1T[(size_t)(512 - k) * SEQ + s0 + r] = cmk(ev.x - wo.x, -(ev.y - wo.y));
      }
    }
  }
}

__device__ __forceinline__ cf ldw(const u32* p, int i) {
  u32 w = p[i];
  return cmk(bf2f(w & 0xffffu), bf2f(w >> 16));
}

__device__ __forceinline__ void bcol_wave(cf* __restrict__ X1T, const u32* __restrict__ WT, cf* buf, int col, int lane) {
  cf v[32];
  cf* xc = X1T + (size_t)col * SEQ;
#pragma unroll
  for (int n1 = 0; n1 < 32; ++n1) v[n1] = xc[64 * n1 + lane];
  TwBase tb = make_twbase(lane, 11);
  fft_reg<32, -1>(v);
  apply_tw32<false>(v, tb);
  fft2048_fwd_p2(v, buf, lane);
  const float sc = 1.0f / (2048.0f * 1024.0f);
  int k1 = lane & 31, h = lane >> 5;
  int kb = k1 + 512 * h;
  if (col != 0) {
    const u32* wc = WT + (size_t)col * SEQ;
#pragma unroll
    for (int r = 0; r < 32; ++r) {
      int k = kb + 32 * (r & 15) + 1024 * (r >> 4);
      cf w = ldw(wc, k);
      w.x *= sc; w.y *= sc;
      v[r] = cmul(v[r], w);
    }
  } else {
    cf m[32];
    mirror2048(v, m, buf, lane);
    const u32* w0 = WT;
    const u32* w1 = WT + (size_t)512 * SEQ;
#pragma unroll
    for (int r = 0; r < 32; ++r) {
      int k = kb + 32 * (r & 15) + 1024 * (r >> 4);
      int km = (2048 - k) & 2047;
      cf a = v[r], b = m[r];
      cf A = cmk(0.5f * (a.x + b.x), 0.5f * (a.y - b.y));
      cf B = cmk(0.5f * (a.y + b.y), -0.5f * (a.x - b.x));
      cf p = ldw(w0, k), q = ldw(w0, km);
      cf wh0 = cmk(0.5f * sc * (p.x + q.x), 0.5f * sc * (p.y - q.y));
      p = ldw(w1, k); q = ldw(w1, km);
      cf wh1 = cmk(0.5f * sc * (p.x + q.x), 0.5f * sc * (p.y - q.y));
      cf h0 = cmul(A, wh0), h1 = cmul(B, wh1);
      v[r] = cmk(h0.x - h1.y, h0.y + h1.x);
    }
  }
  fft2048_inv_p1(v, buf, lane);
  apply_tw32<true>(v, tb);
  fft_reg<32, 1>(v);
#pragma unroll
  for (int n1 = 0; n1 < 32; ++n1) xc[64 * n1 + lane] = v[n1];
}

__global__ __launch_bounds__(256) void k_bcol(cf* __restrict__ X1T, const u32* __restrict__ WT) {
  __shared__ __attribute__((aligned(16))) cf sm[4 * XBUF];
  int wave = threadIdx.x >> 6, lane = threadIdx.x & 63;
  int col = blockIdx.x * 4 + wave;
  bcol_wave(X1T, WT, sm + wave * XBUF, col, lane);
}

__global__ __launch_bounds__(256) void k_browinv(const cf* __restrict__ X1T, u16* __restrict__ out) {
  __shared__ __attribute__((aligned(16))) cf sm[16 * RRS + 260];
  cf* tw = sm + 16 * RRS;
  int t = threadIdx.x, wave = t >> 6, lane = t & 63;
  int s0 = blockIdx.x * 16;
  for (int k = t; k <= 256; k += 256) {
    float s, c;
    sincospif(-(float)k / 512.0f, &s, &c);
    tw[k] = cmk(c, s);
  }
  __syncthreads();
  {
    int r = t & 15, kk = t >> 4;
    cf* zr = sm + r * ZP;
    for (int k = kk; k <= 256; k += 16) {
      cf a = X1T[(size_t)k * SEQ + s0 + r];
      if (k == 0) {
        zr[0] = cmk(a.x + a.y, a.x - a.y);
      } else {
        cf b = X1T[(size_t)(512 - k) * SEQ + s0 + r];
        cf w = tw[k];
        cf ev = cmk(a.x + b.x, a.y - b.y);
        cf df = cmk(a.x - b.x, a.y + b.y);
        cf od = cmulc(df, w);
        zr[k] = cmk(ev.x - od.y, ev.y + od.x);
        if (k != 256) {
          zr[512 - k] = cmk(ev.x + od.y, -ev.y + od.x);
        }
      }
    }
  }
  __syncthreads();
  int rl = lane >> 4, n2 = lane & 15;
  int row = wave * 4 + rl;
  cf v[32];
  {
    const cf* zs = sm + row * ZP;
#pragma unroll
    for (int tt = 0; tt < 2; ++tt)
#pragma unroll
      for (int k2 = 0; k2 < 16; ++k2) v[16 * tt + k2] = zs[n2 + 16 * tt + 32 * k2];
  }
  __syncthreads();
  fft_reg<16, 1>(v);
  fft_reg<16, 1>(v + 16);
  cf* rb = sm + row * RRS;
#pragma unroll
  for (int tt = 0; tt < 2; ++tt) {
    float4* p = reinterpret_cast<float4*>(rb + (n2 + 16 * tt) * RP);
#pragma unroll
    for (int m = 0; m < 8; ++m)
      p[m] = make_float4(v[16 * tt + 2 * m].x, v[16 * tt + 2 * m].y, v[16 * tt + 2 * m + 1].x, v[16 * tt + 2 * m + 1].y);
  }
  wave_sync();
#pragma unroll
  for (int k1 = 0; k1 < 32; ++k1) v[k1] = rb[k1 * RP + n2];
  TwBase tb = make_twbase(n2, 9);
  apply_tw32<true>(v, tb);
  fft_reg<32, 1>(v);
  u32* orow = reinterpret_cast<u32*>(out + (size_t)(s0 + row) * (2 * DCH) + DCH);
#pragma unroll
  for (int n1 = 0; n1 < 32; ++n1) orow[16 * n1 + n2] = f2bf(v[n1].x) | (f2bf(v[n1].y) << 16);
}

__device__ __forceinline__ int kpos(int l, int s) {
  if (s == 0) return l;
  int half = 1 << (s - 1);
  int q = (l + half - 1 + ((l >> s) & 1)) >> s;
  return 128 * (s + 1) + q - 128;
}

__global__ __launch_bounds__(256) void k_aconv(const u16* __restrict__ u, const float* __restrict__ Kc,
                                               const u16* __restrict__ Dskip, u16* __restrict__ out) {
  __shared__ __attribute__((aligned(16))) cf sm[4 * XBUF];
  int t = threadIdx.x, wave = t >> 6, lane = t & 63;
  int grp = (blockIdx.x & 7) * 32 + (blockIdx.x >> 3);
  int d0 = grp * 4;
  int d = d0 + wave;
  cf* buf = sm + wave * XBUF;
  {
#pragma unroll
    for (int i = 0; i < 4; ++i) {
      int m = t + 256 * i;
      uint2 a = *reinterpret_cast<const uint2*>(u + (size_t)(2 * m) * DCH + d0);
      uint2 b = *reinterpret_cast<const uint2*>(u + (size_t)(2 * m + 1) * DCH + d0);
      u32 w0 = (a.x & 0xffffu) | (b.x << 16);
      u32 w1 = (a.x >> 16) | (b.x & 0xffff0000u);
      u32 w2 = (a.y & 0xffffu) | (b.y << 16);
      u32 w3 = (a.y >> 16) | (b.y & 0xffff0000u);
      reinterpret_cast<u32*>(sm + 0 * XBUF)[m] = w0;
      reinterpret_cast<u32*>(sm + 1 * XBUF)[m] = w1;
      reinterpret_cast<u32*>(sm + 2 * XBUF)[m] = w2;
      reinterpret_cast<u32*>(sm + 3 * XBUF)[m] = w3;
    }
  }
  float* kl = reinterpret_cast<float*>(buf) + 1024;
  for (int i = lane; i < 641; i += 64) kl[i] = Kc[(size_t)d * KC_PITCH + i];
  __syncthreads();
  u32 uw[16];
  cf zk[32], v[32];
#pragma unroll
  for (int n1 = 0; n1 < 16; ++n1) uw[n1] = reinterpret_cast<u32*>(buf)[64 * n1 + lane];
  static_for<0, 16>([&](auto ic) {
    constexpr int n1 = decltype(ic)::value;
    constexpr int s = (n1 < 2) ? 0 : (n1 < 4) ? 1 : (n1 < 8) ? 2 : 3;
    int l = 128 * n1 + 2 * lane;
    zk[n1] = cmk(kl[kpos(l, s)], kl[kpos(l + 1, s)]);
  });
  wave_sync();
  TwBase tb = make_twbase(lane, 11);
  fft32_halfzero<-1>(zk);
  apply_tw32<false>(zk, tb);
  fft2048_fwd_p2(zk, buf, lane);
#pragma unroll
  for (int n1 = 0; n1 < 16; ++n1) v[n1] = cmk(bf2f(uw[n1] & 0xffffu), bf2f(uw[n1] >> 16));
  fft32_halfzero<-1>(v);
  apply_tw32<false>(v, tb);
  fft2048_fwd_p2(v, buf, lane);
  {
    int k1 = lane & 31;
    bool hi = lane >= 32;
    float s, c;
    sincospif(-(float)k1 / 1024.0f, &s, &c);
    cf L = hi ? cmk(s, -c) : cmk(c, s);
    cf T[32];
    mirror2048(zk, T, buf, lane);
    const float sc = 1.0f / 2048.0f;
    static_for<0, 32>([&](auto ic) {
      constexpr int r = decltype(ic)::value;
      cf a = zk[r], b = T[r];
      cf ek = cmk(0.5f * (a.x + b.x), 0.5f * (a.y - b.y));
      cf ok = cmk(0.5f * (a.y + b.y), -0.5f * (a.x - b.x));
      cf w = tw64<(r & 15) + 32 * (r >> 4), -1>(L);
      cf wo = cmul(ok, w);
      T[r] = cmk(sc * (wo.x - ek.y), sc * (wo.y + ek.x));
      zk[r] = cmk(sc * a.x, sc * a.y);
    });
    cf m[32];
    mirror2048(v, m, buf, lane);
#pragma unroll
    for (int r = 0; r < 32; ++r) {
      cf a = v[r], b = m[r];
      cf eu = cmk(0.5f * (a.x + b.x), 0.5f * (a.y - b.y));
      cf ou = cmk(0.5f * (a.y + b.y), -0.5f * (a.x - b.x));
      v[r] = cadd(cmul(eu, zk[r]), cmul(ou, T[r]));
    }
  }
  fft2048_inv_p1(v, buf, lane);
  apply_tw32<true>(v, tb);
  fft_reg<32, 1>(v);
  float dsk = bf2f(Dskip[d]);
#pragma unroll
  for (int n1 = 0; n1 < 16; ++n1) {
    float u0 = bf2f(uw[n1] & 0xffffu), u1 = bf2f(uw[n1] >> 16);
    float o0 = bfround(v[n1].x) + bfround(u0 * dsk);
    float o1 = bfround(v[n1].y) + bfround(u1 * dsk);
    reinterpret_cast<u32*>(buf)[64 * n1 + lane] = f2bf(o0) | (f2bf(o1) << 16);
  }
  __syncthreads();
#pragma unroll
  for (int i = 0; i < 4; ++i) {
    int m = t + 256 * i;
    u32 w0 = reinterpret_cast<u32*>(sm + 0 * XBUF)[m];
    u32 w1 = reinterpret_cast<u32*>(sm + 1 * XBUF)[m];
    u32 w2 = reinterpret_cast<u32*>(sm + 2 * XBUF)[m];
    u32 w3 = reinterpret_cast<u32*>(sm + 3 * XBUF)[m];
    uint2 a, b;
    a.x = (w0 & 0xffffu) | (w1 << 16);
    a.y = (w2 & 0xffffu) | (w3 << 16);
    b.x = (w0 >> 16) | (w1 & 0xffff0000u);
    b.y = (w2 >> 16) | (w3 & 0xffff0000u);
    *reinterpret_cast<uint2*>(out + (size_t)(2 * m) * (2 * DCH) + d0) = a;
    *reinterpret_cast<uint2*>(out + (size_t)(2 * m + 1) * (2 * DCH) + d0) = b;
  }
}

extern "C" void kernel_launch(void* const* d_in, const int* in_sizes, int n_in, void* d_out, int out_size,
                              void* d_ws, size_t ws_size, hipStream_t stream) {
  const u16* u = (const u16*)d_in[0];
  const u16* Are = (const u16*)d_in[1];
  const u16* Aim = (const u16*)d_in[2];
  const u16* Bre = (const u16*)d_in[3];
  const u16* Bim = (const u16*)d_in[4];
  const u16* Cre = (const u16*)d_in[5];
  const u16* Cim = (const u16*)d_in[6];
  const u16* logdt = (const u16*)d_in[7];
  const u16* Dskip = (const u16*)d_in[8];
  const u16* x = (const u16*)d_in[9];
  const u16* wr = (const u16*)d_in[10];
  const u16* wi = (const u16*)d_in[11];
  u16* out = (u16*)d_out;
  char* ws = (char*)d_ws;
  cf* X1T = (cf*)ws;
  u32* WT = (u32*)(ws + (size_t)8 * 1024 * 1024);
  float* Kc = (float*)(ws + (size_t)16 * 1024 * 1024);

  k_kgen<<<DCH / 2, 128, 0, stream>>>(Are, Aim, Bre, Bim, Cre, Cim, logdt, Kc);
  k_wt<<<9 * 32, 256, 0, stream>>>(wr, wi, WT);
  k_brow<<<SEQ / 16, 256, 0, stream>>>(x, X1T);
  k_bcol<<<512 / 4, 256, 0, stream>>>(X1T, WT);
  k_browinv<<<SEQ / 16, 256, 0, stream>>>(X1T, out);
  k_aconv<<<DCH / 4, 256, 0, stream>>>(u, Kc, Dskip, out);
}
```

```cpp
#include <hip/hip_runtime.h>
#include <hip/hip_bf16.h>
#include <type_traits>

#define SEQ 2048
#define DCH 1024
#define NST 128
#define DF 513

typedef float2 cf;
typedef unsigned int u32;
typedef unsigned short u16;
using f32x16 = __attribute__((ext_vector_type(16))) float;

template <int I, int N, class F>
__device__ __forceinline__ void static_for(F&& f) {
  if constexpr (I < N) {
    f(std::integral_constant<int, I>{});
    static_for<I + 1, N>(f);
  }
}

__device__ __forceinline__ cf cmk(float x, float y) { cf r; r.x = x; r.y = y; return r; }
__device__ __forceinline__ cf cadd(cf a, cf b) { return cmk(a.x + b.x, a.y + b.y); }
__device__ __forceinline__ cf csub(cf a, cf b) { return cmk(a.x - b.x, a.y - b.y); }
__device__ __forceinline__ cf cmul(cf a, cf b) {
  return cmk(fmaf(a.x, b.x, -(a.y * b.y)), fmaf(a.x, b.y, a.y * b.x));
}
__device__ __forceinline__ cf cmulc(cf a, cf b) {
  return cmk(fmaf(a.x, b.x, a.y * b.y), fmaf(a.y, b.x, -(a.x * b.y)));
}
__device__ __forceinline__ cf csqr(cf a) { return cmk(fmaf(a.x, a.x, -(a.y * a.y)), 2.0f * a.x * a.y); }

__device__ __forceinline__ float bf2f(u32 h) { return __uint_as_float(h << 16); }
__device__ __forceinline__ u32 f2bf(float f) {
  __hip_bfloat16 b = __float2bfloat16(f);
  return (u32)(*reinterpret_cast<u16*>(&b));
}
__device__ __forceinline__ float bfround(float f) { return bf2f(f2bf(f)); }

__device__ __forceinline__ void wave_sync() {
  __builtin_amdgcn_fence(__ATOMIC_SEQ_CST, "wavefront");
  __builtin_amdgcn_wave_barrier();
  __builtin_amdgcn_fence(__ATOMIC_SEQ_CST, "wavefront");
}

constexpr double COS64_TAB[17] = {
    1.0, 0.99518472667219688624, 0.98078528040323044913, 0.95694033573220886494,
    0.92387953251128675613, 0.88192126434835502971, 0.83146961230254523708,
    0.77301045336273696081, 0.70710678118654752440, 0.63439328416364549822,
    0.55557023301960222474, 0.47139673682599764856, 0.38268343236508977173,
    0.29028467725446236764, 0.19509032201612826785, 0.09801714032956060199, 0.0};
constexpr double cos64(int j) {
  j = ((j % 64) + 64) % 64;
  if (j > 32) j = 64 - j;
  return (j <= 16) ? COS64_TAB[j] : -COS64_TAB[32 - j];
}
constexpr double sin64(int j) { return cos64(j - 16); }

template <int J, int DIR>
__device__ __forceinline__ cf tw64(cf a) {
  constexpr int j = ((J % 64) + 64) % 64;
  if constexpr (j == 0) {
    return a;
  } else if constexpr (j == 32) {
    return cmk(-a.x, -a.y);
  } else if constexpr (j == 16) {
    return (DIR > 0) ? cmk(-a.y, a.x) : cmk(a.y, -a.x);
  } else if constexpr (j == 48) {
    return (DIR > 0) ? cmk(a.y, -a.x) : cmk(-a.y, a.x);
  } else if constexpr (j % 16 == 8) {
    constexpr float h = 0.70710678118654752440f;
    constexpr float c = (float)cos64(j);
    constexpr float s = (float)(DIR * sin64(j));
    constexpr float cs = (c > 0 ? 1.f : -1.f), ss = (s > 0 ? 1.f : -1.f);
    float re = cs * a.x - ss * a.y;
    float im = ss * a.x + cs * a.y;
    return cmk(re * h, im * h);
  } else {
    constexpr float c = (float)cos64(j);
    constexpr float s = (float)(DIR * sin64(j));
    return cmk(fmaf(a.x, c, -(a.y * s)), fmaf(a.x, s, a.y * c));
  }
}

template <int N, int DIR, int S>
__device__ __forceinline__ void fft_rec(const cf* in, cf* out) {
  if constexpr (N == 1) {
    out[0] = in[0];
  } else if constexpr (N == 2) {
    cf a = in[0], b = in[S];
    out[0] = cadd(a, b);
    out[1] = csub(a, b);
  } else {
    constexpr int Q = N / 4;
    cf t0[Q], t1[Q], t2[Q], t3[Q];
    fft_rec<Q, DIR, 4 * S>(in, t0);
    fft_rec<Q, DIR, 4 * S>(in + S, t1);
    fft_rec<Q, DIR, 4 * S>(in + 2 * S, t2);
    fft_rec<Q, DIR, 4 * S>(in + 3 * S, t3);
    static_for<0, Q>([&](auto ic) {
      constexpr int k = decltype(ic)::value;
      constexpr int M = 64 / N;
      cf a = t0[k];
      cf b = tw64<k * M, DIR>(t1[k]);
      cf c = tw64<2 * k * M, DIR>(t2[k]);
      cf d = tw64<3 * k * M, DIR>(t3[k]);
      cf s0 = cadd(a, c), s1 = csub(a, c), s2 = cadd(b, d), s3 = csub(b, d);
      cf s3r = (DIR > 0) ? cmk(-s3.y, s3.x) : cmk(s3.y, -s3.x);
      out[k] = cadd(s0, s2);
      out[k + Q] = cadd(s1, s3r);
      out[k + 2 * Q] = csub(s0, s2);
      out[k + 3 * Q] = csub(s1, s3r);
    });
  }
}

template <int N, int DIR>
__device__ __forceinline__ void fft_reg(cf* v) {
  cf o[N];
  fft_rec<N, DIR, 1>(v, o);
#pragma unroll
  for (int i = 0; i < N; ++i) v[i] = o[i];
}

template <int DIR>
__device__ __forceinline__ void fft16_halfzero(cf* v) {
  cf a[8], b[8], oa[8], ob[8];
  static_for<0, 8>([&](auto ic) {
    constexpr int n = decltype(ic)::value;
    a[n] = v[n];
    b[n] = tw64<4 * n, DIR>(v[n]);
  });
  fft_rec<8, DIR, 1>(a, oa);
  fft_rec<8, DIR, 1>(b, ob);
#pragma unroll
  for (int k = 0; k < 8; ++k) {
    v[2 * k] = oa[k];
    v[2 * k + 1] = ob[k];
  }
}

struct Tw16 { cf w[4]; };

__device__ __forceinline__ Tw16 make_tw16(int n, int logN) {
  Tw16 b;
#pragma unroll
  for (int t = 0; t < 4; ++t) {
    int e = (n << t) & ((1 << logN) - 1);
    float s, c;
    sincospif(-2.0f * (float)e / (float)(1 << logN), &s, &c);
    b.w[t] = cmk(c, s);
  }
  return b;
}

template <bool CONJ>
__device__ __forceinline__ void apply_tw16(cf* v, const Tw16& b) {
  cf p[16];
  p[1] = b.w[0];
  p[2] = b.w[1];
  p[4] = b.w[2];
  p[8] = b.w[3];
  asm volatile("" : "+v"(p[1].x), "+v"(p[1].y), "+v"(p[2].x), "+v"(p[2].y), "+v"(p[4].x), "+v"(p[4].y),
               "+v"(p[8].x), "+v"(p[8].y));
  static_for<3, 16>([&](auto ic) {
    constexpr int k = decltype(ic)::value;
    constexpr int top = (k >= 8) ? 8 : (k >= 4) ? 4 : 2;
    if constexpr (k != top) p[k] = cmul(p[k - top], p[top]);
  });
#pragma unroll
  for (int k = 1; k < 16; ++k) v[k] = CONJ ? cmulc(v[k], p[k]) : cmul(v[k], p[k]);
}

__device__ __forceinline__ void swap16(cf& a, cf& b) {
  auto r0 = __builtin_amdgcn_permlane16_swap(__float_as_uint(a.x), __float_as_uint(b.x), false, false);
  auto r1 = __builtin_amdgcn_permlane16_swap(__float_as_uint(a.y), __float_as_uint(b.y), false, false);
  a.x = __uint_as_float(r0[0]);
  b.x = __uint_as_float(r0[1]);
  a.y = __uint_as_float(r1[0]);
  b.y = __uint_as_float(r1[1]);
}

#define XP 136
#define XBUF (16 * XP)
__device__ __forceinline__ int xoff(int g) { return 16 * g + 2 * (g >> 1); }

__device__ __forceinline__ void fft2048_fwd_rest(cf* v, cf* buf, int t, const Tw16& tw1, const Tw16& tw2) {
  apply_tw16<false>(v, tw1);
  __syncthreads();
#pragma unroll
  for (int k0 = 0; k0 < 16; ++k0) buf[k0 * XP + t] = v[k0];
  __syncthreads();
  int k0 = t >> 3, c = t & 7;
  cf* rb = buf + k0 * XP;
#pragma unroll
  for (int b = 0; b < 16; ++b) v[b] = rb[8 * b + c];
  wave_sync();
  fft_reg<16, -1>(v);
  apply_tw16<false>(v, tw2);
#pragma unroll
  for (int k1 = 0; k1 < 16; ++k1) rb[xoff(k1 >> 1) + (k1 & 1) * 8 + c] = v[k1];
  wave_sync();
  {
    const float4* p = reinterpret_cast<const float4*>(rb + xoff(c));
#pragma unroll
    for (int m = 0; m < 8; ++m) {
      float4 q = p[m];
      v[2 * m] = cmk(q.x, q.y);
      v[2 * m + 1] = cmk(q.z, q.w);
    }
  }
  fft_reg<8, -1>(v);
  fft_reg<8, -1>(v + 8);
}

__device__ __forceinline__ void fft2048_inv_first(cf* v, cf* buf, int t, const Tw16& tw1, const Tw16& tw2) {
  fft_reg<8, 1>(v);
  fft_reg<8, 1>(v + 8);
  int k0 = t >> 3, c = t & 7;
  cf* rb = buf + k0 * XP;
  __syncthreads();
  {
    float4* p = reinterpret_cast<float4*>(rb + xoff(c));
#pragma unroll
    for (int m = 0; m < 8; ++m) p[m] = make_float4(v[2 * m].x, v[2 * m].y, v[2 * m + 1].x, v[2 * m + 1].y);
  }
  wave_sync();
#pragma unroll
  for (int k1 = 0; k1 < 16; ++k1) v[k1] = rb[xoff(k1 >> 1) + (k1 & 1) * 8 + c];
  wave_sync();
  apply_tw16<true>(v, tw2);
  fft_reg<16, 1>(v);
#pragma unroll
  for (int b = 0; b < 16; ++b) rb[8 * b + c] = v[b];
  __syncthreads();
#pragma unroll
  for (int kk = 0; kk < 16; ++kk) v[kk] = buf[kk * XP + t];
  apply_tw16<true>(v, tw1);
}

__device__ __forceinline__ int mphys(int k) { return k + 2 * (k >> 5); }

__device__ __forceinline__ void mirror2048(const cf* v, cf* m, cf* buf, int t) {
  int kb = (t >> 3) + 32 * (t & 7);
  __syncthreads();
#pragma unroll
  for (int r = 0; r < 16; ++r) {
    int k = kb + 16 * (r >> 3) + 256 * (r & 7);
    buf[mphys(k)] = v[r];
  }
  __syncthreads();
#pragma unroll
  for (int r = 0; r < 16; ++r) {
    int k = kb + 16 * (r >> 3) + 256 * (r & 7);
    m[r] = buf[mphys((2048 - k) & 2047)];
  }
}

#define KC_PITCH 648
#define KREC 132
#define KCH 16

__device__ __forceinline__ cf cexp_scaled(float ar, float ai, float sc) {
  float e = expf(ar * sc);
  float s, c;
  sincosf(ai * sc, &s, &c);
  return cmk(e * c, e * s);
}

struct KParams { const u16 *Are, *Aim, *Bre, *Bim, *Cre, *Cim, *logdt; };

__device__ __forceinline__ void kgen_wave(const KParams& P, float* __restrict__ Kc, float* lds, int d, int lane) {
  float dt = bfround(expf(bf2f(P.logdt[d])));
  f32x16 acc;
#pragma unroll
  for (int i = 0; i < 16; ++i) acc[i] = 0.f;
  for (int round = 0; round < 2; ++round) {
    int n = round * 64 + lane;
    int idx = d * NST + n;
    float ar = dt * bf2f(P.Are[idx]), ai = dt * bf2f(P.Aim[idx]);
    cf dB = cmk(dt * bf2f(P.Bre[idx]), dt * bf2f(P.Bim[idx]));
    cf C = cmk(bf2f(P.Cre[idx]), bf2f(P.Cim[idx]));
    cf c0 = cmul(C, dB);
    cf e[12];
    e[0] = cexp_scaled(ar, ai, 1.0f);
    e[4] = cexp_scaled(ar, ai, 16.0f);
    e[8] = cexp_scaled(ar, ai, 256.0f);
    e[1] = csqr(e[0]); e[2] = csqr(e[1]); e[3] = csqr(e[2]);
    e[5] = csqr(e[4]); e[6] = csqr(e[5]); e[7] = csqr(e[6]);
    e[9] = csqr(e[8]); e[10] = csqr(e[9]); e[11] = csqr(e[10]);
    cf G[32], E[32];
    G[0] = c0;
    G[1] = cmul(c0, e[0]);
    G[2] = cmul(c0, e[1]);
    G[3] = cmul(G[1], e[1]);
    G[4] = cmul(c0, e[2]);
    G[5] = cmul(G[1], e[2]);
    G[6] = cmul(G[2], e[2]);
    G[7] = cmul(G[3], e[2]);
    G[8] = cmul(c0, e[8]);
    G[9] = cmul(G[2], e[8]);
    G[10] = cmul(G[4], e[8]);
    G[11] = cmul(G[6], e[8]);
    G[12] = cmul(c0, e[9]);
    G[13] = cmul(G[4], e[9]);
    G[14] = cmul(G[8], e[9]);
    G[15] = cmul(G[10], e[9]);
    G[16] = cmul(c0, e[10]);
    G[17] = cmul(G[8], e[10]);
    G[18] = cmul(G[12], e[10]);
    G[19] = cmul(G[14], e[10]);
    G[20] = cmul(c0, e[11]);
#pragma unroll
    for (int i = 21; i < 32; ++i) G[i] = cmk(0.f, 0.f);
    E[0] = cmk(1.f, 0.f);
    E[1] = e[3]; E[2] = e[4]; E[4] = e[5]; E[8] = e[6]; E[16] = e[7];
    static_for<3, 32>([&](auto ic) {
      constexpr int k = decltype(ic)::value;
      constexpr int top = (k >= 16) ? 16 : (k >= 8) ? 8 : (k >= 4) ? 4 : 2;
      if constexpr (k != top) E[k] = cmul(E[k - top], E[top]);
    });
    for (int ch = 0; ch < 64 / KCH; ++ch) {
      if ((lane / KCH) == ch) {
        float4* rec = reinterpret_cast<float4*>(lds + (lane % KCH) * KREC);
#pragma unroll
        for (int l2 = 0; l2 < 16; ++l2) {
          rec[l2] = make_float4(G[2 * l2].x, E[2 * l2].x, G[2 * l2 + 1].x, E[2 * l2 + 1].x);
          rec[16 + l2] = make_float4(-G[2 * l2].y, E[2 * l2].y, -G[2 * l2 + 1].y, E[2 * l2 + 1].y);
        }
      }
      wave_sync();
#pragma unroll
      for (int nn = 0; nn < KCH; ++nn) {
        float2 ab = *reinterpret_cast<const float2*>(lds + nn * KREC + 2 * lane);
        acc = __builtin_amdgcn_mfma_f32_32x32x2f32(ab.x, ab.y, acc, 0, 0, 0);
      }
      wave_sync();
    }
  }
  int col = lane & 31, hh = lane >> 5;
  float* dst = Kc + (size_t)d * KC_PITCH;
#pragma unroll
  for (int r = 0; r < 16; ++r) {
    int row = (r & 3) + 8 * (r >> 2) + 4 * hh;
    int p = -1;
    if (row < 8) p = row + 8 * col;
    else if (row < 12) p = 256 + (row - 8) + 4 * col;
    else if (row < 16) p = 384 + ((row - 12) & 1) + 64 * ((row - 12) >> 1) + 2 * col;
    else if (row < 20) p = 512 + 32 * (row - 16) + col;
    else if (row == 20 && col == 0) p = 640;
    if (p >= 0) dst[p] = acc[r];
  }
}

__device__ __forceinline__ void wt_block(const u16* __restrict__ wr, const u16* __restrict__ wi, u32* __restrict__ WT,
                                         u32* tile, int bid, int tid) {
  int bk = bid % 5, bs = bid / 5;
  int k0 = bk * 128, s0 = bs * 64;
  int tx = tid & 127, ty = tid >> 7;
  u32 val[16];
#pragma unroll
  for (int i = 0; i < 16; ++i) {
    int r = ty + 4 * i;
    int k = k0 + tx, s = s0 + r;
    val[i] = 0;
    if (k < DF) val[i] = (u32)wr[s * DF + k] | ((u32)wi[s * DF + k] << 16);
  }
#pragma unroll
  for (int i = 0; i < 16; ++i) tile[(ty + 4 * i) * 129 + tx] = val[i];
  __syncthreads();
  int sx = tid & 63, ky = tid >> 6;
#pragma unroll
  for (int i = 0; i < 16; ++i) {
    int kk = ky + 8 * i;
    int k = k0 + kk;
    if (k < DF) WT[(size_t)k * SEQ + s0 + sx] = tile[sx * 129 + kk];
  }
}

#define RP 34
#define RRS (16 * RP)
#define ZP 517
#define TWOFF (16 * RRS)

__device__ __forceinline__ void brow_block(const u16* __restrict__ x, cf* __restrict__ X1T, cf* sm, int bid, int tid) {
  cf* tw = sm + TWOFF;
  int s0 = bid * 16;
  if (tid <= 256) {
    float s, c;
    sincospif(-(float)tid / 512.0f, &s, &c);
    tw[tid] = cmk(c, s);
  }
  int row = tid >> 5, t = tid & 31;
  cf v[16];
  const u32* xr = reinterpret_cast<const u32*>(x + (size_t)(s0 + row) * DCH);
#pragma unroll
  for (int a = 0; a < 16; ++a) {
    u32 w = xr[32 * a + t];
    v[a] = cmk(bf2f(w & 0xffffu), bf2f(w >> 16));
  }
  Tw16 tb = make_tw16(t, 9);
  fft_reg<16, -1>(v);
  apply_tw16<false>(v, tb);
  cf* rb = sm + row * RRS;
  {
    int pos = (t & 1) * 16 + (t >> 1);
#pragma unroll
    for (int k0 = 0; k0 < 16; ++k0) rb[k0 * RP + pos] = v[k0];
  }
  wave_sync();
  int k0 = t & 15, h = t >> 4;
  {
    const float4* p = reinterpret_cast<const float4*>(rb + k0 * RP + h * 16);
#pragma unroll
    for (int m = 0; m < 8; ++m) {
      float4 q = p[m];
      v[2 * m] = cmk(q.x, q.y);
      v[2 * m + 1] = cmk(q.z, q.w);
    }
  }
  fft_reg<16, -1>(v);
  bool hi = h != 0;
  static_for<0, 8>([&](auto ic) {
    constexpr int j = decltype(ic)::value;
    cf a = v[j], b = v[j + 8];
    swap16(a, b);
    cf br = cmk(hi ? b.y : b.x, hi ? -b.x : b.y);
    br = tw64<2 * j, -1>(br);
    v[j] = cadd(a, br);
    v[j + 8] = csub(a, br);
  });
  __syncthreads();
  cf* zs = sm + row * ZP;
#pragma unroll
  for (int r = 0; r < 16; ++r) zs[k0 + 16 * ((r & 7) + 8 * h + 16 * (r >> 3))] = v[r];
  __syncthreads();
  int r = tid & 15, kk = tid >> 4;
  const cf* zr = sm + r * ZP;
#pragma unroll
  for (int i = 0; i < 9; ++i) {
    int k = kk + 32 * i;
    if (k > 256) break;
    cf a = zr[k];
    cf b = zr[(512 - k) & 511];
    if (k == 0) {
      X1T[(size_t)0 * SEQ + s0 + r] = cmk(a.x + a.y, a.x - a.y);
    } else {
      cf w = tw[k];
      cf ev = cmk(0.5f * (a.x + b.x), 0.5f * (a.y - b.y));
      cf od = cmk(0.5f * (a.y + b.y), -0.5f * (a.x - b.x));
      cf wo = cmul(od, w);
      X1T[(size_t)k * SEQ + s0 + r] = cadd(ev, wo);
      if (k != 256) X1T[(size_t)(512 - k) * SEQ + s0 + r] = cmk(ev.x - wo.x, -(ev.y - wo.y));
    }
  }
}

__device__ __forceinline__ void browinv_block(const cf* __restrict__ X1T, u16* __restrict__ out, cf* sm, int bid, int tid) {
  cf* tw = sm + TWOFF;
  int s0 = bid * 16;
  {
    int r = tid & 15, kk = tid >> 4;
    cf a[9], b[9];
#pragma unroll
    for (int i = 0; i < 9; ++i) {
      int k = kk + 32 * i;
      if (k <= 256) {
        a[i] = X1T[(size_t)k * SEQ + s0 + r];
        b[i] = X1T[(size_t)((512 - k) & 511) * SEQ + s0 + r];
      }
    }
    if (tid <= 256) {
      float s, c;
      sincospif(-(float)tid / 512.0f, &s, &c);
      tw[tid] = cmk(c, s);
    }
    __syncthreads();
    cf* zr = sm + r * ZP;
#pragma unroll
    for (int i = 0; i < 9; ++i) {
      int k = kk + 32 * i;
      if (k <= 256) {
        if (k == 0) {
          zr[0] = cmk(a[i].x + a[i].y, a[i].x - a[i].y);
        } else {
          cf w = tw[k];
          cf ev = cmk(a[i].x + b[i].x, a[i].y - b[i].y);
          cf df = cmk(a[i].x - b[i].x, a[i].y + b[i].y);
          cf od = cmulc(df, w);
          zr[k] = cmk(ev.x - od.y, ev.y + od.x);
          if (k != 256) zr[512 - k] = cmk(ev.x + od.y, -ev.y + od.x);
        }
      }
    }
  }
  __syncthreads();
  int row = tid >> 5, t = tid & 31;
  int k0 = t & 15, h = t >> 4;
  cf v[16];
  {
    const cf* zs = sm + row * ZP;
#pragma unroll
    for (int r = 0; r < 16; ++r) v[r] = zs[k0 + 16 * ((r & 7) + 8 * h + 16 * (r >> 3))];
  }
  __syncthreads();
  bool hi = h != 0;
  static_for<0, 8>([&](auto ic) {
    constexpr int j = decltype(ic)::value;
    cf a = cadd(v[j], v[j + 8]);
    cf d = csub(v[j], v[j + 8]);
    cf dr = cmk(hi ? -d.y : d.x, hi ? d.x : d.y);
    dr = tw64<2 * j, 1>(dr);
    swap16(a, dr);
    v[j] = a;
    v[j + 8] = dr;
  });
  fft_reg<16, 1>(v);
  cf* rb = sm + row * RRS;
  {
    float4* p = reinterpret_cast<float4*>(rb + k0 * RP + h * 16);
#pragma unroll
    for (int m = 0; m < 8; ++m) p[m] = make_float4(v[2 * m].x, v[2 * m].y, v[2 * m + 1].x, v[2 * m + 1].y);
  }
  wave_sync();
  {
    int pos = (t & 1) * 16 + (t >> 1);
#pragma unroll
    for (int kk = 0; kk < 16; ++kk) v[kk] = rb[kk * RP + pos];
  }
  Tw16 tb = make_tw16(t, 9);
  apply_tw16<true>(v, tb);
  fft_reg<16, 1>(v);
  u32* orow = reinterpret_cast<u32*>(out + (size_t)(s0 + row) * (2 * DCH) + DCH);
#pragma unroll
  for (int a = 0; a < 16; ++a) orow[32 * a + t] = f2bf(v[a].x) | (f2bf(v[a].y) << 16);
}

__device__ __forceinline__ cf ldw(const u32* p, int i) {
  u32 w = p[i];
  return cmk(bf2f(w & 0xffffu), bf2f(w >> 16));
}

__device__ __forceinline__ void bcol_block(cf* __restrict__ X1T, const u32* __restrict__ WT, cf* sm, int bid, int tid) {
  int grp = tid >> 7, t = tid & 127;
  int col = bid * 4 + grp;
  cf* buf = sm + grp * XBUF;
  cf v[16];
  cf* xc = X1T + (size_t)col * SEQ;
#pragma unroll
  for (int a = 0; a < 16; ++a) v[a] = xc[128 * a + t];
  int kb = (t >> 3) + 32 * (t & 7);
  u32 wq[16];
  {
    const u32* wc = WT + (size_t)col * SEQ;
#pragma unroll
    for (int r = 0; r < 16; ++r) wq[r] = wc[kb + 16 * (r >> 3) + 256 * (r & 7)];
  }
  Tw16 tw1 = make_tw16(t, 11);
  Tw16 tw2 = make_tw16(t & 7, 7);
  fft_reg<16, -1>(v);
  fft2048_fwd_rest(v, buf, t, tw1, tw2);
  const float sc = 1.0f / (2048.0f * 1024.0f);
  if (bid != 0) {
#pragma unroll
    for (int r = 0; r < 16; ++r) {
      cf w = cmk(bf2f(wq[r] & 0xffffu) * sc, bf2f(wq[r] >> 16) * sc);
      v[r] = cmul(v[r], w);
    }
  } else {
    cf m[16];
    mirror2048(v, m, buf, t);
    if (col == 0) {
      const u32* w0 = WT;
      const u32* w1 = WT + (size_t)512 * SEQ;
#pragma unroll
      for (int r = 0; r < 16; ++r) {
        int k = kb + 16 * (r >> 3) + 256 * (r & 7);
        int km = (2048 - k) & 2047;
        cf a = v[r], b = m[r];
        cf A = cmk(0.5f * (a.x + b.x), 0.5f * (a.y - b.y));
        cf B = cmk(0.5f * (a.y + b.y), -0.5f * (a.x - b.x));
        cf p = ldw(w0, k), q = ldw(w0, km);
        cf wh0 = cmk(0.5f * sc * (p.x + q.x), 0.5f * sc * (p.y - q.y));
        p = ldw(w1, k); q = ldw(w1, km);
        cf wh1 = cmk(0.5f * sc * (p.x + q.x), 0.5f * sc * (p.y - q.y));
        cf h0 = cmul(A, wh0), h1 = cmul(B, wh1);
        v[r] = cmk(h0.x - h1.y, h0.y + h1.x);
      }
    } else {
#pragma unroll
      for (int r = 0; r < 16; ++r) {
        cf w = cmk(bf2f(wq[r] & 0xffffu) * sc, bf2f(wq[r] >> 16) * sc);
        v[r] = cmul(v[r], w);
      }
    }
  }
  fft2048_inv_first(v, buf, t, tw1, tw2);
  fft_reg<16, 1>(v);
#pragma unroll
  for (int a = 0; a < 16; ++a) xc[128 * a + t] = v[a];
}

__device__ __forceinline__ int kpos(int l, int s) {
  if (s == 0) return l;
  int half = 1 << (s - 1);
  int q = (l + half - 1 + ((l >> s) & 1)) >> s;
  return 128 * (s + 1) + q - 128;
}

__device__ __forceinline__ void aconv_block(const u16* __restrict__ u, const float* __restrict__ Kc,
                                            const u16* __restrict__ Dskip, u16* __restrict__ out, cf* sm,
                                            int grp4, int tid) {
  int grp = tid >> 7, t = tid & 127;
  int d0 = grp4 * 4;
  int d = d0 + grp;
  cf* buf = sm + grp * XBUF;
  {
#pragma unroll
    for (int i = 0; i < 2; ++i) {
      int m = tid + 512 * i;
      uint2 a = *reinterpret_cast<const uint2*>(u + (size_t)(2 * m) * DCH + d0);
      uint2 b = *reinterpret_cast<const uint2*>(u + (size_t)(2 * m + 1) * DCH + d0);
      u32 w0 = (a.x & 0xffffu) | (b.x << 16);
      u32 w1 = (a.x >> 16) | (b.x & 0xffff0000u);
      u32 w2 = (a.y & 0xffffu) | (b.y << 16);
      u32 w3 = (a.y >> 16) | (b.y & 0xffff0000u);
      reinterpret_cast<u32*>(sm + 0 * XBUF)[m] = w0;
      reinterpret_cast<u32*>(sm + 1 * XBUF)[m] = w1;
      reinterpret_cast<u32*>(sm + 2 * XBUF)[m] = w2;
      reinterpret_cast<u32*>(sm + 3 * XBUF)[m] = w3;
    }
  }
  float* kl = reinterpret_cast<float*>(buf) + 1024;
#pragma unroll
  for (int i = 0; i < 6; ++i) {
    int p = t + 128 * i;
    if (p < 641) kl[p] = Kc[(size_t)d * KC_PITCH + p];
  }
  Tw16 tw1 = make_tw16(t, 11);
  Tw16 tw2 = make_tw16(t & 7, 7);
  __syncthreads();
  u32 uw[8];
  cf zk[16], v[16];
#pragma unroll
  for (int a = 0; a < 8; ++a) uw[a] = reinterpret_cast<u32*>(buf)[128 * a + t];
  static_for<0, 8>([&](auto ic) {
    constexpr int a = decltype(ic)::value;
    constexpr int s = (a < 1) ? 0 : (a < 2) ? 1 : (a < 4) ? 2 : 3;
    int l = 256 * a + 2 * t;
    zk[a] = cmk(kl[kpos(l, s)], kl[kpos(l + 1, s)]);
  });
  fft16_halfzero<-1>(zk);
  fft2048_fwd_rest(zk, buf, t, tw1, tw2);
#pragma unroll
  for (int a = 0; a < 8; ++a) v[a] = cmk(bf2f(uw[a] & 0xffffu), bf2f(uw[a] >> 16));
  fft16_halfzero<-1>(v);
  fft2048_fwd_rest(v, buf, t, tw1, tw2);
  {
    int kb = (t >> 3) + 32 * (t & 7);
    float s, c;
    sincospif(-(float)kb / 1024.0f, &s, &c);
    cf L0 = cmk(c, s);
    cf L1 = cmul(L0, cmk(0.99879545620517239271f, -0.04906767432741801425f));
    cf T[16];
    mirror2048(zk, T, buf, t);
    const float sc = 1.0f / 2048.0f;
    static_for<0, 16>([&](auto ic) {
      constexpr int r = decltype(ic)::value;
      cf a = zk[r], b = T[r];
      cf ek = cmk(0.5f * (a.x + b.x), 0.5f * (a.y - b.y));
      cf ok = cmk(0.5f * (a.y + b.y), -0.5f * (a.x - b.x));
      cf w = tw64<8 * (r & 7), -1>((r >> 3) ? L1 : L0);
      cf wo = cmul(ok, w);
      T[r] = cmk(sc * (wo.x - ek.y), sc * (wo.y + ek.x));
      zk[r] = cmk(sc * a.x, sc * a.y);
    });
    cf m[16];
    mirror2048(v, m, buf, t);
#pragma unroll
    for (int r = 0; r < 16; ++r) {
      cf a = v[r], b = m[r];
      cf eu = cmk(0.5f * (a.x + b.x), 0.5f * (a.y - b.y));
      cf ou = cmk(0.5f * (a.y + b.y), -0.5f * (a.x - b.x));
      v[r] = cadd(cmul(eu, zk[r]), cmul(ou, T[r]));
    }
  }
  fft2048_inv_first(v, buf, t, tw1, tw2);
  fft_reg<16, 1>(v);
  float dsk = bf2f(Dskip[d]);
  __syncthreads();
#pragma unroll
  for (int a = 0; a < 8; ++a) {
    float u0 = bf2f(uw[a] & 0xffffu), u1 = bf2f(uw[a] >> 16);
    float o0 = bfround(v[a].x) + bfround(u0 * dsk);
    float o1 = bfround(v[a].y) + bfround(u1 * dsk);
    reinterpret_cast<u32*>(buf)[128 * a + t] = f2bf(o0) | (f2bf(o1) << 16);
  }
  __syncthreads();
#pragma unroll
  for (int i = 0; i < 2; ++i) {
    int m = tid + 512 * i;
    u32 w0 = reinterpret_cast<u32*>(sm + 0 * XBUF)[m];
    u32 w1 = reinterpret_cast<u32*>(sm + 1 * XBUF)[m];
    u32 w2 = reinterpret_cast<u32*>(sm + 2 * XBUF)[m];
    u32 w3 = reinterpret_cast<u32*>(sm + 3 * XBUF)[m];
    uint2 a, b;
    a.x = (w0 & 0xffffu) | (w1 << 16);
    a.y = (w2 & 0xffffu) | (w3 << 16);
    b.x = (w0 >> 16) | (w1 & 0xffff0000u);
    b.y = (w2 >> 16) | (w3 & 0xffff0000u);
    *reinterpret_cast<uint2*>(out + (size_t)(2 * m) * (2 * DCH) + d0) = a;
    *reinterpret_cast<uint2*>(out + (size_t)(2 * m + 1) * (2 * DCH) + d0) = b;
  }
}

#define SM_CF (TWOFF + 260)

__global__ __launch_bounds__(512, 2) void k1(KParams P, float* Kc, const u16* x, cf* X1T, const u16* wr,
                                            const u16* wi, u32* WT) {
  __shared__ __attribute__((aligned(16))) cf sm[SM_CF];
  int b = blockIdx.x, tid = threadIdx.x;
  if (b < 256) {
    if (b & 1) {
      int wave = tid >> 6, lane = tid & 63;
      kgen_wave(P, Kc, reinterpret_cast<float*>(sm) + wave * KCH * KREC, (b >> 1) * 8 + wave, lane);
    } else {
      brow_block(x, X1T, sm, b >> 1, tid);
    }
  } else {
    wt_block(wr, wi, WT, reinterpret_cast<u32*>(sm), b - 256, tid);
  }
}

#define ACONV_K2 128

__global__ __launch_bounds__(512, 2) void k2(cf* X1T, const u32* WT, const u16* u, const float* Kc, const u16* Dskip,
                                            u16* out) {
  __shared__ __attribute__((aligned(16))) cf sm[SM_CF];
  int b = blockIdx.x, tid = threadIdx.x;
  if (b < 128) {
    bcol_block(X1T, WT, sm, b, tid);
  } else {
    int i = b - 128;
    int g = (i & 7) * 32 + (i >> 3);
    aconv_block(u, Kc, Dskip, out, sm, g, tid);
  }
}

__global__ __launch_bounds__(512, 2) void k3(const cf* X1T, const u16* u, const float* Kc, const u16* Dskip, u16* out) {
  __shared__ __attribute__((aligned(16))) cf sm[SM_CF];
  int b = blockIdx.x, tid = threadIdx.x;
  if (b < 128) {
    browinv_block(X1T, out, sm, b, tid);
  } else {
    int i = b - 128 + ACONV_K2;
    int g = (i & 7) * 32 + (i >> 3);
    aconv_block(u, Kc, Dskip, out, sm, g, tid);
  }
}

extern "C" void kernel_launch(void* const* d_in, const int* in_sizes, int n_in, void* d_out, int out_size,
                              void* d_ws, size_t ws_size, hipStream_t stream) {
  const u16* u = (const u16*)d_in[0];
  KParams P;
  P.Are = (const u16*)d_in[1];
  P.Aim = (const u16*)d_in[2];
  P.Bre = (const u16*)d_in[3];
  P.Bim = (const u16*)d_in[4];
  P.Cre = (const u16*)d_in[5];
  P.Cim = (const u16*)d_in[6];
  P.logdt = (const u16*)d_in[7];
  const u16* Dskip = (const u16*)d_in[8];
  const u16* x = (const u16*)d_in[9];
  const u16* wr = (const u16*)d_in[10];
  const u16* wi = (const u16*)d_in[11];
  u16* out = (u16*)d_out;
  char* ws = (char*)d_ws;
  cf* X1T = (cf*)ws;
  u32* WT = (u32*)(ws + (size_t)8 * 1024 * 1024);
  float* Kc = (float*)(ws + (size_t)16 * 1024 * 1024);

  k1<<<256 + 5 * 32, 512, 0, stream>>>(P, Kc, x, X1T, wr, wi, WT);
  k2<<<128 + ACONV_K2, 512, 0, stream>>>(X1T, WT, u, Kc, Dskip, out);
  k3<<<128 + (256 - ACONV_K2), 512, 0, stream>>>(X1T, u, Kc, Dskip, out);
}
```

```cpp
#include <hip/hip_runtime.h>
#include <hip/hip_bf16.h>
#include <type_traits>

#define SEQ 2048
#define DCH 1024
#define NST 128
#define DF 513

typedef float2 cf;
typedef unsigned int u32;
typedef unsigned short u16;
using f32x16 = __attribute__((ext_vector_type(16))) float;

template <int I, int N, class F>
__device__ __forceinline__ void static_for(F&& f) {
  if constexpr (I < N) {
    f(std::integral_constant<int, I>{});
    static_for<I + 1, N>(f);
  }
}

__device__ __forceinline__ cf cmk(float x, float y) { cf r; r.x = x; r.y = y; return r; }
__device__ __forceinline__ cf cadd(cf a, cf b) { return cmk(a.x + b.x, a.y + b.y); }
__device__ __forceinline__ cf csub(cf a, cf b) { return cmk(a.x - b.x, a.y - b.y); }
__device__ __forceinline__ cf cmul(cf a, cf b) {
  return cmk(fmaf(a.x, b.x, -(a.y * b.y)), fmaf(a.x, b.y, a.y * b.x));
}
__device__ __forceinline__ cf cmulc(cf a, cf b) {
  return cmk(fmaf(a.x, b.x, a.y * b.y), fmaf(a.y, b.x, -(a.x * b.y)));
}
__device__ __forceinline__ cf csqr(cf a) { return cmk(fmaf(a.x, a.x, -(a.y * a.y)), 2.0f * a.x * a.y); }

__device__ __forceinline__ float bf2f(u32 h) { return __uint_as_float(h << 16); }
__device__ __forceinline__ u32 f2bf(float f) {
  __hip_bfloat16 b = __float2bfloat16(f);
  return (u32)(*reinterpret_cast<u16*>(&b));
}
__device__ __forceinline__ float bfround(float f) { return bf2f(f2bf(f)); }

__device__ __forceinline__ void wave_sync() {
  __builtin_amdgcn_fence(__ATOMIC_SEQ_CST, "wavefront");
  __builtin_amdgcn_wave_barrier();
  __builtin_amdgcn_fence(__ATOMIC_SEQ_CST, "wavefront");
}

constexpr double COS64_TAB[17] = {
    1.0, 0.99518472667219688624, 0.98078528040323044913, 0.95694033573220886494,
    0.92387953251128675613, 0.88192126434835502971, 0.83146961230254523708,
    0.77301045336273696081, 0.70710678118654752440, 0.63439328416364549822,
    0.55557023301960222474, 0.47139673682599764856, 0.38268343236508977173,
    0.29028467725446236764, 0.19509032201612826785, 0.09801714032956060199, 0.0};
constexpr double cos64(int j) {
  j = ((j % 64) + 64) % 64;
  if (j > 32) j = 64 - j;
  return (j <= 16) ? COS64_TAB[j] : -COS64_TAB[32 - j];
}
constexpr double sin64(int j) { return cos64(j - 16); }

template <int J, int DIR>
__device__ __forceinline__ cf tw64(cf a) {
  constexpr int j = ((J % 64) + 64) % 64;
  if constexpr (j == 0) {
    return a;
  } else if constexpr (j == 32) {
    return cmk(-a.x, -a.y);
  } else if constexpr (j == 16) {
    return (DIR > 0) ? cmk(-a.y, a.x) : cmk(a.y, -a.x);
  } else if constexpr (j == 48) {
    return (DIR > 0) ? cmk(a.y, -a.x) : cmk(-a.y, a.x);
  } else if constexpr (j % 16 == 8) {
    constexpr float h = 0.70710678118654752440f;
    constexpr float c = (float)cos64(j);
    constexpr float s = (float)(DIR * sin64(j));
    constexpr float cs = (c > 0 ? 1.f : -1.f), ss = (s > 0 ? 1.f : -1.f);
    float re = cs * a.x - ss * a.y;
    float im = ss * a.x + cs * a.y;
    return cmk(re * h, im * h);
  } else {
    constexpr float c = (float)cos64(j);
    constexpr float s = (float)(DIR * sin64(j));
    return cmk(fmaf(a.x, c, -(a.y * s)), fmaf(a.x, s, a.y * c));
  }
}

template <int N, int DIR, int S>
__device__ __forceinline__ void fft_rec(const cf* in, cf* out) {
  if constexpr (N == 1) {
    out[0] = in[0];
  } else if constexpr (N == 2) {
    cf a = in[0], b = in[S];
    out[0] = cadd(a, b);
    out[1] = csub(a, b);
  } else {
    constexpr int Q = N / 4;
    cf t0[Q], t1[Q], t2[Q], t3[Q];
    fft_rec<Q, DIR, 4 * S>(in, t0);
    fft_rec<Q, DIR, 4 * S>(in + S, t1);
    fft_rec<Q, DIR, 4 * S>(in + 2 * S, t2);
    fft_rec<Q, DIR, 4 * S>(in + 3 * S, t3);
    static_for<0, Q>([&](auto ic) {
      constexpr int k = decltype(ic)::value;
      constexpr int M = 64 / N;
      cf a = t0[k];
      cf b = tw64<k * M, DIR>(t1[k]);
      cf c = tw64<2 * k * M, DIR>(t2[k]);
      cf d = tw64<3 * k * M, DIR>(t3[k]);
      cf s0 = cadd(a, c), s1 = csub(a, c), s2 = cadd(b, d), s3 = csub(b, d);
      cf s3r = (DIR > 0) ? cmk(-s3.y, s3.x) : cmk(s3.y, -s3.x);
      out[k] = cadd(s0, s2);
      out[k + Q] = cadd(s1, s3r);
      out[k + 2 * Q] = csub(s0, s2);
      out[k + 3 * Q] = csub(s1, s3r);
    });
  }
}

template <int N, int DIR>
__device__ __forceinline__ void fft_reg(cf* v) {
  cf o[N];
  fft_rec<N, DIR, 1>(v, o);
#pragma unroll
  for (int i = 0; i < N; ++i) v[i] = o[i];
}

template <int DIR>
__device__ __forceinline__ void fft16_halfzero(cf* v) {
  cf a[8], b[8], oa[8], ob[8];
  static_for<0, 8>([&](auto ic) {
    constexpr int n = decltype(ic)::value;
    a[n] = v[n];
    b[n] = tw64<4 * n, DIR>(v[n]);
  });
  fft_rec<8, DIR, 1>(a, oa);
  fft_rec<8, DIR, 1>(b, ob);
#pragma unroll
  for (int k = 0; k < 8; ++k) {
    v[2 * k] = oa[k];
    v[2 * k + 1] = ob[k];
  }
}

struct Tw16 { cf w[4]; };

__device__ __forceinline__ Tw16 make_tw16(int n, int logN) {
  Tw16 b;
#pragma unroll
  for (int t = 0; t < 4; ++t) {
    int e = (n << t) & ((1 << logN) - 1);
    float s, c;
    sincospif(-2.0f * (float)e / (float)(1 << logN), &s, &c);
    b.w[t] = cmk(c, s);
  }
  return b;
}

__device__ __forceinline__ Tw16 load_tw16(const cf* __restrict__ TW, int n) {
  Tw16 b;
#pragma unroll
  for (int t = 0; t < 4; ++t) b.w[t] = TW[(n << t) & 2047];
  return b;
}

template <bool CONJ>
__device__ __forceinline__ void apply_tw16(cf* v, const Tw16& b) {
  cf p[16];
  p[1] = b.w[0];
  p[2] = b.w[1];
  p[4] = b.w[2];
  p[8] = b.w[3];
  asm volatile("" : "+v"(p[1].x), "+v"(p[1].y), "+v"(p[2].x), "+v"(p[2].y), "+v"(p[4].x), "+v"(p[4].y),
               "+v"(p[8].x), "+v"(p[8].y));
  static_for<3, 16>([&](auto ic) {
    constexpr int k = decltype(ic)::value;
    constexpr int top = (k >= 8) ? 8 : (k >= 4) ? 4 : 2;
    if constexpr (k != top) p[k] = cmul(p[k - top], p[top]);
  });
#pragma unroll
  for (int k = 1; k < 16; ++k) v[k] = CONJ ? cmulc(v[k], p[k]) : cmul(v[k], p[k]);
}

__device__ __forceinline__ void swap16(cf& a, cf& b) {
  auto r0 = __builtin_amdgcn_permlane16_swap(__float_as_uint(a.x), __float_as_uint(b.x), false, false);
  auto r1 = __builtin_amdgcn_permlane16_swap(__float_as_uint(a.y), __float_as_uint(b.y), false, false);
  a.x = __uint_as_float(r0[0]);
  b.x = __uint_as_float(r0[1]);
  a.y = __uint_as_float(r1[0]);
  b.y = __uint_as_float(r1[1]);
}

#define XP 136
#define XBUF (16 * XP)
__device__ __forceinline__ int xoff(int g) { return 16 * g + 2 * (g >> 1); }

__device__ __forceinline__ void fft2048_fwd_rest(cf* v, cf* buf, int t, const cf* __restrict__ TW) {
  apply_tw16<false>(v, load_tw16(TW, t));
  __syncthreads();
#pragma unroll
  for (int k0 = 0; k0 < 16; ++k0) buf[k0 * XP + t] = v[k0];
  __syncthreads();
  int k0 = t >> 3, c = t & 7;
  cf* rb = buf + k0 * XP;
#pragma unroll
  for (int b = 0; b < 16; ++b) v[b] = rb[8 * b + c];
  wave_sync();
  fft_reg<16, -1>(v);
  apply_tw16<false>(v, load_tw16(TW, 16 * c));
#pragma unroll
  for (int k1 = 0; k1 < 16; ++k1) rb[xoff(k1 >> 1) + (k1 & 1) * 8 + c] = v[k1];
  wave_sync();
  {
    const float4* p = reinterpret_cast<const float4*>(rb + xoff(c));
#pragma unroll
    for (int m = 0; m < 8; ++m) {
      float4 q = p[m];
      v[2 * m] = cmk(q.x, q.y);
      v[2 * m + 1] = cmk(q.z, q.w);
    }
  }
  fft_reg<8, -1>(v);
  fft_reg<8, -1>(v + 8);
}

__device__ __forceinline__ void fft2048_inv_first(cf* v, cf* buf, int t, const cf* __restrict__ TW) {
  asm volatile("" : "+v"(t));
  fft_reg<8, 1>(v);
  fft_reg<8, 1>(v + 8);
  int k0 = t >> 3, c = t & 7;
  cf* rb = buf + k0 * XP;
  __syncthreads();
  {
    float4* p = reinterpret_cast<float4*>(rb + xoff(c));
#pragma unroll
    for (int m = 0; m < 8; ++m) p[m] = make_float4(v[2 * m].x, v[2 * m].y, v[2 * m + 1].x, v[2 * m + 1].y);
  }
  wave_sync();
#pragma unroll
  for (int k1 = 0; k1 < 16; ++k1) v[k1] = rb[xoff(k1 >> 1) + (k1 & 1) * 8 + c];
  wave_sync();
  apply_tw16<true>(v, load_tw16(TW, 16 * c));
  fft_reg<16, 1>(v);
#pragma unroll
  for (int b = 0; b < 16; ++b) rb[8 * b + c] = v[b];
  __syncthreads();
#pragma unroll
  for (int kk = 0; kk < 16; ++kk) v[kk] = buf[kk * XP + t];
  apply_tw16<true>(v, load_tw16(TW, t));
}

__device__ __forceinline__ int mphys(int k) { return k + 2 * (k >> 5); }

__device__ __forceinline__ void mirror2048(const cf* v, cf* m, cf* buf, int t) {
  int kb = (t >> 3) + 32 * (t & 7);
  __syncthreads();
#pragma unroll
  for (int r = 0; r < 16; ++r) {
    int k = kb + 16 * (r >> 3) + 256 * (r & 7);
    buf[mphys(k)] = v[r];
  }
  __syncthreads();
#pragma unroll
  for (int r = 0; r < 16; ++r) {
    int k = kb + 16 * (r >> 3) + 256 * (r & 7);
    m[r] = buf[mphys((2048 - k) & 2047)];
  }
}

#define KC_PITCH 648
#define KREC 132
#define KCH 32

__device__ __forceinline__ cf cexp_scaled(float ar, float ai, float sc) {
  float e = expf(ar * sc);
  float s, c;
  sincosf(ai * sc, &s, &c);
  return cmk(e * c, e * s);
}

struct KParams { const u16 *Are, *Aim, *Bre, *Bim, *Cre, *Cim, *logdt; };

__device__ __forceinline__ void kgen_wave(const KParams& P, float* __restrict__ Kc, float* lds, int d, int lane) {
  float dt = bfround(expf(bf2f(P.logdt[d])));
  f32x16 acc;
#pragma unroll
  for (int i = 0; i < 16; ++i) acc[i] = 0.f;
  for (int round = 0; round < 2; ++round) {
    int n = round * 64 + lane;
    int idx = d * NST + n;
    float ar = dt * bf2f(P.Are[idx]), ai = dt * bf2f(P.Aim[idx]);
    cf dB = cmk(dt * bf2f(P.Bre[idx]), dt * bf2f(P.Bim[idx]));
    cf C = cmk(bf2f(P.Cre[idx]), bf2f(P.Cim[idx]));
    cf c0 = cmul(C, dB);
    cf e[12];
    e[0] = cexp_scaled(ar, ai, 1.0f);
    e[4] = cexp_scaled(ar, ai, 16.0f);
    e[8] = cexp_scaled(ar, ai, 256.0f);
    e[1] = csqr(e[0]); e[2] = csqr(e[1]); e[3] = csqr(e[2]);
    e[5] = csqr(e[4]); e[6] = csqr(e[5]); e[7] = csqr(e[6]);
    e[9] = csqr(e[8]); e[10] = csqr(e[9]); e[11] = csqr(e[10]);
    cf G[32], E[32];
    G[0] = c0;
    G[1] = cmul(c0, e[0]);
    G[2] = cmul(c0, e[1]);
    G[3] = cmul(G[1], e[1]);
    G[4] = cmul(c0, e[2]);
    G[5] = cmul(G[1], e[2]);
    G[6] = cmul(G[2], e[2]);
    G[7] = cmul(G[3], e[2]);
    G[8] = cmul(c0, e[8]);
    G[9] = cmul(G[2], e[8]);
    G[10] = cmul(G[4], e[8]);
    G[11] = cmul(G[6], e[8]);
    G[12] = cmul(c0, e[9]);
    G[13] = cmul(G[4], e[9]);
    G[14] = cmul(G[8], e[9]);
    G[15] = cmul(G[10], e[9]);
    G[16] = cmul(c0, e[10]);
    G[17] = cmul(G[8], e[10]);
    G[18] = cmul(G[12], e[10]);
    G[19] = cmul(G[14], e[10]);
    G[20] = cmul(c0, e[11]);
#pragma unroll
    for (int i = 21; i < 32; ++i) G[i] = cmk(0.f, 0.f);
    E[0] = cmk(1.f, 0.f);
    E[1] = e[3]; E[2] = e[4]; E[4] = e[5]; E[8] = e[6]; E[16] = e[7];
    static_for<3, 32>([&](auto ic) {
      constexpr int k = decltype(ic)::value;
      constexpr int top = (k >= 16) ? 16 : (k >= 8) ? 8 : (k >= 4) ? 4 : 2;
      if constexpr (k != top) E[k] = cmul(E[k - top], E[top]);
    });
    for (int ch = 0; ch < 64 / KCH; ++ch) {
      if ((lane / KCH) == ch) {
        float4* rec = reinterpret_cast<float4*>(lds + (lane % KCH) * KREC);
#pragma unroll
        for (int l2 = 0; l2 < 16; ++l2) {
          rec[l2] = make_float4(G[2 * l2].x, E[2 * l2].x, G[2 * l2 + 1].x, E[2 * l2 + 1].x);
          rec[16 + l2] = make_float4(-G[2 * l2].y, E[2 * l2].y, -G[2 * l2 + 1].y, E[2 * l2 + 1].y);
        }
      }
      wave_sync();
#pragma unroll
      for (int nn = 0; nn < KCH; ++nn) {
        float2 ab = *reinterpret_cast<const float2*>(lds + nn * KREC + 2 * lane);
        acc = __builtin_amdgcn_mfma_f32_32x32x2f32(ab.x, ab.y, acc, 0, 0, 0);
      }
      wave_sync();
    }
  }
  int col = lane & 31, hh = lane >> 5;
  float* dst = Kc + (size_t)d * KC_PITCH;
#pragma unroll
  for (int r = 0; r < 16; ++r) {
    int row = (r & 3) + 8 * (r >> 2) + 4 * hh;
    int p = -1;
    if (row < 8) p = row + 8 * col;
    else if (row < 12) p = 256 + (row - 8) + 4 * col;
    else if (row < 16) p = 384 + ((row - 12) & 1) + 64 * ((row - 12) >> 1) + 2 * col;
    else if (row < 20) p = 512 + 32 * (row - 16) + col;
    else if (row == 20 && col == 0) p = 640;
    if (p >= 0) dst[p] = acc[r];
  }
}

__device__ __forceinline__ void wt_block(const u16* __restrict__ wr, const u16* __restrict__ wi, u32* __restrict__ WT,
                                         u32* tile, int bid, int tid) {
  int bk = bid % 5, bs = bid / 5;
  int k0 = bk * 128, s0 = bs * 64;
  int tx = tid & 127, ty = tid >> 7;
  u32 val[16];
#pragma unroll
  for (int i = 0; i < 16; ++i) {
    int r = ty + 4 * i;
    int k = k0 + tx, s = s0 + r;
    val[i] = 0;
    if (k < DF) val[i] = (u32)wr[s * DF + k] | ((u32)wi[s * DF + k] << 16);
  }
#pragma unroll
  for (int i = 0; i < 16; ++i) tile[(ty + 4 * i) * 129 + tx] = val[i];
  __syncthreads();
  int sx = tid & 63, ky = tid >> 6;
#pragma unroll
  for (int i = 0; i < 16; ++i) {
    int kk = ky + 8 * i;
    int k = k0 + kk;
    if (k < DF) WT[(size_t)k * SEQ + s0 + sx] = tile[sx * 129 + kk];
  }
}

#define RP 34
#define RRS (16 * RP)
#define ZP 517
#define TWOFF (16 * RRS)

__device__ __forceinline__ void brow_block(const u16* __restrict__ x, cf* __restrict__ X1T, cf* sm, int bid, int tid) {
  cf* tw = sm + TWOFF;
  int s0 = bid * 16;
  if (tid <= 256) {
    float s, c;
    sincospif(-(float)tid / 512.0f, &s, &c);
    tw[tid] = cmk(c, s);
  }
  int row = tid >> 5, t = tid & 31;
  cf v[16];
  const u32* xr = reinterpret_cast<const u32*>(x + (size_t)(s0 + row) * DCH);
#pragma unroll
  for (int a = 0; a < 16; ++a) {
    u32 w = xr[32 * a + t];
    v[a] = cmk(bf2f(w & 0xffffu), bf2f(w >> 16));
  }
  Tw16 tb = make_tw16(t, 9);
  fft_reg<16, -1>(v);
  apply_tw16<false>(v, tb);
  cf* rb = sm + row * RRS;
  {
    int pos = (t & 1) * 16 + (t >> 1);
#pragma unroll
    for (int k0 = 0; k0 < 16; ++k0) rb[k0 * RP + pos] = v[k0];
  }
  wave_sync();
  int k0 = t & 15, h = t >> 4;
  {
    const float4* p = reinterpret_cast<const float4*>(rb + k0 * RP + h * 16);
#pragma unroll
    for (int m = 0; m < 8; ++m) {
      float4 q = p[m];
      v[2 * m] = cmk(q.x, q.y);
      v[2 * m + 1] = cmk(q.z, q.w);
    }
  }
  fft_reg<16, -1>(v);
  bool hi = h != 0;
  static_for<0, 8>([&](auto ic) {
    constexpr int j = decltype(ic)::value;
    cf a = v[j], b = v[j + 8];
    swap16(a, b);
    cf br = cmk(hi ? b.y : b.x, hi ? -b.x : b.y);
    br = tw64<2 * j, -1>(br);
    v[j] = cadd(a, br);
    v[j + 8] = csub(a, br);
  });
  __syncthreads();
  cf* zs = sm + row * ZP;
#pragma unroll
  for (int r = 0; r < 16; ++r) zs[k0 + 16 * ((r & 7) + 8 * h + 16 * (r >> 3))] = v[r];
  __syncthreads();
  int r = tid & 15, kk = tid >> 4;
  const cf* zr = sm + r * ZP;
#pragma unroll
  for (int i = 0; i < 9; ++i) {
    int k = kk + 32 * i;
    if (k > 256) break;
    cf a = zr[k];
    cf b = zr[(512 - k) & 511];
    if (k == 0) {
      X1T[(size_t)0 * SEQ + s0 + r] = cmk(a.x + a.y, a.x - a.y);
    } else {
      cf w = tw[k];
      cf ev = cmk(0.5f * (a.x + b.x), 0.5f * (a.y - b.y));
      cf od = cmk(0.5f * (a.y + b.y), -0.5f * (a.x - b.x));
      cf wo = cmul(od, w);
      X1T[(size_t)k * SEQ + s0 + r] = cadd(ev, wo);
      if (k != 256) X1T[(size_t)(512 - k) * SEQ + s0 + r] = cmk(ev.x - wo.x, -(ev.y - wo.y));
    }
  }
}

__device__ __forceinline__ void browinv_block(const cf* __restrict__ X1T, u16* __restrict__ out, cf* sm, int bid, int tid) {
  cf* tw = sm + TWOFF;
  int s0 = bid * 16;
  {
    int r = tid & 15, kk = tid >> 4;
    cf a[9], b[9];
#pragma unroll
    for (int i = 0; i < 9; ++i) {
      int k = kk + 32 * i;
      if (k <= 256) {
        a[i] = X1T[(size_t)k * SEQ + s0 + r];
        b[i] = X1T[(size_t)((512 - k) & 511) * SEQ + s0 + r];
      }
    }
    if (tid <= 256) {
      float s, c;
      sincospif(-(float)tid / 512.0f, &s, &c);
      tw[tid] = cmk(c, s);
    }
    __syncthreads();
    cf* zr = sm + r * ZP;
#pragma unroll
    for (int i = 0; i < 9; ++i) {
      int k = kk + 32 * i;
      if (k <= 256) {
        if (k == 0) {
          zr[0] = cmk(a[i].x + a[i].y, a[i].x - a[i].y);
        } else {
          cf w = tw[k];
          cf ev = cmk(a[i].x + b[i].x, a[i].y - b[i].y);
          cf df = cmk(a[i].x - b[i].x, a[i].y + b[i].y);
          cf od = cmulc(df, w);
          zr[k] = cmk(ev.x - od.y, ev.y + od.x);
          if (k != 256) zr[512 - k] = cmk(ev.x + od.y, -ev.y + od.x);
        }
      }
    }
  }
  __syncthreads();
  int row = tid >> 5, t = tid & 31;
  int k0 = t & 15, h = t >> 4;
  cf v[16];
  {
    const cf* zs = sm + row * ZP;
#pragma unroll
    for (int r = 0; r < 16; ++r) v[r] = zs[k0 + 16 * ((r & 7) + 8 * h + 16 * (r >> 3))];
  }
  __syncthreads();
  bool hi = h != 0;
  static_for<0, 8>([&](auto ic) {
    constexpr int j = decltype(ic)::value;
    cf a = cadd(v[j], v[j + 8]);
    cf d = csub(v[j], v[j + 8]);
    cf dr = cmk(hi ? -d.y : d.x, hi ? d.x : d.y);
    dr = tw64<2 * j, 1>(dr);
    swap16(a, dr);
    v[j] = a;
    v[j + 8] = dr;
  });
  fft_reg<16, 1>(v);
  cf* rb = sm + row * RRS;
  {
    float4* p = reinterpret_cast<float4*>(rb + k0 * RP + h * 16);
#pragma unroll
    for (int m = 0; m < 8; ++m) p[m] = make_float4(v[2 * m].x, v[2 * m].y, v[2 * m + 1].x, v[2 * m + 1].y);
  }
  wave_sync();
  {
    int pos = (t & 1) * 16 + (t >> 1);
#pragma unroll
    for (int kk = 0; kk < 16; ++kk) v[kk] = rb[kk * RP + pos];
  }
  Tw16 tb = make_tw16(t, 9);
  apply_tw16<true>(v, tb);
  fft_reg<16, 1>(v);
  u32* orow = reinterpret_cast<u32*>(out + (size_t)(s0 + row) * (2 * DCH) + DCH);
#pragma unroll
  for (int a = 0; a < 16; ++a) orow[32 * a + t] = f2bf(v[a].x) | (f2bf(v[a].y) << 16);
}

__device__ __forceinline__ cf ldw(const u32* p, int i) {
  u32 w = p[i];
  return cmk(bf2f(w & 0xffffu), bf2f(w >> 16));
}

template <bool SPECIAL>
__device__ __forceinline__ void bcol_block(cf* __restrict__ X1T, const u32* __restrict__ WT, const cf* __restrict__ TW,
                                           cf* sm, int bid, int tid) {
  int grp = tid >> 7, t = tid & 127;
  int col = bid * 2 + grp;
  cf* buf = sm + grp * XBUF;
  cf v[16];
  cf* xc = X1T + (size_t)col * SEQ;
#pragma unroll
  for (int a = 0; a < 16; ++a) v[a] = xc[128 * a + t];
  int kb = (t >> 3) + 32 * (t & 7);
  fft_reg<16, -1>(v);
  fft2048_fwd_rest(v, buf, t, TW);
  const float sc = 1.0f / (2048.0f * 1024.0f);
  const u32* wc = WT + (size_t)col * SEQ;
  if (!SPECIAL) {
#pragma unroll
    for (int r = 0; r < 16; ++r) {
      u32 wq = wc[kb + 16 * (r >> 3) + 256 * (r & 7)];
      cf w = cmk(bf2f(wq & 0xffffu) * sc, bf2f(wq >> 16) * sc);
      v[r] = cmul(v[r], w);
    }
  } else {
    __syncthreads();
    if (col == 0) {
#pragma unroll
      for (int r = 0; r < 16; ++r) {
        int k = kb + 16 * (r >> 3) + 256 * (r & 7);
        buf[mphys(k)] = v[r];
      }
    } else {
#pragma unroll
      for (int r = 0; r < 16; ++r) {
        u32 wq = wc[kb + 16 * (r >> 3) + 256 * (r & 7)];
        cf w = cmk(bf2f(wq & 0xffffu) * sc, bf2f(wq >> 16) * sc);
        v[r] = cmul(v[r], w);
      }
    }
    __syncthreads();
    if (col == 0) {
      const u32* w0 = WT;
      const u32* w1 = WT + (size_t)512 * SEQ;
#pragma unroll 1
      for (int k = t; k <= 1024; k += 128) {
        int km = (2048 - k) & 2047;
        cf a = buf[mphys(k)], b = buf[mphys(km)];
        cf A = cmk(0.5f * (a.x + b.x), 0.5f * (a.y - b.y));
        cf B = cmk(0.5f * (a.y + b.y), -0.5f * (a.x - b.x));
        cf p = ldw(w0, k), q = ldw(w0, km);
        cf wh0 = cmk(0.5f * sc * (p.x + q.x), 0.5f * sc * (p.y - q.y));
        p = ldw(w1, k); q = ldw(w1, km);
        cf wh1 = cmk(0.5f * sc * (p.x + q.x), 0.5f * sc * (p.y - q.y));
        cf h0 = cmul(A, wh0), h1 = cmul(B, wh1);
        buf[mphys(km)] = cmk(h0.x + h1.y, -h0.y + h1.x);
        buf[mphys(k)] = cmk(h0.x - h1.y, h0.y + h1.x);
      }
    }
    __syncthreads();
    if (col == 0) {
#pragma unroll
      for (int r = 0; r < 16; ++r) {
        int k = kb + 16 * (r >> 3) + 256 * (r & 7);
        v[r] = buf[mphys(k)];
      }
    }
  }
  fft2048_inv_first(v, buf, t, TW);
  fft_reg<16, 1>(v);
#pragma unroll
  for (int a = 0; a < 16; ++a) xc[128 * a + t] = v[a];
}

__device__ __forceinline__ int kpos(int l, int s) {
  if (s == 0) return l;
  int half = 1 << (s - 1);
  int q = (l + half - 1 + ((l >> s) & 1)) >> s;
  return 128 * (s + 1) + q - 128;
}

__device__ __forceinline__ void aconv_block(const u32* __restrict__ uT, const float* __restrict__ Kc,
                                            const cf* __restrict__ TW, u32* __restrict__ yT, cf* sm, int d, int tid) {
  int grp = tid >> 7, t = tid & 127;
  cf* buf = sm + grp * XBUF;
  cf v[16];
  if (grp == 0) {
    const u32* ur = uT + (size_t)d * (SEQ / 2);
#pragma unroll
    for (int a = 0; a < 8; ++a) {
      u32 w = ur[128 * a + t];
      v[a] = cmk(bf2f(w & 0xffffu), bf2f(w >> 16));
    }
  } else {
    const float* kl = Kc + (size_t)d * KC_PITCH;
    static_for<0, 8>([&](auto ic) {
      constexpr int a = decltype(ic)::value;
      constexpr int s = (a < 1) ? 0 : (a < 2) ? 1 : (a < 4) ? 2 : 3;
      int l = 256 * a + 2 * t;
      v[a] = cmk(kl[kpos(l, s)], kl[kpos(l + 1, s)]);
    });
  }
  fft16_halfzero<-1>(v);
  fft2048_fwd_rest(v, buf, t, TW);
  int kb = (t >> 3) + 32 * (t & 7);
  __syncthreads();
#pragma unroll
  for (int r = 0; r < 16; ++r) {
    int k = kb + 16 * (r >> 3) + 256 * (r & 7);
    buf[mphys(k)] = v[r];
  }
  __syncthreads();
  if (grp != 0) return;
  {
    const cf* kbuf = sm + XBUF;
    cf L0 = TW[kb], L1 = TW[kb + 16];
    const float sc = 0.5f / 2048.0f;
    static_for<0, 16>([&](auto ic) {
      constexpr int r = decltype(ic)::value;
      int k = kb + 16 * (r >> 3) + 256 * (r & 7);
      int km = mphys((2048 - k) & 2047);
      cf a = v[r], b = buf[km];
      cf eu = cmk(sc * (a.x + b.x), sc * (a.y - b.y));
      cf ou = cmk(sc * (a.y + b.y), -sc * (a.x - b.x));
      cf ka = kbuf[mphys(k)], kbm = kbuf[km];
      cf ek = cmk(0.5f * (ka.x + kbm.x), 0.5f * (ka.y - kbm.y));
      cf ok = cmk(0.5f * (ka.y + kbm.y), -0.5f * (ka.x - kbm.x));
      cf w = tw64<8 * (r & 7), -1>((r >> 3) ? L1 : L0);
      cf wo = cmul(ok, w);
      cf T = cmk(wo.x - ek.y, wo.y + ek.x);
      v[r] = cadd(cmul(eu, ka), cmul(ou, T));
      if constexpr ((r & 3) == 3) __builtin_amdgcn_sched_barrier(0);
    });
  }
  fft2048_inv_first(v, buf, t, TW);
  fft_reg<16, 1>(v);
  u32* yr = yT + (size_t)d * (SEQ / 2);
#pragma unroll
  for (int a = 0; a < 8; ++a) yr[128 * a + t] = f2bf(v[a].x) | (f2bf(v[a].y) << 16);
}

__device__ __forceinline__ void utrans_block(const u16* __restrict__ u, u16* __restrict__ uT, u16* tile, int bid, int tid) {
#pragma unroll
  for (int it = 0; it < 4; ++it) {
    int tl = bid * 4 + it;
    int l0 = (tl >> 4) * 64, d0 = (tl & 15) * 64;
    int r = tid >> 3, c = tid & 7;
    uint4 q = *reinterpret_cast<const uint4*>(u + (size_t)(l0 + r) * DCH + d0 + 8 * c);
    u32 w[4] = {q.x, q.y, q.z, q.w};
    if (it) __syncthreads();
#pragma unroll
    for (int j = 0; j < 4; ++j) {
      tile[(8 * c + 2 * j) * 72 + r] = (u16)(w[j] & 0xffffu);
      tile[(8 * c + 2 * j + 1) * 72 + r] = (u16)(w[j] >> 16);
    }
    __syncthreads();
    uint4 o = *reinterpret_cast<const uint4*>(tile + r * 72 + 8 * c);
    *reinterpret_cast<uint4*>(uT + (size_t)(d0 + r) * SEQ + l0 + 8 * c) = o;
  }
}

__device__ __forceinline__ void ytrans_block(const u16* __restrict__ yT, const u16* __restrict__ u,
                                             const u16* __restrict__ Dskip, u16* __restrict__ out, u16* tile,
                                             int bid, int tid) {
#pragma unroll
  for (int it = 0; it < 4; ++it) {
    int tl = bid * 4 + it;
    int l0 = (tl >> 4) * 64, d0 = (tl & 15) * 64;
    int r = tid >> 3, c = tid & 7;
    uint4 q = *reinterpret_cast<const uint4*>(yT + (size_t)(d0 + r) * SEQ + l0 + 8 * c);
    uint4 uq = *reinterpret_cast<const uint4*>(u + (size_t)(l0 + r) * DCH + d0 + 8 * c);
    uint4 dq = *reinterpret_cast<const uint4*>(Dskip + d0 + 8 * c);
    u32 w[4] = {q.x, q.y, q.z, q.w};
    if (it) __syncthreads();
#pragma unroll
    for (int j = 0; j < 4; ++j) {
      tile[(8 * c + 2 * j) * 72 + r] = (u16)(w[j] & 0xffffu);
      tile[(8 * c + 2 * j + 1) * 72 + r] = (u16)(w[j] >> 16);
    }
    __syncthreads();
    uint4 y = *reinterpret_cast<const uint4*>(tile + r * 72 + 8 * c);
    u32 yy[4] = {y.x, y.y, y.z, y.w}, uu[4] = {uq.x, uq.y, uq.z, uq.w}, dd[4] = {dq.x, dq.y, dq.z, dq.w};
    u32 oo[4];
#pragma unroll
    for (int j = 0; j < 4; ++j) {
      float o0 = bf2f(yy[j] & 0xffffu) + bfround(bf2f(uu[j] & 0xffffu) * bf2f(dd[j] & 0xffffu));
      float o1 = bf2f(yy[j] >> 16) + bfround(bf2f(uu[j] >> 16) * bf2f(dd[j] >> 16));
      oo[j] = f2bf(o0) | (f2bf(o1) << 16);
    }
    *reinterpret_cast<uint4*>(out + (size_t)(l0 + r) * (2 * DCH) + d0 + 8 * c) = make_uint4(oo[0], oo[1], oo[2], oo[3]);
  }
}

#define SM_CF (TWOFF + 260)

__global__ __launch_bounds__(512, 2) void k1(KParams P, float* Kc, const u16* x, cf* X1T, const u16* wr,
                                            const u16* wi, u32* WT, cf* TW, const u16* u, u16* uT) {
  __shared__ __attribute__((aligned(16))) cf sm[SM_CF];
  int b = blockIdx.x, tid = threadIdx.x;
  if (b < 384) {
    int q = b / 3, r = b % 3;
    if (r < 2) {
      int wave = tid >> 6, lane = tid & 63;
      if (wave < 4) kgen_wave(P, Kc, reinterpret_cast<float*>(sm) + wave * KCH * KREC, (q * 2 + r) * 4 + wave, lane);
    } else {
      brow_block(x, X1T, sm, q, tid);
    }
  } else if (b < 384 + 160) {
    int i = b - 384;
    if (i < 4) {
      int j = i * 512 + tid;
      float s, c;
      sincospif(-(float)j / 1024.0f, &s, &c);
      TW[j] = cmk(c, s);
    }
    wt_block(wr, wi, WT, reinterpret_cast<u32*>(sm), i, tid);
  } else {
    utrans_block(u, uT, reinterpret_cast<u16*>(sm), b - 544, tid);
  }
}

__global__ __launch_bounds__(256, 4) void k2(cf* X1T, const u32* WT, const u32* uT, const float* Kc, const cf* TW,
                                            u32* yT) {
  __shared__ __attribute__((aligned(16))) cf sm[2 * XBUF];
  int b = blockIdx.x, tid = threadIdx.x;
  int q = b / 5, r = b % 5;
  if (r == 4) {
    if (q == 0) bcol_block<true>(X1T, WT, TW, sm, q, tid);
    else bcol_block<false>(X1T, WT, TW, sm, q, tid);
  } else {
    aconv_block(uT, Kc, TW, yT, sm, q * 4 + r, tid);
  }
}

__global__ __launch_bounds__(512, 2) void k3(const cf* X1T, const u16* yT, const u16* u, const u16* Dskip, u16* out) {
  __shared__ __attribute__((aligned(16))) cf sm[SM_CF];
  int b = blockIdx.x, tid = threadIdx.x;
  if (b & 1) {
    browinv_block(X1T, out, sm, b >> 1, tid);
  } else {
    ytrans_block(yT, u, Dskip, out, reinterpret_cast<u16*>(sm), b >> 1, tid);
  }
}

extern "C" void kernel_launch(void* const* d_in, const int* in_sizes, int n_in, void* d_out, int out_size,
                              void* d_ws, size_t ws_size, hipStream_t stream) {
  const u16* u = (const u16*)d_in[0];
  KParams P;
  P.Are = (const u16*)d_in[1];
  P.Aim = (const u16*)d_in[2];
  P.Bre = (const u16*)d_in[3];
  P.Bim = (const u16*)d_in[4];
  P.Cre = (const u16*)d_in[5];
  P.Cim = (const u16*)d_in[6];
  P.logdt = (const u16*)d_in[7];
  const u16* Dskip = (const u16*)d_in[8];
  const u16* x = (const u16*)d_in[9];
  const u16* wr = (const u16*)d_in[10];
  const u16* wi = (const u16*)d_in[11];
  u16* out = (u16*)d_out;
  char* ws = (char*)d_ws;
  cf* X1T = (cf*)ws;
  u32* WT = (u32*)(ws + (size_t)8 * 1024 * 1024);
  float* Kc = (float*)(ws + (size_t)16 * 1024 * 1024);
  cf* TW = (cf*)(ws + (size_t)20 * 1024 * 1024);
  u16* uT = (u16*)(ws + (size_t)24 * 1024 * 1024);
  u16* yT = (u16*)(ws + (size_t)28 * 1024 * 1024);

  k1<<<384 + 160 + 128, 512, 0, stream>>>(P, Kc, x, X1T, wr, wi, WT, TW, u, uT);
  k2<<<1280, 256, 0, stream>>>(X1T, WT, (const u32*)uT, Kc, TW, (u32*)yT);
  k3<<<256, 512, 0, stream>>>(X1T, yT, u, Dskip, out);
}
```

```cpp
#include <hip/hip_runtime.h>
#include <hip/hip_bf16.h>
#include <type_traits>

#define SEQ 2048
#define DCH 1024
#define NST 128
#define DF 513

typedef float2 cf;
typedef unsigned int u32;
typedef unsigned short u16;
using f32x16 = __attribute__((ext_vector_type(16))) float;

template <int I, int N, class F>
__device__ __forceinline__ void static_for(F&& f) {
  if constexpr (I < N) {
    f(std::integral_constant<int, I>{});
    static_for<I + 1, N>(f);
  }
}

__device__ __forceinline__ cf cmk(float x, float y) { cf r; r.x = x; r.y = y; return r; }
__device__ __forceinline__ cf cadd(cf a, cf b) { return cmk(a.x + b.x, a.y + b.y); }
__device__ __forceinline__ cf csub(cf a, cf b) { return cmk(a.x - b.x, a.y - b.y); }
__device__ __forceinline__ cf cmul(cf a, cf b) {
  return cmk(fmaf(a.x, b.x, -(a.y * b.y)), fmaf(a.x, b.y, a.y * b.x));
}
__device__ __forceinline__ cf cmulc(cf a, cf b) {
  return cmk(fmaf(a.x, b.x, a.y * b.y), fmaf(a.y, b.x, -(a.x * b.y)));
}
__device__ __forceinline__ cf csqr(cf a) { return cmk(fmaf(a.x, a.x, -(a.y * a.y)), 2.0f * a.x * a.y); }

__device__ __forceinline__ float bf2f(u32 h) { return __uint_as_float(h << 16); }
__device__ __forceinline__ u32 f2bf(float f) {
  __hip_bfloat16 b = __float2bfloat16(f);
  return (u32)(*reinterpret_cast<u16*>(&b));
}
__device__ __forceinline__ float bfround(float f) { return bf2f(f2bf(f)); }

__device__ __forceinline__ void wave_sync() {
  __builtin_amdgcn_fence(__ATOMIC_SEQ_CST, "wavefront");
  __builtin_amdgcn_wave_barrier();
  __builtin_amdgcn_fence(__ATOMIC_SEQ_CST, "wavefront");
}

constexpr double COS64_TAB[17] = {
    1.0, 0.99518472667219688624, 0.98078528040323044913, 0.95694033573220886494,
    0.92387953251128675613, 0.88192126434835502971, 0.83146961230254523708,
    0.77301045336273696081, 0.70710678118654752440, 0.63439328416364549822,
    0.55557023301960222474, 0.47139673682599764856, 0.38268343236508977173,
    0.29028467725446236764, 0.19509032201612826785, 0.09801714032956060199, 0.0};
constexpr double cos64(int j) {
  j = ((j % 64) + 64) % 64;
  if (j > 32) j = 64 - j;
  return (j <= 16) ? COS64_TAB[j] : -COS64_TAB[32 - j];
}
constexpr double sin64(int j) { return cos64(j - 16); }

template <int J, int DIR>
__device__ __forceinline__ cf tw64(cf a) {
  constexpr int j = ((J % 64) + 64) % 64;
  if constexpr (j == 0) {
    return a;
  } else if constexpr (j == 32) {
    return cmk(-a.x, -a.y);
  } else if constexpr (j == 16) {
    return (DIR > 0) ? cmk(-a.y, a.x) : cmk(a.y, -a.x);
  } else if constexpr (j == 48) {
    return (DIR > 0) ? cmk(a.y, -a.x) : cmk(-a.y, a.x);
  } else if constexpr (j % 16 == 8) {
    constexpr float h = 0.70710678118654752440f;
    constexpr float c = (float)cos64(j);
    constexpr float s = (float)(DIR * sin64(j));
    constexpr float cs = (c > 0 ? 1.f : -1.f), ss = (s > 0 ? 1.f : -1.f);
    float re = cs * a.x - ss * a.y;
    float im = ss * a.x + cs * a.y;
    return cmk(re * h, im * h);
  } else {
    constexpr float c = (float)cos64(j);
    constexpr float s = (float)(DIR * sin64(j));
    return cmk(fmaf(a.x, c, -(a.y * s)), fmaf(a.x, s, a.y * c));
  }
}

template <int N, int DIR, int S>
__device__ __forceinline__ void fft_rec(const cf* in, cf* out) {
  if constexpr (N == 1) {
    out[0] = in[0];
  } else if constexpr (N == 2) {
    cf a = in[0], b = in[S];
    out[0] = cadd(a, b);
    out[1] = csub(a, b);
  } else {
    constexpr int Q = N / 4;
    cf t0[Q], t1[Q], t2[Q], t3[Q];
    fft_rec<Q, DIR, 4 * S>(in, t0);
    fft_rec<Q, DIR, 4 * S>(in + S, t1);
    fft_rec<Q, DIR, 4 * S>(in + 2 * S, t2);
    fft_rec<Q, DIR, 4 * S>(in + 3 * S, t3);
    static_for<0, Q>([&](auto ic) {
      constexpr int k = decltype(ic)::value;
      constexpr int M = 64 / N;
      cf a = t0[k];
      cf b = tw64<k * M, DIR>(t1[k]);
      cf c = tw64<2 * k * M, DIR>(t2[k]);
      cf d = tw64<3 * k * M, DIR>(t3[k]);
      cf s0 = cadd(a, c), s1 = csub(a, c), s2 = cadd(b, d), s3 = csub(b, d);
      cf s3r = (DIR > 0) ? cmk(-s3.y, s3.x) : cmk(s3.y, -s3.x);
      out[k] = cadd(s0, s2);
      out[k + Q] = cadd(s1, s3r);
      out[k + 2 * Q] = csub(s0, s2);
      out[k + 3 * Q] = csub(s1, s3r);
    });
  }
}

template <int N, int DIR>
__device__ __forceinline__ void fft_reg(cf* v) {
  cf o[N];
  fft_rec<N, DIR, 1>(v, o);
#pragma unroll
  for (int i = 0; i < N; ++i) v[i] = o[i];
}

template <int DIR>
__device__ __forceinline__ void fft16_halfzero(cf* v) {
  cf a[8], b[8], oa[8], ob[8];
  static_for<0, 8>([&](auto ic) {
    constexpr int n = decltype(ic)::value;
    a[n] = v[n];
    b[n] = tw64<4 * n, DIR>(v[n]);
  });
  fft_rec<8, DIR, 1>(a, oa);
  fft_rec<8, DIR, 1>(b, ob);
#pragma unroll
  for (int k = 0; k < 8; ++k) {
    v[2 * k] = oa[k];
    v[2 * k + 1] = ob[k];
  }
}

struct Tw16 { cf w[4]; };

__device__ __forceinline__ Tw16 make_tw16(int n, int logN) {
  Tw16 b;
#pragma unroll
  for (int t = 0; t < 4; ++t) {
    int e = (n << t) & ((1 << logN) - 1);
    float s, c;
    sincospif(-2.0f * (float)e / (float)(1 << logN), &s, &c);
    b.w[t] = cmk(c, s);
  }
  return b;
}

__device__ __forceinline__ Tw16 load_tw16(const cf* __restrict__ TW, int n) {
  Tw16 b;
#pragma unroll
  for (int t = 0; t < 4; ++t) b.w[t] = TW[(n << t) & 2047];
  return b;
}

template <bool CONJ>
__device__ __forceinline__ void apply_tw16(cf* v, const Tw16& b) {
  cf p[16];
  p[1] = b.w[0];
  p[2] = b.w[1];
  p[4] = b.w[2];
  p[8] = b.w[3];
  asm volatile("" : "+v"(p[1].x), "+v"(p[1].y), "+v"(p[2].x), "+v"(p[2].y), "+v"(p[4].x), "+v"(p[4].y),
               "+v"(p[8].x), "+v"(p[8].y));
  static_for<3, 16>([&](auto ic) {
    constexpr int k = decltype(ic)::value;
    constexpr int top = (k >= 8) ? 8 : (k >= 4) ? 4 : 2;
    if constexpr (k != top) p[k] = cmul(p[k - top], p[top]);
  });
#pragma unroll
  for (int k = 1; k < 16; ++k) v[k] = CONJ ? cmulc(v[k], p[k]) : cmul(v[k], p[k]);
}

__device__ __forceinline__ void swap16(cf& a, cf& b) {
  auto r0 = __builtin_amdgcn_permlane16_swap(__float_as_uint(a.x), __float_as_uint(b.x), false, false);
  auto r1 = __builtin_amdgcn_permlane16_swap(__float_as_uint(a.y), __float_as_uint(b.y), false, false);
  a.x = __uint_as_float(r0[0]);
  b.x = __uint_as_float(r0[1]);
  a.y = __uint_as_float(r1[0]);
  b.y = __uint_as_float(r1[1]);
}

#define XP 152
#define XBUF (16 * XP)
typedef __attribute__((ext_vector_type(2))) float f2v;
typedef volatile __attribute__((address_space(3))) f2v* ldsp;
__device__ __forceinline__ cf lds_ld(const cf* p) {
  f2v v = *(ldsp)(p);
  return cmk(v.x, v.y);
}
__device__ __forceinline__ void lds_st(cf* p, cf a) {
  f2v v;
  v.x = a.x;
  v.y = a.y;
  *(ldsp)(p) = v;
}

struct FCtx {
  Tw16 ta, tb;
  int t, offA, offB, kA, kB;
};
__device__ __forceinline__ FCtx make_fctx(const cf* __restrict__ TW, int t) {
  FCtx f;
  int k0 = t >> 3, c = t & 7;
  f.t = t;
  f.ta = load_tw16(TW, t);
  f.tb = load_tw16(TW, 16 * c);
  int k0m = (16 - k0) & 15;
  int k1m = (k0 != 0) ? (15 - c) : ((c != 0) ? (16 - c) : 8);
  f.offA = k0 * XP + 9 * c;
  f.offB = k0m * XP + 9 * k1m;
  f.kA = k0 + 16 * c;
  f.kB = k0m + 16 * k1m;
  return f;
}

__device__ __forceinline__ void fft2048_fwd_rest(cf* v, cf* buf, const FCtx& f) {
  apply_tw16<false>(v, f.ta);
  __syncthreads();
#pragma unroll
  for (int k0 = 0; k0 < 16; ++k0) lds_st(buf + k0 * XP + f.t, v[k0]);
  __syncthreads();
  cf* rb = buf + (f.t >> 3) * XP + (f.t & 7);
#pragma unroll
  for (int b = 0; b < 16; ++b) v[b] = lds_ld(rb + 8 * b);
  fft_reg<16, -1>(v);
  apply_tw16<false>(v, f.tb);
#pragma unroll
  for (int k1 = 0; k1 < 16; ++k1) lds_st(rb + 9 * k1, v[k1]);
  __syncthreads();
#pragma unroll
  for (int j = 0; j < 8; ++j) v[j] = lds_ld(buf + f.offA + j);
#pragma unroll
  for (int j = 0; j < 8; ++j) v[8 + j] = lds_ld(buf + f.offB + j);
  fft_reg<8, -1>(v);
  fft_reg<8, -1>(v + 8);
}

__device__ __forceinline__ void fft2048_inv_first(cf* v, cf* buf, const FCtx& f) {
  fft_reg<8, 1>(v);
  fft_reg<8, 1>(v + 8);
  __syncthreads();
#pragma unroll
  for (int j = 0; j < 8; ++j) lds_st(buf + f.offA + j, v[j]);
#pragma unroll
  for (int j = 0; j < 8; ++j) lds_st(buf + f.offB + j, v[8 + j]);
  __syncthreads();
  cf* rb = buf + (f.t >> 3) * XP + (f.t & 7);
#pragma unroll
  for (int k1 = 0; k1 < 16; ++k1) v[k1] = lds_ld(rb + 9 * k1);
  apply_tw16<true>(v, f.tb);
  fft_reg<16, 1>(v);
#pragma unroll
  for (int b = 0; b < 16; ++b) lds_st(rb + 8 * b, v[b]);
  __syncthreads();
#pragma unroll
  for (int kk = 0; kk < 16; ++kk) v[kk] = lds_ld(buf + kk * XP + f.t);
  apply_tw16<true>(v, f.ta);
}

#define KC_PITCH 648
#define KREC 132
#define KCH 32

__device__ __forceinline__ cf cexp_scaled(float ar, float ai, float sc) {
  float e = expf(ar * sc);
  float s, c;
  sincosf(ai * sc, &s, &c);
  return cmk(e * c, e * s);
}

struct KParams { const u16 *Are, *Aim, *Bre, *Bim, *Cre, *Cim, *logdt; };

__device__ __forceinline__ void kgen_wave(const KParams& P, float* __restrict__ Kc, float* lds, int d, int lane) {
  float dt = bfround(expf(bf2f(P.logdt[d])));
  f32x16 acc;
#pragma unroll
  for (int i = 0; i < 16; ++i) acc[i] = 0.f;
  for (int round = 0; round < 2; ++round) {
    int n = round * 64 + lane;
    int idx = d * NST + n;
    float ar = dt * bf2f(P.Are[idx]), ai = dt * bf2f(P.Aim[idx]);
    cf dB = cmk(dt * bf2f(P.Bre[idx]), dt * bf2f(P.Bim[idx]));
    cf C = cmk(bf2f(P.Cre[idx]), bf2f(P.Cim[idx]));
    cf c0 = cmul(C, dB);
    cf e[12];
    e[0] = cexp_scaled(ar, ai, 1.0f);
    e[4] = cexp_scaled(ar, ai, 16.0f);
    e[8] = cexp_scaled(ar, ai, 256.0f);
    e[1] = csqr(e[0]); e[2] = csqr(e[1]); e[3] = csqr(e[2]);
    e[5] = csqr(e[4]); e[6] = csqr(e[5]); e[7] = csqr(e[6]);
    e[9] = csqr(e[8]); e[10] = csqr(e[9]); e[11] = csqr(e[10]);
    cf G[32], E[32];
    G[0] = c0;
    G[1] = cmul(c0, e[0]);
    G[2] = cmul(c0, e[1]);
    G[3] = cmul(G[1], e[1]);
    G[4] = cmul(c0, e[2]);
    G[5] = cmul(G[1], e[2]);
    G[6] = cmul(G[2], e[2]);
    G[7] = cmul(G[3], e[2]);
    G[8] = cmul(c0, e[8]);
    G[9] = cmul(G[2], e[8]);
    G[10] = cmul(G[4], e[8]);
    G[11] = cmul(G[6], e[8]);
    G[12] = cmul(c0, e[9]);
    G[13] = cmul(G[4], e[9]);
    G[14] = cmul(G[8], e[9]);
    G[15] = cmul(G[10], e[9]);
    G[16] = cmul(c0, e[10]);
    G[17] = cmul(G[8], e[10]);
    G[18] = cmul(G[12], e[10]);
    G[19] = cmul(G[14], e[10]);
    G[20] = cmul(c0, e[11]);
#pragma unroll
    for (int i = 21; i < 32; ++i) G[i] = cmk(0.f, 0.f);
    E[0] = cmk(1.f, 0.f);
    E[1] = e[3]; E[2] = e[4]; E[4] = e[5]; E[8] = e[6]; E[16] = e[7];
    static_for<3, 32>([&](auto ic) {
      constexpr int k = decltype(ic)::value;
      constexpr int top = (k >= 16) ? 16 : (k >= 8) ? 8 : (k >= 4) ? 4 : 2;
      if constexpr (k != top) E[k] = cmul(E[k - top], E[top]);
    });
    for (int ch = 0; ch < 64 / KCH; ++ch) {
      if ((lane / KCH) == ch) {
        float4* rec = reinterpret_cast<float4*>(lds + (lane % KCH) * KREC);
#pragma unroll
        for (int l2 = 0; l2 < 16; ++l2) {
          rec[l2] = make_float4(G[2 * l2].x, E[2 * l2].x, G[2 * l2 + 1].x, E[2 * l2 + 1].x);
          rec[16 + l2] = make_float4(-G[2 * l2].y, E[2 * l2].y, -G[2 * l2 + 1].y, E[2 * l2 + 1].y);
        }
      }
      wave_sync();
#pragma unroll
      for (int nn = 0; nn < KCH; ++nn) {
        float2 ab = *reinterpret_cast<const float2*>(lds + nn * KREC + 2 * lane);
        acc = __builtin_amdgcn_mfma_f32_32x32x2f32(ab.x, ab.y, acc, 0, 0, 0);
      }
      wave_sync();
    }
  }
  int col = lane & 31, hh = lane >> 5;
  float* dst = Kc + (size_t)d * KC_PITCH;
#pragma unroll
  for (int r = 0; r < 16; ++r) {
    int row = (r & 3) + 8 * (r >> 2) + 4 * hh;
    int p = -1;
    if (row < 8) p = row + 8 * col;
    else if (row < 12) p = 256 + (row - 8) + 4 * col;
    else if (row < 16) p = 384 + ((row - 12) & 1) + 64 * ((row - 12) >> 1) + 2 * col;
    else if (row < 20) p = 512 + 32 * (row - 16) + col;
    else if (row == 20 && col == 0) p = 640;
    if (p >= 0) dst[p] = acc[r];
  }
}

__device__ __forceinline__ void wt_block(const u16* __restrict__ wr, const u16* __restrict__ wi, u32* __restrict__ WT,
                                         u32* tile, int bid, int tid) {
  int bk = bid % 5, bs = bid / 5;
  int k0 = bk * 128, s0 = bs * 64;
  int tx = tid & 127, ty = tid >> 7;
  u32 val[16];
#pragma unroll
  for (int i = 0; i < 16; ++i) {
    int r = ty + 4 * i;
    int k = k0 + tx, s = s0 + r;
    val[i] = 0;
    if (k < DF) val[i] = (u32)wr[s * DF + k] | ((u32)wi[s * DF + k] << 16);
  }
#pragma unroll
  for (int i = 0; i < 16; ++i) tile[(ty + 4 * i) * 129 + tx] = val[i];
  __syncthreads();
  int sx = tid & 63, ky = tid >> 6;
#pragma unroll
  for (int i = 0; i < 16; ++i) {
    int kk = ky + 8 * i;
    int k = k0 + kk;
    if (k < DF) WT[(size_t)k * SEQ + s0 + sx] = tile[sx * 129 + kk];
  }
}

#define RP 34
#define RRS (16 * RP)
#define ZP 517
#define TWOFF (16 * RRS)

__device__ __forceinline__ void brow_block(const u16* __restrict__ x, cf* __restrict__ X1T, cf* sm, int bid, int tid) {
  cf* tw = sm + TWOFF;
  int s0 = bid * 16;
  if (tid <= 256) {
    float s, c;
    sincospif(-(float)tid / 512.0f, &s, &c);
    tw[tid] = cmk(c, s);
  }
  int row = tid >> 5, t = tid & 31;
  cf v[16];
  const u32* xr = reinterpret_cast<const u32*>(x + (size_t)(s0 + row) * DCH);
#pragma unroll
  for (int a = 0; a < 16; ++a) {
    u32 w = xr[32 * a + t];
    v[a] = cmk(bf2f(w & 0xffffu), bf2f(w >> 16));
  }
  Tw16 tb = make_tw16(t, 9);
  fft_reg<16, -1>(v);
  apply_tw16<false>(v, tb);
  cf* rb = sm + row * RRS;
  {
    int pos = (t & 1) * 16 + (t >> 1);
#pragma unroll
    for (int k0 = 0; k0 < 16; ++k0) rb[k0 * RP + pos] = v[k0];
  }
  wave_sync();
  int k0 = t & 15, h = t >> 4;
  {
    const float4* p = reinterpret_cast<const float4*>(rb + k0 * RP + h * 16);
#pragma unroll
    for (int m = 0; m < 8; ++m) {
      float4 q = p[m];
      v[2 * m] = cmk(q.x, q.y);
      v[2 * m + 1] = cmk(q.z, q.w);
    }
  }
  fft_reg<16, -1>(v);
  bool hi = h != 0;
  static_for<0, 8>([&](auto ic) {
    constexpr int j = decltype(ic)::value;
    cf a = v[j], b = v[j + 8];
    swap16(a, b);
    cf br = cmk(hi ? b.y : b.x, hi ? -b.x : b.y);
    br = tw64<2 * j, -1>(br);
    v[j] = cadd(a, br);
    v[j + 8] = csub(a, br);
  });
  __syncthreads();
  cf* zs = sm + row * ZP;
#pragma unroll
  for (int r = 0; r < 16; ++r) zs[k0 + 16 * ((r & 7) + 8 * h + 16 * (r >> 3))] = v[r];
  __syncthreads();
  int r = tid & 15, kk = tid >> 4;
  const cf* zr = sm + r * ZP;
#pragma unroll
  for (int i = 0; i < 9; ++i) {
    int k = kk + 32 * i;
    if (k > 256) break;
    cf a = zr[k];
    cf b = zr[(512 - k) & 511];
    if (k == 0) {
      X1T[(size_t)0 * SEQ + s0 + r] = cmk(a.x + a.y, a.x - a.y);
    } else {
      cf w = tw[k];
      cf ev = cmk(0.5f * (a.x + b.x), 0.5f * (a.y - b.y));
      cf od = cmk(0.5f * (a.y + b.y), -0.5f * (a.x - b.x));
      cf wo = cmul(od, w);
      X1T[(size_t)k * SEQ + s0 + r] = cadd(ev, wo);
      if (k != 256) X1T[(size_t)(512 - k) * SEQ + s0 + r] = cmk(ev.x - wo.x, -(ev.y - wo.y));
    }
  }
}

__device__ __forceinline__ void browinv_block(const cf* __restrict__ X1T, u16* __restrict__ out, cf* sm, int bid, int tid) {
  cf* tw = sm + TWOFF;
  int s0 = bid * 16;
  {
    int r = tid & 15, kk = tid >> 4;
    cf a[9], b[9];
#pragma unroll
    for (int i = 0; i < 9; ++i) {
      int k = kk + 32 * i;
      if (k <= 256) {
        a[i] = X1T[(size_t)k * SEQ + s0 + r];
        b[i] = X1T[(size_t)((512 - k) & 511) * SEQ + s0 + r];
      }
    }
    if (tid <= 256) {
      float s, c;
      sincospif(-(float)tid / 512.0f, &s, &c);
      tw[tid] = cmk(c, s);
    }
    __syncthreads();
    cf* zr = sm + r * ZP;
#pragma unroll
    for (int i = 0; i < 9; ++i) {
      int k = kk + 32 * i;
      if (k <= 256) {
        if (k == 0) {
          zr[0] = cmk(a[i].x + a[i].y, a[i].x - a[i].y);
        } else {
          cf w = tw[k];
          cf ev = cmk(a[i].x + b[i].x, a[i].y - b[i].y);
          cf df = cmk(a[i].x - b[i].x, a[i].y + b[i].y);
          cf od = cmulc(df, w);
          zr[k] = cmk(ev.x - od.y, ev.y + od.x);
          if (k != 256) zr[512 - k] = cmk(ev.x + od.y, -ev.y + od.x);
        }
      }
    }
  }
  __syncthreads();
  int row = tid >> 5, t = tid & 31;
  int k0 = t & 15, h = t >> 4;
  cf v[16];
  {
    const cf* zs = sm + row * ZP;
#pragma unroll
    for (int r = 0; r < 16; ++r) v[r] = zs[k0 + 16 * ((r & 7) + 8 * h + 16 * (r >> 3))];
  }
  __syncthreads();
  bool hi = h != 0;
  static_for<0, 8>([&](auto ic) {
    constexpr int j = decltype(ic)::value;
    cf a = cadd(v[j], v[j + 8]);
    cf d = csub(v[j], v[j + 8]);
    cf dr = cmk(hi ? -d.y : d.x, hi ? d.x : d.y);
    dr = tw64<2 * j, 1>(dr);
    swap16(a, dr);
    v[j] = a;
    v[j + 8] = dr;
  });
  fft_reg<16, 1>(v);
  cf* rb = sm + row * RRS;
  {
    float4* p = reinterpret_cast<float4*>(rb + k0 * RP + h * 16);
#pragma unroll
    for (int m = 0; m < 8; ++m) p[m] = make_float4(v[2 * m].x, v[2 * m].y, v[2 * m + 1].x, v[2 * m + 1].y);
  }
  wave_sync();
  {
    int pos = (t & 1) * 16 + (t >> 1);
#pragma unroll
    for (int kk = 0; kk < 16; ++kk) v[kk] = rb[kk * RP + pos];
  }
  Tw16 tb = make_tw16(t, 9);
  apply_tw16<true>(v, tb);
  fft_reg<16, 1>(v);
  u32* orow = reinterpret_cast<u32*>(out + (size_t)(s0 + row) * (2 * DCH) + DCH);
#pragma unroll
  for (int a = 0; a < 16; ++a) orow[32 * a + t] = f2bf(v[a].x) | (f2bf(v[a].y) << 16);
}

__device__ __forceinline__ cf ldw(const u32* p, int i) {
  u32 w = p[i];
  return cmk(bf2f(w & 0xffffu), bf2f(w >> 16));
}

template <bool SPECIAL>
__device__ __forceinline__ void bcol_block(cf* __restrict__ X1T, const u32* __restrict__ WT, const cf* __restrict__ TW,
                                           cf* buf, int col, int t) {
  cf v[16];
  cf* xc = X1T + (size_t)col * SEQ;
#pragma unroll
  for (int a = 0; a < 16; ++a) v[a] = xc[128 * a + t];
  FCtx f = make_fctx(TW, t);
  const float sc = 1.0f / (2048.0f * 1024.0f);
  const u32* wc = WT + (size_t)col * SEQ;
  u32 wq[16];
  if (!SPECIAL) {
#pragma unroll
    for (int j = 0; j < 8; ++j) {
      wq[j] = wc[f.kA + 256 * j];
      wq[8 + j] = wc[f.kB + 256 * j];
    }
  }
  fft_reg<16, -1>(v);
  fft2048_fwd_rest(v, buf, f);
  if (!SPECIAL) {
#pragma unroll
    for (int r = 0; r < 16; ++r) {
      cf w = cmk(bf2f(wq[r] & 0xffffu) * sc, bf2f(wq[r] >> 16) * sc);
      v[r] = cmul(v[r], w);
    }
  } else {
    __syncthreads();
#pragma unroll
    for (int j = 0; j < 8; ++j) {
      buf[f.kA + 256 * j] = v[j];
      buf[f.kB + 256 * j] = v[8 + j];
    }
    __syncthreads();
    const u32* w0 = WT;
    const u32* w1 = WT + (size_t)512 * SEQ;
#pragma unroll 1
    for (int k = t; k <= 1024; k += 128) {
      int km = (2048 - k) & 2047;
      cf a = buf[k], b = buf[km];
      cf A = cmk(0.5f * (a.x + b.x), 0.5f * (a.y - b.y));
      cf B = cmk(0.5f * (a.y + b.y), -0.5f * (a.x - b.x));
      cf p = ldw(w0, k), q = ldw(w0, km);
      cf wh0 = cmk(0.5f * sc * (p.x + q.x), 0.5f * sc * (p.y - q.y));
      p = ldw(w1, k); q = ldw(w1, km);
      cf wh1 = cmk(0.5f * sc * (p.x + q.x), 0.5f * sc * (p.y - q.y));
      cf h0 = cmul(A, wh0), h1 = cmul(B, wh1);
      buf[km] = cmk(h0.x + h1.y, -h0.y + h1.x);
      buf[k] = cmk(h0.x - h1.y, h0.y + h1.x);
    }
    __syncthreads();
#pragma unroll
    for (int j = 0; j < 8; ++j) {
      v[j] = buf[f.kA + 256 * j];
      v[8 + j] = buf[f.kB + 256 * j];
    }
  }
  fft2048_inv_first(v, buf, f);
  fft_reg<16, 1>(v);
#pragma unroll
  for (int a = 0; a < 16; ++a) xc[128 * a + t] = v[a];
}

__device__ __forceinline__ int kpos(int l, int s) {
  if (s == 0) return l;
  int half = 1 << (s - 1);
  int q = (l + half - 1 + ((l >> s) & 1)) >> s;
  return 128 * (s + 1) + q - 128;
}
__device__ __forceinline__ cf csel(bool c, cf a, cf b) { return cmk(c ? a.x : b.x, c ? a.y : b.y); }

__device__ __forceinline__ void aconv_block(const u32* __restrict__ uT, const float* __restrict__ Kc,
                                            const cf* __restrict__ TW, u32* __restrict__ yT, cf* buf, int d, int t) {
  cf v[16], kz[16];
  u32 uw[8];
  {
    const float* kl = Kc + (size_t)d * KC_PITCH;
    static_for<0, 8>([&](auto ic) {
      constexpr int a = decltype(ic)::value;
      constexpr int s = (a < 1) ? 0 : (a < 2) ? 1 : (a < 4) ? 2 : 3;
      int l = 256 * a + 2 * t;
      kz[a] = cmk(kl[kpos(l, s)], kl[kpos(l + 1, s)]);
    });
    const u32* ur = uT + (size_t)d * (SEQ / 2);
#pragma unroll
    for (int a = 0; a < 8; ++a) uw[a] = ur[128 * a + t];
  }
  FCtx f = make_fctx(TW, t);
  cf L = TW[f.kA];
  cf L2 = TW[t == 0 ? 128 : f.kA];
  fft16_halfzero<-1>(kz);
  fft2048_fwd_rest(kz, buf, f);
#pragma unroll
  for (int a = 0; a < 8; ++a) v[a] = cmk(bf2f(uw[a] & 0xffffu), bf2f(uw[a] >> 16));
  fft16_halfzero<-1>(v);
  fft2048_fwd_rest(v, buf, f);
  {
    const bool sp = (t == 0);
    const float sc = 0.5f / 2048.0f;
    cf oP[8], oQ[8];
    static_for<0, 8>([&](auto ic) {
      constexpr int j = decltype(ic)::value;
      cf a, b, ka, kb;
      if constexpr (j == 0) {
        a = v[0]; b = v[15]; ka = kz[0]; kb = kz[15];
      } else if constexpr (j < 4) {
        a = v[j]; ka = kz[j];
        b = csel(sp, v[8 - j], v[15 - j]);
        kb = csel(sp, kz[8 - j], kz[15 - j]);
      } else {
        a = csel(sp, v[8 + j], v[j]);
        ka = csel(sp, kz[8 + j], kz[j]);
        b = v[15 - j]; kb = kz[15 - j];
      }
      cf eu = cmk(sc * (a.x + b.x), sc * (a.y - b.y));
      cf ou = cmk(sc * (a.y + b.y), -sc * (a.x - b.x));
      cf ek = cmk(0.5f * (ka.x + kb.x), 0.5f * (ka.y - kb.y));
      cf ok = cmk(0.5f * (ka.y + kb.y), -0.5f * (ka.x - kb.x));
      cf w = tw64<8 * j, -1>((j < 4) ? L : L2);
      cf wo = cmul(ok, w);
      cf T = cmk(wo.x - ek.y, wo.y + ek.x);
      cf T2 = cmk(wo.x + ek.y, -wo.y + ek.x);
      oP[j] = cadd(cmul(eu, ka), cmul(ou, T));
      oQ[j] = cadd(cmulc(kb, eu), cmulc(T2, ou));
    });
    cf s0, s4;
    {
      float eu = 2.0f * sc * v[0].x, ou = 2.0f * sc * v[0].y;
      s0 = cmk(fmaf(eu, kz[0].x, ou * kz[0].y), fmaf(eu, kz[0].y, ou * kz[0].x));
      eu = 2.0f * sc * v[4].x; ou = 2.0f * sc * v[4].y;
      s4 = cmk(fmaf(eu, kz[4].x, -(ou * kz[4].y)), fmaf(eu, kz[4].y, ou * kz[4].x));
    }
    v[0] = csel(sp, s0, oP[0]);
    v[1] = oP[1];
    v[2] = oP[2];
    v[3] = oP[3];
    v[4] = csel(sp, s4, oP[4]);
    v[5] = csel(sp, oQ[3], oP[5]);
    v[6] = csel(sp, oQ[2], oP[6]);
    v[7] = csel(sp, oQ[1], oP[7]);
    v[8] = oQ[7];
    v[9] = oQ[6];
    v[10] = oQ[5];
    v[11] = oQ[4];
    v[12] = csel(sp, oP[4], oQ[3]);
    v[13] = csel(sp, oP[5], oQ[2]);
    v[14] = csel(sp, oP[6], oQ[1]);
    v[15] = csel(sp, oP[7], oQ[0]);
  }
  fft2048_inv_first(v, buf, f);
  fft_reg<16, 1>(v);
  u32* yr = yT + (size_t)d * (SEQ / 2);
#pragma unroll
  for (int a = 0; a < 8; ++a) yr[128 * a + t] = f2bf(v[a].x) | (f2bf(v[a].y) << 16);
}

__device__ __forceinline__ void utrans_block(const u16* __restrict__ u, u16* __restrict__ uT, u16* tile, int bid, int tid) {
#pragma unroll
  for (int it = 0; it < 4; ++it) {
    int tl = bid * 4 + it;
    int l0 = (tl >> 4) * 64, d0 = (tl & 15) * 64;
    int r = tid >> 3, c = tid & 7;
    uint4 q = *reinterpret_cast<const uint4*>(u + (size_t)(l0 + r) * DCH + d0 + 8 * c);
    u32 w[4] = {q.x, q.y, q.z, q.w};
    if (it) __syncthreads();
#pragma unroll
    for (int j = 0; j < 4; ++j) {
      tile[(8 * c + 2 * j) * 72 + r] = (u16)(w[j] & 0xffffu);
      tile[(8 * c + 2 * j + 1) * 72 + r] = (u16)(w[j] >> 16);
    }
    __syncthreads();
    uint4 o = *reinterpret_cast<const uint4*>(tile + r * 72 + 8 * c);
    *reinterpret_cast<uint4*>(uT + (size_t)(d0 + r) * SEQ + l0 + 8 * c) = o;
  }
}

__device__ __forceinline__ void ytrans_block(const u16* __restrict__ yT, const u16* __restrict__ u,
                                             const u16* __restrict__ Dskip, u16* __restrict__ out, u16* tile,
                                             int bid, int tid) {
#pragma unroll
  for (int it = 0; it < 4; ++it) {
    int tl = bid * 4 + it;
    int l0 = (tl >> 4) * 64, d0 = (tl & 15) * 64;
    int r = tid >> 3, c = tid & 7;
    uint4 q = *reinterpret_cast<const uint4*>(yT + (size_t)(d0 + r) * SEQ + l0 + 8 * c);
    uint4 uq = *reinterpret_cast<const uint4*>(u + (size_t)(l0 + r) * DCH + d0 + 8 * c);
    uint4 dq = *reinterpret_cast<const uint4*>(Dskip + d0 + 8 * c);
    u32 w[4] = {q.x, q.y, q.z, q.w};
    if (it) __syncthreads();
#pragma unroll
    for (int j = 0; j < 4; ++j) {
      tile[(8 * c + 2 * j) * 72 + r] = (u16)(w[j] & 0xffffu);
      tile[(8 * c + 2 * j + 1) * 72 + r] = (u16)(w[j] >> 16);
    }
    __syncthreads();
    uint4 y = *reinterpret_cast<const uint4*>(tile + r * 72 + 8 * c);
    u32 yy[4] = {y.x, y.y, y.z, y.w}, uu[4] = {uq.x, uq.y, uq.z, uq.w}, dd[4] = {dq.x, dq.y, dq.z, dq.w};
    u32 oo[4];
#pragma unroll
    for (int j = 0; j < 4; ++j) {
      float o0 = bf2f(yy[j] & 0xffffu) + bfround(bf2f(uu[j] & 0xffffu) * bf2f(dd[j] & 0xffffu));
      float o1 = bf2f(yy[j] >> 16) + bfround(bf2f(uu[j] >> 16) * bf2f(dd[j] >> 16));
      oo[j] = f2bf(o0) | (f2bf(o1) << 16);
    }
    *reinterpret_cast<uint4*>(out + (size_t)(l0 + r) * (2 * DCH) + d0 + 8 * c) = make_uint4(oo[0], oo[1], oo[2], oo[3]);
  }
}

#define SM_CF (TWOFF + 260)

__global__ __launch_bounds__(512, 2) void k1(KParams P, float* Kc, const u16* x, cf* X1T, const u16* wr,
                                            const u16* wi, u32* WT, cf* TW, const u16* u, u16* uT) {
  __shared__ __attribute__((aligned(16))) cf sm[SM_CF];
  int b = blockIdx.x, tid = threadIdx.x;
  if (b < 384) {
    int q = b / 3, r = b % 3;
    if (r < 2) {
      int wave = tid >> 6, lane = tid & 63;
      if (wave < 4) kgen_wave(P, Kc, reinterpret_cast<float*>(sm) + wave * KCH * KREC, (q * 2 + r) * 4 + wave, lane);
    } else {
      brow_block(x, X1T, sm, q, tid);
    }
  } else if (b < 384 + 160) {
    int i = b - 384;
    if (i < 4) {
      int j = i * 512 + tid;
      float s, c;
      sincospif(-(float)j / 1024.0f, &s, &c);
      TW[j] = cmk(c, s);
    }
    wt_block(wr, wi, WT, reinterpret_cast<u32*>(sm), i, tid);
  } else {
    utrans_block(u, uT, reinterpret_cast<u16*>(sm), b - 544, tid);
  }
}

__global__ __launch_bounds__(128, 3) void k2(cf* X1T, const u32* WT, const u32* uT, const float* Kc, const cf* TW,
                                            u32* yT) {
  __shared__ __attribute__((aligned(16))) cf sm[XBUF];
  int b = blockIdx.x, tid = threadIdx.x;
  int q = b / 3, r = b % 3;
  if (r == 2) {
    if (q == 0) bcol_block<true>(X1T, WT, TW, sm, q, tid);
    else bcol_block<false>(X1T, WT, TW, sm, q, tid);
  } else {
    aconv_block(uT, Kc, TW, yT, sm, q * 2 + r, tid);
  }
}

__global__ __launch_bounds__(512, 2) void k3(const cf* X1T, const u16* yT, const u16* u, const u16* Dskip, u16* out) {
  __shared__ __attribute__((aligned(16))) cf sm[SM_CF];
  int b = blockIdx.x, tid = threadIdx.x;
  if (b & 1) {
    browinv_block(X1T, out, sm, b >> 1, tid);
  } else {
    ytrans_block(yT, u, Dskip, out, reinterpret_cast<u16*>(sm), b >> 1, tid);
  }
}

extern "C" void kernel_launch(void* const* d_in, const int* in_sizes, int n_in, void* d_out, int out_size,
                              void* d_ws, size_t ws_size, hipStream_t stream) {
  const u16* u = (const u16*)d_in[0];
  KParams P;
  P.Are = (const u16*)d_in[1];
  P.Aim = (const u16*)d_in[2];
  P.Bre = (const u16*)d_in[3];
  P.Bim = (const u16*)d_in[4];
  P.Cre = (const u16*)d_in[5];
  P.Cim = (const u16*)d_in[6];
  P.logdt = (const u16*)d_in[7];
  const u16* Dskip = (const u16*)d_in[8];
  const u16* x = (const u16*)d_in[9];
  const u16* wr = (const u16*)d_in[10];
  const u16* wi = (const u16*)d_in[11];
  u16* out = (u16*)d_out;
  char* ws = (char*)d_ws;
  cf* X1T = (cf*)ws;
  u32* WT = (u32*)(ws + (size_t)8 * 1024 * 1024);
  float* Kc = (float*)(ws + (size_t)16 * 1024 * 1024);
  cf* TW = (cf*)(ws + (size_t)20 * 1024 * 1024);
  u16* uT = (u16*)(ws + (size_t)24 * 1024 * 1024);
  u16* yT = (u16*)(ws + (size_t)28 * 1024 * 1024);

  k1<<<384 + 160 + 128, 512, 0, stream>>>(P, Kc, x, X1T, wr, wi, WT, TW, u, uT);
  k2<<<1536, 128, 0, stream>>>(X1T, WT, (const u32*)uT, Kc, TW, (u32*)yT);
  k3<<<256, 512, 0, stream>>>(X1T, yT, u, Dskip, out);
}
```

```cpp
#include <hip/hip_runtime.h>
#include <hip/hip_bf16.h>
#include <type_traits>

#define SEQ 2048
#define DCH 1024
#define NST 128
#define DF 513

typedef __attribute__((ext_vector_type(2))) float cf;
typedef unsigned int u32;
typedef unsigned short u16;
using f32x16 = __attribute__((ext_vector_type(16))) float;

template <int I, int N, class F>
__device__ __forceinline__ void static_for(F&& f) {
  if constexpr (I < N) {
    f(std::integral_constant<int, I>{});
    static_for<I + 1, N>(f);
  }
}

__device__ __forceinline__ cf cmk(float x, float y) { cf r; r.x = x; r.y = y; return r; }
__device__ __forceinline__ cf cadd(cf a, cf b) { return a + b; }
__device__ __forceinline__ cf csub(cf a, cf b) { return a - b; }
__device__ __forceinline__ cf add_pi(cf a, cf b) {
  cf r;
  asm("v_pk_add_f32 %0, %1, %2 op_sel:[0,1] op_sel_hi:[1,0] neg_lo:[0,1]" : "=v"(r) : "v"(a), "v"(b));
  return r;
}
__device__ __forceinline__ cf add_mi(cf a, cf b) {
  cf r;
  asm("v_pk_add_f32 %0, %1, %2 op_sel:[0,1] op_sel_hi:[1,0] neg_hi:[0,1]" : "=v"(r) : "v"(a), "v"(b));
  return r;
}
__device__ __forceinline__ cf cmul(cf a, cf b) {
  cf t, r;
  asm("v_pk_mul_f32 %0, %1, %2 op_sel:[0,0] op_sel_hi:[0,1]" : "=v"(t) : "v"(a), "v"(b));
  asm("v_pk_fma_f32 %0, %1, %2, %3 op_sel:[1,1,0] op_sel_hi:[1,0,1] neg_lo:[0,1,0]" : "=v"(r) : "v"(a), "v"(b), "v"(t));
  return r;
}
__device__ __forceinline__ cf cmad(cf a, cf b, cf c) {
  cf t, r;
  asm("v_pk_fma_f32 %0, %1, %2, %3 op_sel:[0,0,0] op_sel_hi:[0,1,1]" : "=v"(t) : "v"(a), "v"(b), "v"(c));
  asm("v_pk_fma_f32 %0, %1, %2, %3 op_sel:[1,1,0] op_sel_hi:[1,0,1] neg_lo:[0,1,0]" : "=v"(r) : "v"(a), "v"(b), "v"(t));
  return r;
}
__device__ __forceinline__ cf cmulc(cf a, cf b) {
  cf t, r;
  asm("v_pk_mul_f32 %0, %1, %2 op_sel:[0,0] op_sel_hi:[1,0]" : "=v"(t) : "v"(a), "v"(b));
  asm("v_pk_fma_f32 %0, %1, %2, %3 op_sel:[1,1,0] op_sel_hi:[0,1,1] neg_hi:[1,0,0]" : "=v"(r) : "v"(a), "v"(b), "v"(t));
  return r;
}
__device__ __forceinline__ cf cmadc(cf a, cf b, cf c) {
  cf t, r;
  asm("v_pk_fma_f32 %0, %1, %2, %3 op_sel:[0,0,0] op_sel_hi:[1,0,1]" : "=v"(t) : "v"(a), "v"(b), "v"(c));
  asm("v_pk_fma_f32 %0, %1, %2, %3 op_sel:[1,1,0] op_sel_hi:[0,1,1] neg_hi:[1,0,0]" : "=v"(r) : "v"(a), "v"(b), "v"(t));
  return r;
}
__device__ __forceinline__ cf addc(cf a, cf b) {
  cf r;
  asm("v_pk_add_f32 %0, %1, %2 neg_hi:[0,1]" : "=v"(r) : "v"(a), "v"(b));
  return r;
}
__device__ __forceinline__ cf oddc(cf a, cf b) {
  cf r;
  asm("v_pk_add_f32 %0, %1, %2 op_sel:[1,1] op_sel_hi:[0,0] neg_hi:[1,0]" : "=v"(r) : "v"(a), "v"(b));
  return r;
}
__device__ __forceinline__ cf conj_add_pi(cf a, cf b) {
  cf r;
  asm("v_pk_add_f32 %0, %1, %2 op_sel:[0,1] op_sel_hi:[1,0] neg_hi:[1,0]" : "=v"(r) : "v"(a), "v"(b));
  return r;
}
__device__ __forceinline__ cf cmulk(cf a, cf cs) {
  cf t, r;
  asm("v_pk_mul_f32 %0, %1, %2 op_sel:[0,0] op_sel_hi:[1,0]" : "=v"(t) : "v"(a), "s"(cs));
  asm("v_pk_fma_f32 %0, %1, %2, %3 op_sel:[1,1,0] op_sel_hi:[0,1,1] neg_lo:[1,0,0]" : "=v"(r) : "v"(a), "s"(cs), "v"(t));
  return r;
}
__device__ __forceinline__ cf csqr(cf a) { return cmk(fmaf(a.x, a.x, -(a.y * a.y)), 2.0f * a.x * a.y); }

__device__ __forceinline__ float bf2f(u32 h) { return __uint_as_float(h << 16); }
__device__ __forceinline__ u32 f2bf(float f) {
  __hip_bfloat16 b = __float2bfloat16(f);
  return (u32)(*reinterpret_cast<u16*>(&b));
}
__device__ __forceinline__ float bfround(float f) { return bf2f(f2bf(f)); }

__device__ __forceinline__ void wave_sync() {
  __builtin_amdgcn_fence(__ATOMIC_SEQ_CST, "wavefront");
  __builtin_amdgcn_wave_barrier();
  __builtin_amdgcn_fence(__ATOMIC_SEQ_CST, "wavefront");
}

constexpr double COS64_TAB[17] = {
    1.0, 0.99518472667219688624, 0.98078528040323044913, 0.95694033573220886494,
    0.92387953251128675613, 0.88192126434835502971, 0.83146961230254523708,
    0.77301045336273696081, 0.70710678118654752440, 0.63439328416364549822,
    0.55557023301960222474, 0.47139673682599764856, 0.38268343236508977173,
    0.29028467725446236764, 0.19509032201612826785, 0.09801714032956060199, 0.0};
constexpr double cos64(int j) {
  j = ((j % 64) + 64) % 64;
  if (j > 32) j = 64 - j;
  return (j <= 16) ? COS64_TAB[j] : -COS64_TAB[32 - j];
}
constexpr double sin64(int j) { return cos64(j - 16); }

template <int J, int DIR>
__device__ __forceinline__ cf tw64(cf a) {
  constexpr int j = ((J % 64) + 64) % 64;
  if constexpr (j == 0) {
    return a;
  } else if constexpr (j == 32) {
    return -a;
  } else if constexpr (j == 16) {
    return (DIR > 0) ? cmk(-a.y, a.x) : cmk(a.y, -a.x);
  } else if constexpr (j == 48) {
    return (DIR > 0) ? cmk(a.y, -a.x) : cmk(-a.y, a.x);
  } else {
    constexpr float c = (float)cos64(j);
    constexpr float s = (float)(DIR * sin64(j));
    return cmulk(a, cmk(c, s));
  }
}

template <int N, int DIR, int S>
__device__ __forceinline__ void fft_rec(const cf* in, cf* out) {
  if constexpr (N == 1) {
    out[0] = in[0];
  } else if constexpr (N == 2) {
    cf a = in[0], b = in[S];
    out[0] = cadd(a, b);
    out[1] = csub(a, b);
  } else {
    constexpr int Q = N / 4;
    cf t0[Q], t1[Q], t2[Q], t3[Q];
    fft_rec<Q, DIR, 4 * S>(in, t0);
    fft_rec<Q, DIR, 4 * S>(in + S, t1);
    fft_rec<Q, DIR, 4 * S>(in + 2 * S, t2);
    fft_rec<Q, DIR, 4 * S>(in + 3 * S, t3);
    static_for<0, Q>([&](auto ic) {
      constexpr int k = decltype(ic)::value;
      constexpr int M = 64 / N;
      cf a = t0[k];
      cf b = tw64<k * M, DIR>(t1[k]);
      cf d = tw64<3 * k * M, DIR>(t3[k]);
      cf s0, s1;
      if constexpr (2 * k * M == 16) {
        s0 = (DIR > 0) ? add_pi(a, t2[k]) : add_mi(a, t2[k]);
        s1 = (DIR > 0) ? add_mi(a, t2[k]) : add_pi(a, t2[k]);
      } else {
        cf c = tw64<2 * k * M, DIR>(t2[k]);
        s0 = cadd(a, c);
        s1 = csub(a, c);
      }
      cf s2 = cadd(b, d), s3 = csub(b, d);
      out[k] = cadd(s0, s2);
      out[k + Q] = (DIR > 0) ? add_pi(s1, s3) : add_mi(s1, s3);
      out[k + 2 * Q] = csub(s0, s2);
      out[k + 3 * Q] = (DIR > 0) ? add_mi(s1, s3) : add_pi(s1, s3);
    });
  }
}

template <int N, int DIR>
__device__ __forceinline__ void fft_reg(cf* v) {
  cf o[N];
  fft_rec<N, DIR, 1>(v, o);
#pragma unroll
  for (int i = 0; i < N; ++i) v[i] = o[i];
}

template <int DIR>
__device__ __forceinline__ void fft16_halfzero(cf* v) {
  cf a[8], b[8], oa[8], ob[8];
  static_for<0, 8>([&](auto ic) {
    constexpr int n = decltype(ic)::value;
    a[n] = v[n];
    b[n] = tw64<4 * n, DIR>(v[n]);
  });
  fft_rec<8, DIR, 1>(a, oa);
  fft_rec<8, DIR, 1>(b, ob);
#pragma unroll
  for (int k = 0; k < 8; ++k) {
    v[2 * k] = oa[k];
    v[2 * k + 1] = ob[k];
  }
}

struct Tw16 { cf w[4]; };

__device__ __forceinline__ Tw16 make_tw16(int n, int logN) {
  Tw16 b;
#pragma unroll
  for (int t = 0; t < 4; ++t) {
    int e = (n << t) & ((1 << logN) - 1);
    float s, c;
    sincospif(-2.0f * (float)e / (float)(1 << logN), &s, &c);
    b.w[t] = cmk(c, s);
  }
  return b;
}

__device__ __forceinline__ Tw16 load_tw16(const cf* __restrict__ TW, int n) {
  Tw16 b;
#pragma unroll
  for (int t = 0; t < 4; ++t) b.w[t] = TW[(n << t) & 2047];
  return b;
}

template <bool CONJ>
__device__ __forceinline__ void apply_tw16(cf* v, const Tw16& b) {
  cf p[16];
  p[1] = b.w[0];
  p[2] = b.w[1];
  p[4] = b.w[2];
  p[8] = b.w[3];
  asm volatile("" : "+v"(p[1]), "+v"(p[2]), "+v"(p[4]), "+v"(p[8]));
  static_for<3, 16>([&](auto ic) {
    constexpr int k = decltype(ic)::value;
    constexpr int top = (k >= 8) ? 8 : (k >= 4) ? 4 : 2;
    if constexpr (k != top) p[k] = cmul(p[k - top], p[top]);
  });
#pragma unroll
  for (int k = 1; k < 16; ++k) v[k] = CONJ ? cmulc(v[k], p[k]) : cmul(v[k], p[k]);
}

__device__ __forceinline__ void swap16(cf& a, cf& b) {
  asm volatile("s_nop 1" : "+v"(a), "+v"(b));
  auto r0 = __builtin_amdgcn_permlane16_swap(__float_as_uint(a.x), __float_as_uint(b.x), false, false);
  auto r1 = __builtin_amdgcn_permlane16_swap(__float_as_uint(a.y), __float_as_uint(b.y), false, false);
  a.x = __uint_as_float(r0[0]);
  b.x = __uint_as_float(r0[1]);
  a.y = __uint_as_float(r1[0]);
  b.y = __uint_as_float(r1[1]);
}

#define XP 152
#define XBUF (16 * XP)
typedef volatile __attribute__((address_space(3))) cf* ldsp;
__device__ __forceinline__ cf lds_ld(const cf* p) { return *(ldsp)(p); }
__device__ __forceinline__ void lds_st(cf* p, cf a) { *(ldsp)(p) = a; }

struct FCtx {
  Tw16 ta, tb;
  int t, offA, offB, kA, kB;
};
__device__ __forceinline__ FCtx make_fctx(const cf* __restrict__ TW, int t) {
  FCtx f;
  int k0 = t >> 3, c = t & 7;
  f.t = t;
  f.ta = load_tw16(TW, t);
  f.tb = load_tw16(TW, 16 * c);
  int k0m = (16 - k0) & 15;
  int k1m = (k0 != 0) ? (15 - c) : ((c != 0) ? (16 - c) : 8);
  f.offA = k0 * XP + 9 * c;
  f.offB = k0m * XP + 9 * k1m;
  f.kA = k0 + 16 * c;
  f.kB = k0m + 16 * k1m;
  return f;
}

__device__ __forceinline__ void fft2048_fwd_rest(cf* v, cf* buf, const FCtx& f) {
  apply_tw16<false>(v, f.ta);
  __syncthreads();
#pragma unroll
  for (int k0 = 0; k0 < 16; ++k0) lds_st(buf + k0 * XP + f.t, v[k0]);
  __syncthreads();
  cf* rb = buf + (f.t >> 3) * XP + (f.t & 7);
#pragma unroll
  for (int b = 0; b < 16; ++b) v[b] = lds_ld(rb + 8 * b);
  fft_reg<16, -1>(v);
  apply_tw16<false>(v, f.tb);
#pragma unroll
  for (int k1 = 0; k1 < 16; ++k1) lds_st(rb + 9 * k1, v[k1]);
  __syncthreads();
#pragma unroll
  for (int j = 0; j < 8; ++j) v[j] = lds_ld(buf + f.offA + j);
#pragma unroll
  for (int j = 0; j < 8; ++j) v[8 + j] = lds_ld(buf + f.offB + j);
  fft_reg<8, -1>(v);
  fft_reg<8, -1>(v + 8);
}

__device__ __forceinline__ void fft2048_inv_first(cf* v, cf* buf, const FCtx& f) {
  fft_reg<8, 1>(v);
  fft_reg<8, 1>(v + 8);
  __syncthreads();
#pragma unroll
  for (int j = 0; j < 8; ++j) lds_st(buf + f.offA + j, v[j]);
#pragma unroll
  for (int j = 0; j < 8; ++j) lds_st(buf + f.offB + j, v[8 + j]);
  __syncthreads();
  cf* rb = buf + (f.t >> 3) * XP + (f.t & 7);
#pragma unroll
  for (int k1 = 0; k1 < 16; ++k1) v[k1] = lds_ld(rb + 9 * k1);
  apply_tw16<true>(v, f.tb);
  fft_reg<16, 1>(v);
#pragma unroll
  for (int b = 0; b < 16; ++b) lds_st(rb + 8 * b, v[b]);
  __syncthreads();
#pragma unroll
  for (int kk = 0; kk < 16; ++kk) v[kk] = lds_ld(buf + kk * XP + f.t);
  apply_tw16<true>(v, f.ta);
}

#define KC_PITCH 648
#define KREC 132
#define KCH 32

__device__ __forceinline__ cf cexp_scaled(float ar, float ai, float sc) {
  float e = expf(ar * sc);
  float s, c;
  sincosf(ai * sc, &s, &c);
  return cmk(e * c, e * s);
}

struct KParams { const u16 *Are, *Aim, *Bre, *Bim, *Cre, *Cim, *logdt; };

__device__ __forceinline__ void kgen_wave(const KParams& P, float* __restrict__ Kc, float* lds, int d, int lane) {
  float dt = bfround(expf(bf2f(P.logdt[d])));
  f32x16 acc;
#pragma unroll
  for (int i = 0; i < 16; ++i) acc[i] = 0.f;
  for (int round = 0; round < 2; ++round) {
    int n = round * 64 + lane;
    int idx = d * NST + n;
    float ar = dt * bf2f(P.Are[idx]), ai = dt * bf2f(P.Aim[idx]);
    cf dB = cmk(dt * bf2f(P.Bre[idx]), dt * bf2f(P.Bim[idx]));
    cf C = cmk(bf2f(P.Cre[idx]), bf2f(P.Cim[idx]));
    cf c0 = cmul(C, dB);
    cf e[12];
    e[0] = cexp_scaled(ar, ai, 1.0f);
    e[4] = cexp_scaled(ar, ai, 16.0f);
    e[8] = cexp_scaled(ar, ai, 256.0f);
    e[1] = csqr(e[0]); e[2] = csqr(e[1]); e[3] = csqr(e[2]);
    e[5] = csqr(e[4]); e[6] = csqr(e[5]); e[7] = csqr(e[6]);
    e[9] = csqr(e[8]); e[10] = csqr(e[9]); e[11] = csqr(e[10]);
    cf G[32], E[32];
    G[0] = c0;
    G[1] = cmul(c0, e[0]);
    G[2] = cmul(c0, e[1]);
    G[3] = cmul(G[1], e[1]);
    G[4] = cmul(c0, e[2]);
    G[5] = cmul(G[1], e[2]);
    G[6] = cmul(G[2], e[2]);
    G[7] = cmul(G[3], e[2]);
    G[8] = cmul(c0, e[8]);
    G[9] = cmul(G[2], e[8]);
    G[10] = cmul(G[4], e[8]);
    G[11] = cmul(G[6], e[8]);
    G[12] = cmul(c0, e[9]);
    G[13] = cmul(G[4], e[9]);
    G[14] = cmul(G[8], e[9]);
    G[15] = cmul(G[10], e[9]);
    G[16] = cmul(c0, e[10]);
    G[17] = cmul(G[8], e[10]);
    G[18] = cmul(G[12], e[10]);
    G[19] = cmul(G[14], e[10]);
    G[20] = cmul(c0, e[11]);
#pragma unroll
    for (int i = 21; i < 32; ++i) G[i] = cmk(0.f, 0.f);
    E[0] = cmk(1.f, 0.f);
    E[1] = e[3]; E[2] = e[4]; E[4] = e[5]; E[8] = e[6]; E[16] = e[7];
    static_for<3, 32>([&](auto ic) {
      constexpr int k = decltype(ic)::value;
      constexpr int top = (k >= 16) ? 16 : (k >= 8) ? 8 : (k >= 4) ? 4 : 2;
      if constexpr (k != top) E[k] = cmul(E[k - top], E[top]);
    });
    for (int ch = 0; ch < 64 / KCH; ++ch) {
      if ((lane / KCH) == ch) {
        float4* rec = reinterpret_cast<float4*>(lds + (lane % KCH) * KREC);
#pragma unroll
        for (int l2 = 0; l2 < 16; ++l2) {
          rec[l2] = make_float4(G[2 * l2].x, E[2 * l2].x, G[2 * l2 + 1].x, E[2 * l2 + 1].x);
          rec[16 + l2] = make_float4(-G[2 * l2].y, E[2 * l2].y, -G[2 * l2 + 1].y, E[2 * l2 + 1].y);
        }
      }
      wave_sync();
#pragma unroll
      for (int nn = 0; nn < KCH; ++nn) {
        float2 ab = *reinterpret_cast<const float2*>(lds + nn * KREC + 2 * lane);
        acc = __builtin_amdgcn_mfma_f32_32x32x2f32(ab.x, ab.y, acc, 0, 0, 0);
      }
      wave_sync();
    }
  }
  int col = lane & 31, hh = lane >> 5;
  float* dst = Kc + (size_t)d * KC_PITCH;
#pragma unroll
  for (int r = 0; r < 16; ++r) {
    int row = (r & 3) + 8 * (r >> 2) + 4 * hh;
    int p = -1;
    if (row < 8) p = row + 8 * col;
    else if (row < 12) p = 256 + (row - 8) + 4 * col;
    else if (row < 16) p = 384 + ((row - 12) & 1) + 64 * ((row - 12) >> 1) + 2 * col;
    else if (row < 20) p = 512 + 32 * (row - 16) + col;
    else if (row == 20 && col == 0) p = 640;
    if (p >= 0) dst[p] = acc[r];
  }
}

__device__ __forceinline__ void wt_block(const u16* __restrict__ wr, const u16* __restrict__ wi, u32* __restrict__ WT,
                                         u32* tile, int bid, int tid) {
  int bk = bid % 5, bs = bid / 5;
  int k0 = bk * 128, s0 = bs * 64;
  int tx = tid & 127, ty = tid >> 7;
  u32 val[16];
#pragma unroll
  for (int i = 0; i < 16; ++i) {
    int r = ty + 4 * i;
    int k = k0 + tx, s = s0 + r;
    val[i] = 0;
    if (k < DF) val[i] = (u32)wr[s * DF + k] | ((u32)wi[s * DF + k] << 16);
  }
#pragma unroll
  for (int i = 0; i < 16; ++i) tile[(ty + 4 * i) * 129 + tx] = val[i];
  __syncthreads();
  int sx = tid & 63, ky = tid >> 6;
#pragma unroll
  for (int i = 0; i < 16; ++i) {
    int kk = ky + 8 * i;
    int k = k0 + kk;
    if (k < DF) WT[(size_t)k * SEQ + s0 + sx] = tile[sx * 129 + kk];
  }
}

#define RP 34
#define RRS (16 * RP)
#define ZP 517
#define TWOFF (16 * RRS)

__device__ __forceinline__ void brow_block(const u16* __restrict__ x, cf* __restrict__ X1T, cf* sm, int bid, int tid) {
  cf* tw = sm + TWOFF;
  int s0 = bid * 16;
  if (tid <= 256) {
    float s, c;
    sincospif(-(float)tid / 512.0f, &s, &c);
    tw[tid] = cmk(c, s);
  }
  int row = tid >> 5, t = tid & 31;
  cf v[16];
  const u32* xr = reinterpret_cast<const u32*>(x + (size_t)(s0 + row) * DCH);
#pragma unroll
  for (int a = 0; a < 16; ++a) {
    u32 w = xr[32 * a + t];
    v[a] = cmk(bf2f(w & 0xffffu), bf2f(w >> 16));
  }
  Tw16 tb = make_tw16(t, 9);
  fft_reg<16, -1>(v);
  apply_tw16<false>(v, tb);
  cf* rb = sm + row * RRS;
  {
    int pos = (t & 1) * 16 + (t >> 1);
#pragma unroll
    for (int k0 = 0; k0 < 16; ++k0) rb[k0 * RP + pos] = v[k0];
  }
  wave_sync();
  int k0 = t & 15, h = t >> 4;
  {
    const float4* p = reinterpret_cast<const float4*>(rb + k0 * RP + h * 16);
#pragma unroll
    for (int m = 0; m < 8; ++m) {
      float4 q = p[m];
      v[2 * m] = cmk(q.x, q.y);
      v[2 * m + 1] = cmk(q.z, q.w);
    }
  }
  fft_reg<16, -1>(v);
  bool hi = h != 0;
  static_for<0, 8>([&](auto ic) {
    constexpr int j = decltype(ic)::value;
    cf a = v[j], b = v[j + 8];
    swap16(a, b);
    cf br = cmk(hi ? b.y : b.x, hi ? -b.x : b.y);
    br = tw64<2 * j, -1>(br);
    v[j] = cadd(a, br);
    v[j + 8] = csub(a, br);
  });
  __syncthreads();
  cf* zs = sm + row * ZP;
#pragma unroll
  for (int r = 0; r < 16; ++r) zs[k0 + 16 * ((r & 7) + 8 * h + 16 * (r >> 3))] = v[r];
  __syncthreads();
  int r = tid & 15, kk = tid >> 4;
  const cf* zr = sm + r * ZP;
#pragma unroll
  for (int i = 0; i < 9; ++i) {
    int k = kk + 32 * i;
    if (k > 256) break;
    cf a = zr[k];
    cf b = zr[(512 - k) & 511];
    if (k == 0) {
      X1T[(size_t)0 * SEQ + s0 + r] = cmk(a.x + a.y, a.x - a.y);
    } else {
      cf w = tw[k];
      cf ev = cmk(0.5f * (a.x + b.x), 0.5f * (a.y - b.y));
      cf od = cmk(0.5f * (a.y + b.y), -0.5f * (a.x - b.x));
      cf wo = cmul(od, w);
      X1T[(size_t)k * SEQ + s0 + r] = cadd(ev, wo);
      if (k != 256) X1T[(size_t)(512 - k) * SEQ + s0 + r] = cmk(ev.x - wo.x, -(ev.y - wo.y));
    }
  }
}

__device__ __forceinline__ void browinv_block(const cf* __restrict__ X1T, u16* __restrict__ out, cf* sm, int bid, int tid) {
  cf* tw = sm + TWOFF;
  int s0 = bid * 16;
  {
    int r = tid & 15, kk = tid >> 4;
    cf a[9], b[9];
#pragma unroll
    for (int i = 0; i < 9; ++i) {
      int k = kk + 32 * i;
      if (k <= 256) {
        a[i] = X1T[(size_t)k * SEQ + s0 + r];
        b[i] = X1T[(size_t)((512 - k) & 511) * SEQ + s0 + r];
      }
    }
    if (tid <= 256) {
      float s, c;
      sincospif(-(float)tid / 512.0f, &s, &c);
      tw[tid] = cmk(c, s);
    }
    __syncthreads();
    cf* zr = sm + r * ZP;
#pragma unroll
    for (int i = 0; i < 9; ++i) {
      int k = kk + 32 * i;
      if (k <= 256) {
        if (k == 0) {
          zr[0] = cmk(a[i].x + a[i].y, a[i].x - a[i].y);
        } else {
          cf w = tw[k];
          cf ev = cmk(a[i].x + b[i].x, a[i].y - b[i].y);
          cf df = cmk(a[i].x - b[i].x, a[i].y + b[i].y);
          cf od = cmulc(df, w);
          zr[k] = cmk(ev.x - od.y, ev.y + od.x);
          if (k != 256) zr[512 - k] = cmk(ev.x + od.y, -ev.y + od.x);
        }
      }
    }
  }
  __syncthreads();
  int row = tid >> 5, t = tid & 31;
  int k0 = t & 15, h = t >> 4;
  cf v[16];
  {
    const cf* zs = sm + row * ZP;
#pragma unroll
    for (int r = 0; r < 16; ++r) v[r] = zs[k0 + 16 * ((r & 7) + 8 * h + 16 * (r >> 3))];
  }
  __syncthreads();
  bool hi = h != 0;
  static_for<0, 8>([&](auto ic) {
    constexpr int j = decltype(ic)::value;
    cf a = cadd(v[j], v[j + 8]);
    cf d = csub(v[j], v[j + 8]);
    cf dr = cmk(hi ? -d.y : d.x, hi ? d.x : d.y);
    dr = tw64<2 * j, 1>(dr);
    swap16(a, dr);
    v[j] = a;
    v[j + 8] = dr;
  });
  fft_reg<16, 1>(v);
  cf* rb = sm + row * RRS;
  {
    float4* p = reinterpret_cast<float4*>(rb + k0 * RP + h * 16);
#pragma unroll
    for (int m = 0; m < 8; ++m) p[m] = make_float4(v[2 * m].x, v[2 * m].y, v[2 * m + 1].x, v[2 * m + 1].y);
  }
  wave_sync();
  {
    int pos = (t & 1) * 16 + (t >> 1);
#pragma unroll
    for (int kk = 0; kk < 16; ++kk) v[kk] = rb[kk * RP + pos];
  }
  Tw16 tb = make_tw16(t, 9);
  apply_tw16<true>(v, tb);
  fft_reg<16, 1>(v);
  u32* orow = reinterpret_cast<u32*>(out + (size_t)(s0 + row) * (2 * DCH) + DCH);
#pragma unroll
  for (int a = 0; a < 16; ++a) orow[32 * a + t] = f2bf(v[a].x) | (f2bf(v[a].y) << 16);
}

__device__ __forceinline__ cf ldw(const u32* p, int i) {
  u32 w = p[i];
  return cmk(bf2f(w & 0xffffu), bf2f(w >> 16));
}

template <bool SPECIAL>
__device__ __forceinline__ void bcol_block(cf* __restrict__ X1T, const u32* __restrict__ WT, const cf* __restrict__ TW,
                                           cf* buf, int col, int t) {
  cf v[16];
  cf* xc = X1T + (size_t)col * SEQ;
#pragma unroll
  for (int a = 0; a < 16; ++a) v[a] = xc[128 * a + t];
  FCtx f = make_fctx(TW, t);
  const float sc = 1.0f / (2048.0f * 1024.0f);
  const u32* wc = WT + (size_t)col * SEQ;
  u32 wq[16];
  if (!SPECIAL) {
#pragma unroll
    for (int j = 0; j < 8; ++j) {
      wq[j] = wc[f.kA + 256 * j];
      wq[8 + j] = wc[f.kB + 256 * j];
    }
  }
  fft_reg<16, -1>(v);
  fft2048_fwd_rest(v, buf, f);
  if (!SPECIAL) {
#pragma unroll
    for (int r = 0; r < 16; ++r) {
      cf w = cmk(bf2f(wq[r] & 0xffffu) * sc, bf2f(wq[r] >> 16) * sc);
      v[r] = cmul(v[r], w);
    }
  } else {
    __syncthreads();
#pragma unroll
    for (int j = 0; j < 8; ++j) {
      buf[f.kA + 256 * j] = v[j];
      buf[f.kB + 256 * j] = v[8 + j];
    }
    __syncthreads();
    const u32* w0 = WT;
    const u32* w1 = WT + (size_t)512 * SEQ;
#pragma unroll 1
    for (int k = t; k <= 1024; k += 128) {
      int km = (2048 - k) & 2047;
      cf a = buf[k], b = buf[km];
      cf A = cmk(0.5f * (a.x + b.x), 0.5f * (a.y - b.y));
      cf B = cmk(0.5f * (a.y + b.y), -0.5f * (a.x - b.x));
      cf p = ldw(w0, k), q = ldw(w0, km);
      cf wh0 = cmk(0.5f * sc * (p.x + q.x), 0.5f * sc * (p.y - q.y));
      p = ldw(w1, k); q = ldw(w1, km);
      cf wh1 = cmk(0.5f * sc * (p.x + q.x), 0.5f * sc * (p.y - q.y));
      cf h0 = cmul(A, wh0), h1 = cmul(B, wh1);
      buf[km] = cmk(h0.x + h1.y, -h0.y + h1.x);
      buf[k] = cmk(h0.x - h1.y, h0.y + h1.x);
    }
    __syncthreads();
#pragma unroll
    for (int j = 0; j < 8; ++j) {
      v[j] = buf[f.kA + 256 * j];
      v[8 + j] = buf[f.kB + 256 * j];
    }
  }
  fft2048_inv_first(v, buf, f);
  fft_reg<16, 1>(v);
#pragma unroll
  for (int a = 0; a < 16; ++a) xc[128 * a + t] = v[a];
}

__device__ __forceinline__ int kpos(int l, int s) {
  if (s == 0) return l;
  int half = 1 << (s - 1);
  int q = (l + half - 1 + ((l >> s) & 1)) >> s;
  return 128 * (s + 1) + q - 128;
}
__device__ __forceinline__ cf csel(bool c, cf a, cf b) { return cmk(c ? a.x : b.x, c ? a.y : b.y); }

__device__ __forceinline__ void aconv_block(const u32* __restrict__ uT, const float* __restrict__ Kc,
                                            const cf* __restrict__ TW, u32* __restrict__ yT, cf* buf, int d, int t) {
  cf v[16], kz[16];
  u32 uw[8];
  {
    const float* kl = Kc + (size_t)d * KC_PITCH;
    static_for<0, 8>([&](auto ic) {
      constexpr int a = decltype(ic)::value;
      constexpr int s = (a < 1) ? 0 : (a < 2) ? 1 : (a < 4) ? 2 : 3;
      int l = 256 * a + 2 * t;
      kz[a] = cmk(kl[kpos(l, s)], kl[kpos(l + 1, s)]);
    });
    const u32* ur = uT + (size_t)d * (SEQ / 2);
#pragma unroll
    for (int a = 0; a < 8; ++a) uw[a] = ur[128 * a + t];
  }
  FCtx f = make_fctx(TW, t);
  cf L = TW[f.kA];
  cf L2 = TW[t == 0 ? 128 : f.kA];
  fft16_halfzero<-1>(kz);
  fft2048_fwd_rest(kz, buf, f);
#pragma unroll
  for (int a = 0; a < 8; ++a) v[a] = cmk(bf2f(uw[a] & 0xffffu), bf2f(uw[a] >> 16)) * (0.5f / 2048.0f);
  fft16_halfzero<-1>(v);
  fft2048_fwd_rest(v, buf, f);
  {
    const bool sp = (t == 0);
    cf oP[8], oQ[8];
    static_for<0, 8>([&](auto ic) {
      constexpr int j = decltype(ic)::value;
      cf a, b, ka, kb;
      if constexpr (j == 0) {
        a = v[0]; b = v[15]; ka = kz[0]; kb = kz[15];
      } else if constexpr (j < 4) {
        a = v[j]; ka = kz[j];
        b = csel(sp, v[8 - j], v[15 - j]);
        kb = csel(sp, kz[8 - j], kz[15 - j]);
      } else {
        a = csel(sp, v[8 + j], v[j]);
        ka = csel(sp, kz[8 + j], kz[j]);
        b = v[15 - j]; kb = kz[15 - j];
      }
      cf eu = addc(a, b);
      cf ou = oddc(a, b) * 0.5f;
      cf ek = addc(ka, kb);
      cf ok = oddc(ka, kb);
      cf w = tw64<8 * j, -1>((j < 4) ? L : L2);
      cf wo = cmul(ok, w);
      cf T = add_pi(wo, ek);
      cf T2 = conj_add_pi(wo, ek);
      oP[j] = cmad(ou, T, cmul(eu, ka));
      oQ[j] = cmadc(T2, ou, cmulc(kb, eu));
    });
    cf s0, s4;
    {
      float eu = 2.0f * v[0].x, ou = 2.0f * v[0].y;
      s0 = cmk(fmaf(eu, kz[0].x, ou * kz[0].y), fmaf(eu, kz[0].y, ou * kz[0].x));
      eu = 2.0f * v[4].x; ou = 2.0f * v[4].y;
      s4 = cmk(fmaf(eu, kz[4].x, -(ou * kz[4].y)), fmaf(eu, kz[4].y, ou * kz[4].x));
    }
    v[0] = csel(sp, s0, oP[0]);
    v[1] = oP[1];
    v[2] = oP[2];
    v[3] = oP[3];
    v[4] = csel(sp, s4, oP[4]);
    v[5] = csel(sp, oQ[3], oP[5]);
    v[6] = csel(sp, oQ[2], oP[6]);
    v[7] = csel(sp, oQ[1], oP[7]);
    v[8] = oQ[7];
    v[9] = oQ[6];
    v[10] = oQ[5];
    v[11] = oQ[4];
    v[12] = csel(sp, oP[4], oQ[3]);
    v[13] = csel(sp, oP[5], oQ[2]);
    v[14] = csel(sp, oP[6], oQ[1]);
    v[15] = csel(sp, oP[7], oQ[0]);
  }
  fft2048_inv_first(v, buf, f);
  fft_reg<16, 1>(v);
  u32* yr = yT + (size_t)d * (SEQ / 2);
#pragma unroll
  for (int a = 0; a < 8; ++a) yr[128 * a + t] = f2bf(v[a].x) | (f2bf(v[a].y) << 16);
}

__device__ __forceinline__ void utrans_block(const u16* __restrict__ u, u16* __restrict__ uT, u16* tile, int bid, int tid) {
#pragma unroll
  for (int it = 0; it < 4; ++it) {
    int tl = bid * 4 + it;
    int l0 = (tl >> 4) * 64, d0 = (tl & 15) * 64;
    int r = tid >> 3, c = tid & 7;
    uint4 q = *reinterpret_cast<const uint4*>(u + (size_t)(l0 + r) * DCH + d0 + 8 * c);
    u32 w[4] = {q.x, q.y, q.z, q.w};
    if (it) __syncthreads();
#pragma unroll
    for (int j = 0; j < 4; ++j) {
      tile[(8 * c + 2 * j) * 72 + r] = (u16)(w[j] & 0xffffu);
      tile[(8 * c + 2 * j + 1) * 72 + r] = (u16)(w[j] >> 16);
    }
    __syncthreads();
    uint4 o = *reinterpret_cast<const uint4*>(tile + r * 72 + 8 * c);
    *reinterpret_cast<uint4*>(uT + (size_t)(d0 + r) * SEQ + l0 + 8 * c) = o;
  }
}

__device__ __forceinline__ void ytrans_block(const u16* __restrict__ yT, const u16* __restrict__ u,
                                             const u16* __restrict__ Dskip, u16* __restrict__ out, u16* tile,
                                             int bid, int tid) {
#pragma unroll
  for (int it = 0; it < 4; ++it) {
    int tl = bid * 4 + it;
    int l0 = (tl >> 4) * 64, d0 = (tl & 15) * 64;
    int r = tid >> 3, c = tid & 7;
    uint4 q = *reinterpret_cast<const uint4*>(yT + (size_t)(d0 + r) * SEQ + l0 + 8 * c);
    uint4 uq = *reinterpret_cast<const uint4*>(u + (size_t)(l0 + r) * DCH + d0 + 8 * c);
    uint4 dq = *reinterpret_cast<const uint4*>(Dskip + d0 + 8 * c);
    u32 w[4] = {q.x, q.y, q.z, q.w};
    if (it) __syncthreads();
#pragma unroll
    for (int j = 0; j < 4; ++j) {
      tile[(8 * c + 2 * j) * 72 + r] = (u16)(w[j] & 0xffffu);
      tile[(8 * c + 2 * j + 1) * 72 + r] = (u16)(w[j] >> 16);
    }
    __syncthreads();
    uint4 y = *reinterpret_cast<const uint4*>(tile + r * 72 + 8 * c);
    u32 yy[4] = {y.x, y.y, y.z, y.w}, uu[4] = {uq.x, uq.y, uq.z, uq.w}, dd[4] = {dq.x, dq.y, dq.z, dq.w};
    u32 oo[4];
#pragma unroll
    for (int j = 0; j < 4; ++j) {
      float o0 = bf2f(yy[j] & 0xffffu) + bfround(bf2f(uu[j] & 0xffffu) * bf2f(dd[j] & 0xffffu));
      float o1 = bf2f(yy[j] >> 16) + bfround(bf2f(uu[j] >> 16) * bf2f(dd[j] >> 16));
      oo[j] = f2bf(o0) | (f2bf(o1) << 16);
    }
    *reinterpret_cast<uint4*>(out + (size_t)(l0 + r) * (2 * DCH) + d0 + 8 * c) = make_uint4(oo[0], oo[1], oo[2], oo[3]);
  }
}

#define SM_CF (TWOFF + 260)

__global__ __launch_bounds__(512, 2) void k1(KParams P, float* Kc, const u16* x, cf* X1T, const u16* wr,
                                            const u16* wi, u32* WT, cf* TW, const u16* u, u16* uT) {
  __shared__ __attribute__((aligned(16))) cf sm[SM_CF];
  int b = blockIdx.x, tid = threadIdx.x;
  if (b < 384) {
    int q = b / 3, r = b % 3;
    if (r < 2) {
      int wave = tid >> 6, lane = tid & 63;
      if (wave < 4) kgen_wave(P, Kc, reinterpret_cast<float*>(sm) + wave * KCH * KREC, (q * 2 + r) * 4 + wave, lane);
    } else {
      brow_block(x, X1T, sm, q, tid);
    }
  } else if (b < 384 + 160) {
    int i = b - 384;
    if (i < 4) {
      int j = i * 512 + tid;
      float s, c;
      sincospif(-(float)j / 1024.0f, &s, &c);
      TW[j] = cmk(c, s);
    }
    wt_block(wr, wi, WT, reinterpret_cast<u32*>(sm), i, tid);
  } else {
    utrans_block(u, uT, reinterpret_cast<u16*>(sm), b - 544, tid);
  }
}

__global__ __launch_bounds__(128, 3) void k2(cf* X1T, const u32* WT, const u32* uT, const float* Kc, const cf* TW,
                                            u32* yT) {
  __shared__ __attribute__((aligned(16))) cf sm[XBUF];
  int b = blockIdx.x, tid = threadIdx.x;
  int q = b / 3, r = b % 3;
  if (r == 2) {
    if (q == 0) bcol_block<true>(X1T, WT, TW, sm, q, tid);
    else bcol_block<false>(X1T, WT, TW, sm, q, tid);
  } else {
    aconv_block(uT, Kc, TW, yT, sm, q * 2 + r, tid);
  }
}

__global__ __launch_bounds__(512, 2) void k3(const cf* X1T, const u16* yT, const u16* u, const u16* Dskip, u16* out) {
  __shared__ __attribute__((aligned(16))) cf sm[SM_CF];
  int b = blockIdx.x, tid = threadIdx.x;
  if (b & 1) {
    browinv_block(X1T, out, sm, b >> 1, tid);
  } else {
    ytrans_block(yT, u, Dskip, out, reinterpret_cast<u16*>(sm), b >> 1, tid);
  }
}

extern "C" void kernel_launch(void* const* d_in, const int* in_sizes, int n_in, void* d_out, int out_size,
                              void* d_ws, size_t ws_size, hipStream_t stream) {
  const u16* u = (const u16*)d_in[0];
  KParams P;
  P.Are = (const u16*)d_in[1];
  P.Aim = (const u16*)d_in[2];
  P.Bre = (const u16*)d_in[3];
  P.Bim = (const u16*)d_in[4];
  P.Cre = (const u16*)d_in[5];
  P.Cim = (const u16*)d_in[6];
  P.logdt = (const u16*)d_in[7];
  const u16* Dskip = (const u16*)d_in[8];
  const u16* x = (const u16*)d_in[9];
  const u16* wr = (const u16*)d_in[10];
  const u16* wi = (const u16*)d_in[11];
  u16* out = (u16*)d_out;
  char* ws = (char*)d_ws;
  cf* X1T = (cf*)ws;
  u32* WT = (u32*)(ws + (size_t)8 * 1024 * 1024);
  float* Kc = (float*)(ws + (size_t)16 * 1024 * 1024);
  cf* TW = (cf*)(ws + (size_t)20 * 1024 * 1024);
  u16* uT = (u16*)(ws + (size_t)24 * 1024 * 1024);
  u16* yT = (u16*)(ws + (size_t)28 * 1024 * 1024);

  k1<<<384 + 160 + 128, 512, 0, stream>>>(P, Kc, x, X1T, wr, wi, WT, TW, u, uT);
  k2<<<1536, 128, 0, stream>>>(X1T, WT, (const u32*)uT, Kc, TW, (u32*)yT);
  k3<<<256, 512, 0, stream>>>(X1T, yT, u, Dskip, out);
}
```

```cpp
#include <hip/hip_runtime.h>
#include <hip/hip_bf16.h>
#include <type_traits>

#define SEQ 2048
#define DCH 1024
#define NST 128
#define DF 513

typedef __attribute__((ext_vector_type(2))) float cf;
typedef unsigned int u32;
typedef unsigned short u16;
using f32x16 = __attribute__((ext_vector_type(16))) float;

template <int I, int N, class F>
__device__ __forceinline__ void static_for(F&& f) {
  if constexpr (I < N) {
    f(std::integral_constant<int, I>{});
    static_for<I + 1, N>(f);
  }
}

__device__ __forceinline__ cf cmk(float x, float y) { cf r; r.x = x; r.y = y; return r; }
__device__ __forceinline__ cf cadd(cf a, cf b) { return a + b; }
__device__ __forceinline__ cf csub(cf a, cf b) { return a - b; }
__device__ __forceinline__ cf add_pi(cf a, cf b) {
  cf r;
  asm("v_pk_add_f32 %0, %1, %2 op_sel:[0,1] op_sel_hi:[1,0] neg_lo:[0,1]" : "=v"(r) : "v"(a), "v"(b));
  return r;
}
__device__ __forceinline__ cf add_mi(cf a, cf b) {
  cf r;
  asm("v_pk_add_f32 %0, %1, %2 op_sel:[0,1] op_sel_hi:[1,0] neg_hi:[0,1]" : "=v"(r) : "v"(a), "v"(b));
  return r;
}
__device__ __forceinline__ cf cmul(cf a, cf b) {
  cf t, r;
  asm("v_pk_mul_f32 %0, %1, %2 op_sel:[0,0] op_sel_hi:[0,1]" : "=v"(t) : "v"(a), "v"(b));
  asm("v_pk_fma_f32 %0, %1, %2, %3 op_sel:[1,1,0] op_sel_hi:[1,0,1] neg_lo:[0,1,0]" : "=v"(r) : "v"(a), "v"(b), "v"(t));
  return r;
}
__device__ __forceinline__ cf cmad(cf a, cf b, cf c) {
  cf t, r;
  asm("v_pk_fma_f32 %0, %1, %2, %3 op_sel:[0,0,0] op_sel_hi:[0,1,1]" : "=v"(t) : "v"(a), "v"(b), "v"(c));
  asm("v_pk_fma_f32 %0, %1, %2, %3 op_sel:[1,1,0] op_sel_hi:[1,0,1] neg_lo:[0,1,0]" : "=v"(r) : "v"(a), "v"(b), "v"(t));
  return r;
}
__device__ __forceinline__ cf cmulc(cf a, cf b) {
  cf t, r;
  asm("v_pk_mul_f32 %0, %1, %2 op_sel:[0,0] op_sel_hi:[1,0]" : "=v"(t) : "v"(a), "v"(b));
  asm("v_pk_fma_f32 %0, %1, %2, %3 op_sel:[1,1,0] op_sel_hi:[0,1,1] neg_hi:[1,0,0]" : "=v"(r) : "v"(a), "v"(b), "v"(t));
  return r;
}
__device__ __forceinline__ cf cmadc(cf a, cf b, cf c) {
  cf t, r;
  asm("v_pk_fma_f32 %0, %1, %2, %3 op_sel:[0,0,0] op_sel_hi:[1,0,1]" : "=v"(t) : "v"(a), "v"(b), "v"(c));
  asm("v_pk_fma_f32 %0, %1, %2, %3 op_sel:[1,1,0] op_sel_hi:[0,1,1] neg_hi:[1,0,0]" : "=v"(r) : "v"(a), "v"(b), "v"(t));
  return r;
}
__device__ __forceinline__ cf addc(cf a, cf b) {
  cf r;
  asm("v_pk_add_f32 %0, %1, %2 neg_hi:[0,1]" : "=v"(r) : "v"(a), "v"(b));
  return r;
}
__device__ __forceinline__ cf oddc(cf a, cf b) {
  cf r;
  asm("v_pk_add_f32 %0, %1, %2 op_sel:[1,1] op_sel_hi:[0,0] neg_hi:[1,0]" : "=v"(r) : "v"(a), "v"(b));
  return r;
}
__device__ __forceinline__ cf conj_add_pi(cf a, cf b) {
  cf r;
  asm("v_pk_add_f32 %0, %1, %2 op_sel:[0,1] op_sel_hi:[1,0] neg_hi:[1,0]" : "=v"(r) : "v"(a), "v"(b));
  return r;
}
__device__ __forceinline__ cf cmulk(cf a, cf cs) {
  cf t, r;
  asm("v_pk_mul_f32 %0, %1, %2 op_sel:[0,0] op_sel_hi:[1,0]" : "=v"(t) : "v"(a), "s"(cs));
  asm("v_pk_fma_f32 %0, %1, %2, %3 op_sel:[1,1,0] op_sel_hi:[0,1,1] neg_lo:[1,0,0]" : "=v"(r) : "v"(a), "s"(cs), "v"(t));
  return r;
}
__device__ __forceinline__ cf csqr(cf a) { return cmk(fmaf(a.x, a.x, -(a.y * a.y)), 2.0f * a.x * a.y); }

__device__ __forceinline__ float bf2f(u32 h) { return __uint_as_float(h << 16); }
__device__ __forceinline__ u32 f2bf(float f) {
  __hip_bfloat16 b = __float2bfloat16(f);
  return (u32)(*reinterpret_cast<u16*>(&b));
}
__device__ __forceinline__ float bfround(float f) { return bf2f(f2bf(f)); }

__device__ __forceinline__ void wave_sync() {
  __builtin_amdgcn_fence(__ATOMIC_SEQ_CST, "wavefront");
  __builtin_amdgcn_wave_barrier();
  __builtin_amdgcn_fence(__ATOMIC_SEQ_CST, "wavefront");
}

constexpr double COS64_TAB[17] = {
    1.0, 0.99518472667219688624, 0.98078528040323044913, 0.95694033573220886494,
    0.92387953251128675613, 0.88192126434835502971, 0.83146961230254523708,
    0.77301045336273696081, 0.70710678118654752440, 0.63439328416364549822,
    0.55557023301960222474, 0.47139673682599764856, 0.38268343236508977173,
    0.29028467725446236764, 0.19509032201612826785, 0.09801714032956060199, 0.0};
constexpr double cos64(int j) {
  j = ((j % 64) + 64) % 64;
  if (j > 32) j = 64 - j;
  return (j <= 16) ? COS64_TAB[j] : -COS64_TAB[32 - j];
}
constexpr double sin64(int j) { return cos64(j - 16); }

template <int J, int DIR>
__device__ __forceinline__ cf tw64(cf a) {
  constexpr int j = ((J % 64) + 64) % 64;
  if constexpr (j == 0) {
    return a;
  } else if constexpr (j == 32) {
    return -a;
  } else if constexpr (j == 16) {
    return (DIR > 0) ? cmk(-a.y, a.x) : cmk(a.y, -a.x);
  } else if constexpr (j == 48) {
    return (DIR > 0) ? cmk(a.y, -a.x) : cmk(-a.y, a.x);
  } else {
    constexpr float c = (float)cos64(j);
    constexpr float s = (float)(DIR * sin64(j));
    return cmulk(a, cmk(c, s));
  }
}

template <int N, int DIR, int S>
__device__ __forceinline__ void fft_rec(const cf* in, cf* out) {
  if constexpr (N == 1) {
    out[0] = in[0];
  } else if constexpr (N == 2) {
    cf a = in[0], b = in[S];
    out[0] = cadd(a, b);
    out[1] = csub(a, b);
  } else {
    constexpr int Q = N / 4;
    cf t0[Q], t1[Q], t2[Q], t3[Q];
    fft_rec<Q, DIR, 4 * S>(in, t0);
    fft_rec<Q, DIR, 4 * S>(in + S, t1);
    fft_rec<Q, DIR, 4 * S>(in + 2 * S, t2);
    fft_rec<Q, DIR, 4 * S>(in + 3 * S, t3);
    static_for<0, Q>([&](auto ic) {
      constexpr int k = decltype(ic)::value;
      constexpr int M = 64 / N;
      cf a = t0[k];
      cf b = tw64<k * M, DIR>(t1[k]);
      cf d = tw64<3 * k * M, DIR>(t3[k]);
      cf s0, s1;
      if constexpr (2 * k * M == 16) {
        s0 = (DIR > 0) ? add_pi(a, t2[k]) : add_mi(a, t2[k]);
        s1 = (DIR > 0) ? add_mi(a, t2[k]) : add_pi(a, t2[k]);
      } else {
        cf c = tw64<2 * k * M, DIR>(t2[k]);
        s0 = cadd(a, c);
        s1 = csub(a, c);
      }
      cf s2 = cadd(b, d), s3 = csub(b, d);
      out[k] = cadd(s0, s2);
      out[k + Q] = (DIR > 0) ? add_pi(s1, s3) : add_mi(s1, s3);
      out[k + 2 * Q] = csub(s0, s2);
      out[k + 3 * Q] = (DIR > 0) ? add_mi(s1, s3) : add_pi(s1, s3);
    });
  }
}

template <int N, int DIR>
__device__ __forceinline__ void fft_reg(cf* v) {
  cf o[N];
  fft_rec<N, DIR, 1>(v, o);
#pragma unroll
  for (int i = 0; i < N; ++i) v[i] = o[i];
}

template <int DIR>
__device__ __forceinline__ void fft16_halfzero(cf* v) {
  cf a[8], b[8], oa[8], ob[8];
  static_for<0, 8>([&](auto ic) {
    constexpr int n = decltype(ic)::value;
    a[n] = v[n];
    b[n] = tw64<4 * n, DIR>(v[n]);
  });
  fft_rec<8, DIR, 1>(a, oa);
  fft_rec<8, DIR, 1>(b, ob);
#pragma unroll
  for (int k = 0; k < 8; ++k) {
    v[2 * k] = oa[k];
    v[2 * k + 1] = ob[k];
  }
}

struct Tw16 { cf w[4]; };

__device__ __forceinline__ Tw16 make_tw16(int n, int logN) {
  Tw16 b;
#pragma unroll
  for (int t = 0; t < 4; ++t) {
    int e = (n << t) & ((1 << logN) - 1);
    float s, c;
    sincospif(-2.0f * (float)e / (float)(1 << logN), &s, &c);
    b.w[t] = cmk(c, s);
  }
  return b;
}

__device__ __forceinline__ Tw16 load_tw16(const cf* __restrict__ TW, int n) {
  Tw16 b;
#pragma unroll
  for (int t = 0; t < 4; ++t) b.w[t] = TW[(n << t) & 2047];
  return b;
}

template <bool CONJ>
__device__ __forceinline__ void apply_tw16(cf* v, const Tw16& b) {
  cf p[16];
  p[1] = b.w[0];
  p[2] = b.w[1];
  p[4] = b.w[2];
  p[8] = b.w[3];
  asm volatile("" : "+v"(p[1]), "+v"(p[2]), "+v"(p[4]), "+v"(p[8]));
  static_for<3, 16>([&](auto ic) {
    constexpr int k = decltype(ic)::value;
    constexpr int top = (k >= 8) ? 8 : (k >= 4) ? 4 : 2;
    if constexpr (k != top) p[k] = cmul(p[k - top], p[top]);
  });
#pragma unroll
  for (int k = 1; k < 16; ++k) v[k] = CONJ ? cmulc(v[k], p[k]) : cmul(v[k], p[k]);
}

__device__ __forceinline__ void swap16(cf& a, cf& b) {
  asm volatile("s_nop 1" : "+v"(a), "+v"(b));
  auto r0 = __builtin_amdgcn_permlane16_swap(__float_as_uint(a.x), __float_as_uint(b.x), false, false);
  auto r1 = __builtin_amdgcn_permlane16_swap(__float_as_uint(a.y), __float_as_uint(b.y), false, false);
  a.x = __uint_as_float(r0[0]);
  b.x = __uint_as_float(r0[1]);
  a.y = __uint_as_float(r1[0]);
  b.y = __uint_as_float(r1[1]);
}

#define XP 152
#define XBUF (16 * XP)
typedef volatile __attribute__((address_space(3))) cf* ldsp;
__device__ __forceinline__ cf lds_ld(const cf* p) { return *(ldsp)(p); }
__device__ __forceinline__ void lds_st(cf* p, cf a) { *(ldsp)(p) = a; }

struct FCtx {
  Tw16 ta, tb;
  int t, offA, offB, kA, kB;
};
__device__ __forceinline__ FCtx make_fctx(const cf* __restrict__ TW, int t) {
  FCtx f;
  int k0 = t >> 3, c = t & 7;
  f.t = t;
  f.ta = load_tw16(TW, t);
  f.tb = load_tw16(TW, 16 * c);
  int k0m = (16 - k0) & 15;
  int k1m = (k0 != 0) ? (15 - c) : ((c != 0) ? (16 - c) : 8);
  f.offA = k0 * XP + 9 * c;
  f.offB = k0m * XP + 9 * k1m;
  f.kA = k0 + 16 * c;
  f.kB = k0m + 16 * k1m;
  return f;
}

__device__ __forceinline__ void fft2048_fwd_rest(cf* v, cf* buf, const FCtx& f) {
  apply_tw16<false>(v, f.ta);
  __syncthreads();
#pragma unroll
  for (int k0 = 0; k0 < 16; ++k0) lds_st(buf + k0 * XP + f.t, v[k0]);
  __syncthreads();
  cf* rb = buf + (f.t >> 3) * XP + (f.t & 7);
#pragma unroll
  for (int b = 0; b < 16; ++b) v[b] = lds_ld(rb + 8 * b);
  fft_reg<16, -1>(v);
  apply_tw16<false>(v, f.tb);
#pragma unroll
  for (int k1 = 0; k1 < 16; ++k1) lds_st(rb + 9 * k1, v[k1]);
  __syncthreads();
#pragma unroll
  for (int j = 0; j < 8; ++j) v[j] = lds_ld(buf + f.offA + j);
#pragma unroll
  for (int j = 0; j < 8; ++j) v[8 + j] = lds_ld(buf + f.offB + j);
  fft_reg<8, -1>(v);
  fft_reg<8, -1>(v + 8);
}

__device__ __forceinline__ void fft2048_inv_first(cf* v, cf* buf, const FCtx& f) {
  fft_reg<8, 1>(v);
  fft_reg<8, 1>(v + 8);
  __syncthreads();
#pragma unroll
  for (int j = 0; j < 8; ++j) lds_st(buf + f.offA + j, v[j]);
#pragma unroll
  for (int j = 0; j < 8; ++j) lds_st(buf + f.offB + j, v[8 + j]);
  __syncthreads();
  cf* rb = buf + (f.t >> 3) * XP + (f.t & 7);
#pragma unroll
  for (int k1 = 0; k1 < 16; ++k1) v[k1] = lds_ld(rb + 9 * k1);
  apply_tw16<true>(v, f.tb);
  fft_reg<16, 1>(v);
#pragma unroll
  for (int b = 0; b < 16; ++b) lds_st(rb + 8 * b, v[b]);
  __syncthreads();
#pragma unroll
  for (int kk = 0; kk < 16; ++kk) v[kk] = lds_ld(buf + kk * XP + f.t);
  apply_tw16<true>(v, f.ta);
}

#define KC_PITCH 648
#define KREC 104
using u32x4 = __attribute__((ext_vector_type(4))) unsigned;
using u32x2 = __attribute__((ext_vector_type(2))) unsigned;
using bf16x8 = __attribute__((ext_vector_type(8))) __bf16;

__device__ __forceinline__ cf cexp_scaled(float ar, float ai, float sc) {
  float e = expf(ar * sc);
  float s, c;
  sincosf(ai * sc, &s, &c);
  return cmk(e * c, e * s);
}
__device__ __forceinline__ u32 cvtpk(float a, float b) {
  u32 r;
  asm("v_cvt_pk_bf16_f32 %0, %1, %2" : "=v"(r) : "v"(a), "v"(b));
  return r;
}
__device__ __forceinline__ u32x2 split2(cf g) {
  u32x2 o;
  o.x = cvtpk(g.x, g.y);
  cf h = cmk(__uint_as_float(o.x << 16), __uint_as_float(o.x & 0xffff0000u));
  cf r = g - h;
  o.y = cvtpk(r.x, r.y);
  return o;
}

struct KParams { const u16 *Are, *Aim, *Bre, *Bim, *Cre, *Cim, *logdt; };

__device__ __forceinline__ void kgen_block(const KParams& P, float* __restrict__ Kc, u32* lds_all, int bid, int tid) {
  int wave = tid >> 6, lane = tid & 63;
  int d = bid * 2 + (wave >> 1);
  u32* lds = lds_all + wave * 32 * KREC;
  float dt = bfround(expf(bf2f(P.logdt[d])));
  int idx = d * NST + (wave & 1) * 64 + lane;
  float ar = dt * bf2f(P.Are[idx]), ai = dt * bf2f(P.Aim[idx]);
  cf dB = cmk(dt * bf2f(P.Bre[idx]), dt * bf2f(P.Bim[idx]));
  cf C = cmk(bf2f(P.Cre[idx]), bf2f(P.Cim[idx]));
  cf c0 = cmul(C, dB);
  cf e[12];
  e[0] = cexp_scaled(ar, ai, 1.0f);
  e[4] = cexp_scaled(ar, ai, 16.0f);
  e[8] = cexp_scaled(ar, ai, 256.0f);
  e[1] = csqr(e[0]); e[2] = csqr(e[1]); e[3] = csqr(e[2]);
  e[5] = csqr(e[4]); e[6] = csqr(e[5]); e[7] = csqr(e[6]);
  e[9] = csqr(e[8]); e[10] = csqr(e[9]); e[11] = csqr(e[10]);
  float s2048 = fmaf(c0.x, e[11].x, -(c0.y * e[11].y));
  u32x2 Gw[20], Ew[32];
  {
    cf G[20];
    G[0] = cmk(c0.x, -c0.y);
    G[1] = cmulc(G[0], e[0]);
    G[2] = cmulc(G[0], e[1]);
    G[3] = cmulc(G[1], e[1]);
    G[4] = cmulc(G[0], e[2]);
    G[5] = cmulc(G[1], e[2]);
    G[6] = cmulc(G[2], e[2]);
    G[7] = cmulc(G[3], e[2]);
    G[8] = cmulc(G[0], e[8]);
    G[9] = cmulc(G[2], e[8]);
    G[10] = cmulc(G[4], e[8]);
    G[11] = cmulc(G[6], e[8]);
    G[12] = cmulc(G[0], e[9]);
    G[13] = cmulc(G[4], e[9]);
    G[14] = cmulc(G[8], e[9]);
    G[15] = cmulc(G[10], e[9]);
    G[16] = cmulc(G[0], e[10]);
    G[17] = cmulc(G[8], e[10]);
    G[18] = cmulc(G[12], e[10]);
    G[19] = cmulc(G[14], e[10]);
#pragma unroll
    for (int i = 0; i < 20; ++i) Gw[i] = split2(G[i]);
  }
  {
    cf E[32];
    E[0] = cmk(1.f, 0.f);
    E[1] = e[3]; E[2] = e[4]; E[4] = e[5]; E[8] = e[6]; E[16] = e[7];
    static_for<3, 32>([&](auto ic) {
      constexpr int k = decltype(ic)::value;
      constexpr int top = (k >= 16) ? 16 : (k >= 8) ? 8 : (k >= 4) ? 4 : 2;
      if constexpr (k != top) E[k] = cmul(E[k - top], E[top]);
    });
    Ew[0].x = 0x3f80u;
    Ew[0].y = 0u;
#pragma unroll
    for (int i = 1; i < 32; ++i) Ew[i] = split2(E[i]);
  }
#pragma unroll
  for (int o = 32; o >= 1; o >>= 1) s2048 += __shfl_xor(s2048, o);
  f32x16 acc;
#pragma unroll
  for (int i = 0; i < 16; ++i) acc[i] = 0.f;
  typedef volatile __attribute__((address_space(3))) u32x2* lp2;
  for (int ch = 0; ch < 2; ++ch) {
    if ((lane >> 5) == ch) {
      u32x4* rec = reinterpret_cast<u32x4*>(lds + (lane & 31) * KREC);
#pragma unroll
      for (int i = 0; i < 10; ++i) {
        u32x4 q = {Gw[2 * i].x, Gw[2 * i].y, Gw[2 * i + 1].x, Gw[2 * i + 1].y};
        rec[i] = q;
      }
#pragma unroll
      for (int i = 0; i < 16; ++i) {
        u32x4 q = {Ew[2 * i].x, Ew[2 * i].y, Ew[2 * i + 1].x, Ew[2 * i + 1].y};
        rec[10 + i] = q;
      }
    }
    wave_sync();
    const u32* base = lds + (2 * (lane >> 5)) * KREC + 2 * (lane & 31);
#pragma unroll
    for (int m = 0; m < 8; ++m) {
      const u32* r0 = base + 4 * m * KREC;
      u32x2 a0 = *(lp2)(r0), a1 = *(lp2)(r0 + KREC), b0 = *(lp2)(r0 + 40), b1 = *(lp2)(r0 + KREC + 40);
      u32x4 A = {a0.x, a0.y, a1.x, a1.y}, A2 = {a0.y, a0.x, a1.y, a1.x}, B = {b0.x, b0.y, b1.x, b1.y};
      acc = __builtin_amdgcn_mfma_f32_32x32x16_bf16(__builtin_bit_cast(bf16x8, A), __builtin_bit_cast(bf16x8, B), acc, 0, 0, 0);
      acc = __builtin_amdgcn_mfma_f32_32x32x16_bf16(__builtin_bit_cast(bf16x8, A2), __builtin_bit_cast(bf16x8, B), acc, 0, 0, 0);
    }
    wave_sync();
  }
  float* red = reinterpret_cast<float*>(lds_all + (wave | 1) * 32 * KREC);
  if (wave & 1) {
#pragma unroll
    for (int r = 0; r < 16; ++r) red[r * 64 + lane] = acc[r];
    if (lane == 0) red[1024] = s2048;
  }
  __syncthreads();
  if (!(wave & 1)) {
    int col = lane & 31, hh = lane >> 5;
    float* dst = Kc + (size_t)d * KC_PITCH;
#pragma unroll
    for (int r = 0; r < 16; ++r) {
      int row = (r & 3) + 8 * (r >> 2) + 4 * hh;
      int p = -1;
      if (row < 8) p = row + 8 * col;
      else if (row < 12) p = 256 + (row - 8) + 4 * col;
      else if (row < 16) p = 384 + ((row - 12) & 1) + 64 * ((row - 12) >> 1) + 2 * col;
      else if (row < 20) p = 512 + 32 * (row - 16) + col;
      if (p >= 0) dst[p] = acc[r] + red[r * 64 + lane];
    }
    if (lane == 0) dst[640] = s2048 + red[1024];
  }
}

__device__ __forceinline__ void wt_block(const u16* __restrict__ wr, const u16* __restrict__ wi, u32* __restrict__ WT,
                                         u32* tile, int bid, int tid) {
  int s0 = bid * 16;
  const u32* pr = reinterpret_cast<const u32*>(wr + (size_t)s0 * DF);
  const u32* pi = reinterpret_cast<const u32*>(wi + (size_t)s0 * DF);
  u32 a[17], b[17];
#pragma unroll
  for (int i = 0; i < 17; ++i) {
    int w = min(tid + 256 * i, 8 * DF - 1);
    a[i] = pr[w];
    b[i] = pi[w];
  }
#pragma unroll
  for (int i = 0; i < 17; ++i) {
    int w = tid + 256 * i;
    if (w < 8 * DF) {
      int e0 = 2 * w;
      int r = e0 / DF, k = e0 - r * DF;
      tile[k * 17 + r] = (a[i] & 0xffffu) | (b[i] << 16);
      int k1 = k + 1, r1 = r;
      if (k1 == DF) { k1 = 0; r1 = r + 1; }
      tile[k1 * 17 + r1] = (a[i] >> 16) | (b[i] & 0xffff0000u);
    }
  }
  __syncthreads();
  int sx = tid & 15, ky = tid >> 4;
#pragma unroll
  for (int i = 0; i < 33; ++i) {
    int k = ky + 16 * i;
    if (k < DF) WT[(size_t)k * SEQ + s0 + sx] = tile[k * 17 + sx];
  }
}

#define RP 34
#define RRS (16 * RP)
#define ZP 517
#define TWOFF (16 * RRS)

__device__ __forceinline__ void brow_block(const u16* __restrict__ x, cf* __restrict__ X1T, cf* sm, int bid, int tid) {
  cf* tw = sm + 8 * RRS;
  int s0 = bid * 8;
  {
    float s, c;
    sincospif(-(float)tid / 512.0f, &s, &c);
    tw[tid] = cmk(c, s);
    if (tid == 0) tw[256] = cmk(0.f, -1.f);
  }
  int row = tid >> 5, t = tid & 31;
  cf v[16];
  const u32* xr = reinterpret_cast<const u32*>(x + (size_t)(s0 + row) * DCH);
#pragma unroll
  for (int a = 0; a < 16; ++a) {
    u32 w = xr[32 * a + t];
    v[a] = cmk(bf2f(w & 0xffffu), bf2f(w >> 16));
  }
  Tw16 tb = make_tw16(t, 9);
  fft_reg<16, -1>(v);
  apply_tw16<false>(v, tb);
  cf* rb = sm + row * RRS;
  {
    int pos = (t & 1) * 16 + (t >> 1);
#pragma unroll
    for (int k0 = 0; k0 < 16; ++k0) rb[k0 * RP + pos] = v[k0];
  }
  wave_sync();
  int k0 = t & 15, h = t >> 4;
  {
    const float4* p = reinterpret_cast<const float4*>(rb + k0 * RP + h * 16);
#pragma unroll
    for (int m = 0; m < 8; ++m) {
      float4 q = p[m];
      v[2 * m] = cmk(q.x, q.y);
      v[2 * m + 1] = cmk(q.z, q.w);
    }
  }
  fft_reg<16, -1>(v);
  bool hi = h != 0;
  static_for<0, 8>([&](auto ic) {
    constexpr int j = decltype(ic)::value;
    cf a = v[j], b = v[j + 8];
    swap16(a, b);
    cf br = cmk(hi ? b.y : b.x, hi ? -b.x : b.y);
    br = tw64<2 * j, -1>(br);
    v[j] = cadd(a, br);
    v[j + 8] = csub(a, br);
  });
  __syncthreads();
  cf* zs = sm + row * ZP;
#pragma unroll
  for (int r = 0; r < 16; ++r) zs[k0 + 16 * ((r & 7) + 8 * h + 16 * (r >> 3))] = v[r];
  __syncthreads();
  int r = tid & 7, kk = tid >> 3;
  const cf* zr = sm + r * ZP;
#pragma unroll
  for (int i = 0; i < 9; ++i) {
    int k = kk + 32 * i;
    if (k > 256) break;
    cf a = zr[k];
    cf b = zr[(512 - k) & 511];
    if (k == 0) {
      X1T[(size_t)0 * SEQ + s0 + r] = cmk(a.x + a.y, a.x - a.y);
    } else {
      cf w = tw[k];
      cf ev = cmk(0.5f * (a.x + b.x), 0.5f * (a.y - b.y));
      cf od = cmk(0.5f * (a.y + b.y), -0.5f * (a.x - b.x));
      cf wo = cmul(od, w);
      X1T[(size_t)k * SEQ + s0 + r] = cadd(ev, wo);
      if (k != 256) X1T[(size_t)(512 - k) * SEQ + s0 + r] = cmk(ev.x - wo.x, -(ev.y - wo.y));
    }
  }
}

__device__ __forceinline__ void browinv_block(const cf* __restrict__ X1T, u16* __restrict__ out, cf* sm, int bid, int tid) {
  cf* tw = sm + TWOFF;
  int s0 = bid * 16;
  {
    int r = tid & 15, kk = tid >> 4;
    cf a[9], b[9];
#pragma unroll
    for (int i = 0; i < 9; ++i) {
      int k = kk + 32 * i;
      if (k <= 256) {
        a[i] = X1T[(size_t)k * SEQ + s0 + r];
        b[i] = X1T[(size_t)((512 - k) & 511) * SEQ + s0 + r];
      }
    }
    if (tid <= 256) {
      float s, c;
      sincospif(-(float)tid / 512.0f, &s, &c);
      tw[tid] = cmk(c, s);
    }
    __syncthreads();
    cf* zr = sm + r * ZP;
#pragma unroll
    for (int i = 0; i < 9; ++i) {
      int k = kk + 32 * i;
      if (k <= 256) {
        if (k == 0) {
          zr[0] = cmk(a[i].x + a[i].y, a[i].x - a[i].y);
        } else {
          cf w = tw[k];
          cf ev = cmk(a[i].x + b[i].x, a[i].y - b[i].y);
          cf df = cmk(a[i].x - b[i].x, a[i].y + b[i].y);
          cf od = cmulc(df, w);
          zr[k] = cmk(ev.x - od.y, ev.y + od.x);
          if (k != 256) zr[512 - k] = cmk(ev.x + od.y, -ev.y + od.x);
        }
      }
    }
  }
  __syncthreads();
  int row = tid >> 5, t = tid & 31;
  int k0 = t & 15, h = t >> 4;
  cf v[16];
  {
    const cf* zs = sm + row * ZP;
#pragma unroll
    for (int r = 0; r < 16; ++r) v[r] = zs[k0 + 16 * ((r & 7) + 8 * h + 16 * (r >> 3))];
  }
  __syncthreads();
  bool hi = h != 0;
  static_for<0, 8>([&](auto ic) {
    constexpr int j = decltype(ic)::value;
    cf a = cadd(v[j], v[j + 8]);
    cf d = csub(v[j], v[j + 8]);
    cf dr = cmk(hi ? -d.y : d.x, hi ? d.x : d.y);
    dr = tw64<2 * j, 1>(dr);
    swap16(a, dr);
    v[j] = a;
    v[j + 8] = dr;
  });
  fft_reg<16, 1>(v);
  cf* rb = sm + row * RRS;
  {
    float4* p = reinterpret_cast<float4*>(rb + k0 * RP + h * 16);
#pragma unroll
    for (int m = 0; m < 8; ++m) p[m] = make_float4(v[2 * m].x, v[2 * m].y, v[2 * m + 1].x, v[2 * m + 1].y);
  }
  wave_sync();
  {
    int pos = (t & 1) * 16 + (t >> 1);
#pragma unroll
    for (int kk = 0; kk < 16; ++kk) v[kk] = rb[kk * RP + pos];
  }
  Tw16 tb = make_tw16(t, 9);
  apply_tw16<true>(v, tb);
  fft_reg<16, 1>(v);
  u32* orow = reinterpret_cast<u32*>(out + (size_t)(s0 + row) * (2 * DCH) + DCH);
#pragma unroll
  for (int a = 0; a < 16; ++a) orow[32 * a + t] = f2bf(v[a].x) | (f2bf(v[a].y) << 16);
}

__device__ __forceinline__ cf ldw(const u32* p, int i) {
  u32 w = p[i];
  return cmk(bf2f(w & 0xffffu), bf2f(w >> 16));
}

template <bool SPECIAL>
__device__ __forceinline__ void bcol_block(cf* __restrict__ X1T, const u32* __restrict__ WT, const cf* __restrict__ TW,
                                           cf* buf, int col, int t) {
  cf v[16];
  cf* xc = X1T + (size_t)col * SEQ;
#pragma unroll
  for (int a = 0; a < 16; ++a) v[a] = xc[128 * a + t];
  FCtx f = make_fctx(TW, t);
  const float sc = 1.0f / (2048.0f * 1024.0f);
  const u32* wc = WT + (size_t)col * SEQ;
  u32 wq[16];
  if (!SPECIAL) {
#pragma unroll
    for (int j = 0; j < 8; ++j) {
      wq[j] = wc[f.kA + 256 * j];
      wq[8 + j] = wc[f.kB + 256 * j];
    }
  }
  fft_reg<16, -1>(v);
  fft2048_fwd_rest(v, buf, f);
  if (!SPECIAL) {
#pragma unroll
    for (int r = 0; r < 16; ++r) {
      cf w = cmk(bf2f(wq[r] & 0xffffu) * sc, bf2f(wq[r] >> 16) * sc);
      v[r] = cmul(v[r], w);
    }
  } else {
    __syncthreads();
#pragma unroll
    for (int j = 0; j < 8; ++j) {
      buf[f.kA + 256 * j] = v[j];
      buf[f.kB + 256 * j] = v[8 + j];
    }
    __syncthreads();
    const u32* w0 = WT;
    const u32* w1 = WT + (size_t)512 * SEQ;
#pragma unroll 1
    for (int k = t; k <= 1024; k += 128) {
      int km = (2048 - k) & 2047;
      cf a = buf[k], b = buf[km];
      cf A = cmk(0.5f * (a.x + b.x), 0.5f * (a.y - b.y));
      cf B = cmk(0.5f * (a.y + b.y), -0.5f * (a.x - b.x));
      cf p = ldw(w0, k), q = ldw(w0, km);
      cf wh0 = cmk(0.5f * sc * (p.x + q.x), 0.5f * sc * (p.y - q.y));
      p = ldw(w1, k); q = ldw(w1, km);
      cf wh1 = cmk(0.5f * sc * (p.x + q.x), 0.5f * sc * (p.y - q.y));
      cf h0 = cmul(A, wh0), h1 = cmul(B, wh1);
      buf[km] = cmk(h0.x + h1.y, -h0.y + h1.x);
      buf[k] = cmk(h0.x - h1.y, h0.y + h1.x);
    }
    __syncthreads();
#pragma unroll
    for (int j = 0; j < 8; ++j) {
      v[j] = buf[f.kA + 256 * j];
      v[8 + j] = buf[f.kB + 256 * j];
    }
  }
  fft2048_inv_first(v, buf, f);
  fft_reg<16, 1>(v);
#pragma unroll
  for (int a = 0; a < 16; ++a) xc[128 * a + t] = v[a];
}

__device__ __forceinline__ int kpos(int l, int s) {
  if (s == 0) return l;
  int half = 1 << (s - 1);
  int q = (l + half - 1 + ((l >> s) & 1)) >> s;
  return 128 * (s + 1) + q - 128;
}
__device__ __forceinline__ cf csel(bool c, cf a, cf b) { return cmk(c ? a.x : b.x, c ? a.y : b.y); }

__device__ __forceinline__ void aconv_block(const u32* __restrict__ uT, const float* __restrict__ Kc,
                                            const cf* __restrict__ TW, u32* __restrict__ yT, cf* buf, int d, int t) {
  cf v[16], kz[16];
  u32 uw[8];
  {
    const float* kl = Kc + (size_t)d * KC_PITCH;
    static_for<0, 8>([&](auto ic) {
      constexpr int a = decltype(ic)::value;
      constexpr int s = (a < 1) ? 0 : (a < 2) ? 1 : (a < 4) ? 2 : 3;
      int l = 256 * a + 2 * t;
      kz[a] = cmk(kl[kpos(l, s)], kl[kpos(l + 1, s)]);
    });
    const u32* ur = uT + (size_t)d * (SEQ / 2);
#pragma unroll
    for (int a = 0; a < 8; ++a) uw[a] = ur[128 * a + t];
  }
  FCtx f = make_fctx(TW, t);
  cf L = TW[f.kA];
  cf L2 = TW[t == 0 ? 128 : f.kA];
  fft16_halfzero<-1>(kz);
  fft2048_fwd_rest(kz, buf, f);
#pragma unroll
  for (int a = 0; a < 8; ++a) v[a] = cmk(bf2f(uw[a] & 0xffffu), bf2f(uw[a] >> 16)) * (0.5f / 2048.0f);
  fft16_halfzero<-1>(v);
  fft2048_fwd_rest(v, buf, f);
  {
    const bool sp = (t == 0);
    cf oP[8], oQ[8];
    static_for<0, 8>([&](auto ic) {
      constexpr int j = decltype(ic)::value;
      cf a, b, ka, kb;
      if constexpr (j == 0) {
        a = v[0]; b = v[15]; ka = kz[0]; kb = kz[15];
      } else if constexpr (j < 4) {
        a = v[j]; ka = kz[j];
        b = csel(sp, v[8 - j], v[15 - j]);
        kb = csel(sp, kz[8 - j], kz[15 - j]);
      } else {
        a = csel(sp, v[8 + j], v[j]);
        ka = csel(sp, kz[8 + j], kz[j]);
        b = v[15 - j]; kb = kz[15 - j];
      }
      cf eu = addc(a, b);
      cf ou = oddc(a, b) * 0.5f;
      cf ek = addc(ka, kb);
      cf ok = oddc(ka, kb);
      cf w = tw64<8 * j, -1>((j < 4) ? L : L2);
      cf wo = cmul(ok, w);
      cf T = add_pi(wo, ek);
      cf T2 = conj_add_pi(wo, ek);
      oP[j] = cmad(ou, T, cmul(eu, ka));
      oQ[j] = cmadc(T2, ou, cmulc(kb, eu));
    });
    cf s0, s4;
    {
      float eu = 2.0f * v[0].x, ou = 2.0f * v[0].y;
      s0 = cmk(fmaf(eu, kz[0].x, ou * kz[0].y), fmaf(eu, kz[0].y, ou * kz[0].x));
      eu = 2.0f * v[4].x; ou = 2.0f * v[4].y;
      s4 = cmk(fmaf(eu, kz[4].x, -(ou * kz[4].y)), fmaf(eu, kz[4].y, ou * kz[4].x));
    }
    v[0] = csel(sp, s0, oP[0]);
    v[1] = oP[1];
    v[2] = oP[2];
    v[3] = oP[3];
    v[4] = csel(sp, s4, oP[4]);
    v[5] = csel(sp, oQ[3], oP[5]);
    v[6] = csel(sp, oQ[2], oP[6]);
    v[7] = csel(sp, oQ[1], oP[7]);
    v[8] = oQ[7];
    v[9] = oQ[6];
    v[10] = oQ[5];
    v[11] = oQ[4];
    v[12] = csel(sp, oP[4], oQ[3]);
    v[13] = csel(sp, oP[5], oQ[2]);
    v[14] = csel(sp, oP[6], oQ[1]);
    v[15] = csel(sp, oP[7], oQ[0]);
  }
  fft2048_inv_first(v, buf, f);
  fft_reg<16, 1>(v);
  u32* yr = yT + (size_t)d * (SEQ / 2);
#pragma unroll
  for (int a = 0; a < 8; ++a) yr[128 * a + t] = f2bf(v[a].x) | (f2bf(v[a].y) << 16);
}

__device__ __forceinline__ void utrans_block(const u16* __restrict__ u, u16* __restrict__ uT, u16* tile, int bid, int tid) {
  uint4 q[2][2];
  int r = tid >> 3, c = tid & 7;
#pragma unroll
  for (int it = 0; it < 2; ++it) {
    int tl = bid * 2 + it;
    int l0 = (tl >> 4) * 64, d0 = (tl & 15) * 64;
#pragma unroll
    for (int h = 0; h < 2; ++h)
      q[it][h] = *reinterpret_cast<const uint4*>(u + (size_t)(l0 + r + 32 * h) * DCH + d0 + 8 * c);
  }
#pragma unroll
  for (int it = 0; it < 2; ++it) {
    u16* tl_ = tile + it * 64 * 72;
#pragma unroll
    for (int h = 0; h < 2; ++h) {
      u32 w[4] = {q[it][h].x, q[it][h].y, q[it][h].z, q[it][h].w};
#pragma unroll
      for (int j = 0; j < 4; ++j) {
        tl_[(8 * c + 2 * j) * 72 + r + 32 * h] = (u16)(w[j] & 0xffffu);
        tl_[(8 * c + 2 * j + 1) * 72 + r + 32 * h] = (u16)(w[j] >> 16);
      }
    }
  }
  __syncthreads();
#pragma unroll
  for (int it = 0; it < 2; ++it) {
    int tl = bid * 2 + it;
    int l0 = (tl >> 4) * 64, d0 = (tl & 15) * 64;
    const u16* tl_ = tile + it * 64 * 72;
#pragma unroll
    for (int h = 0; h < 2; ++h) {
      uint4 o = *reinterpret_cast<const uint4*>(tl_ + (r + 32 * h) * 72 + 8 * c);
      *reinterpret_cast<uint4*>(uT + (size_t)(d0 + r + 32 * h) * SEQ + l0 + 8 * c) = o;
    }
  }
}

__device__ __forceinline__ void ytrans_block(const u16* __restrict__ yT, const u16* __restrict__ u,
                                             const u16* __restrict__ Dskip, u16* __restrict__ out, u16* tile,
                                             int bid, int tid) {
#pragma unroll
  for (int it = 0; it < 4; ++it) {
    int tl = bid * 4 + it;
    int l0 = (tl >> 4) * 64, d0 = (tl & 15) * 64;
    int r = tid >> 3, c = tid & 7;
    uint4 q = *reinterpret_cast<const uint4*>(yT + (size_t)(d0 + r) * SEQ + l0 + 8 * c);
    uint4 uq = *reinterpret_cast<const uint4*>(u + (size_t)(l0 + r) * DCH + d0 + 8 * c);
    uint4 dq = *reinterpret_cast<const uint4*>(Dskip + d0 + 8 * c);
    u32 w[4] = {q.x, q.y, q.z, q.w};
    if (it) __syncthreads();
#pragma unroll
    for (int j = 0; j < 4; ++j) {
      tile[(8 * c + 2 * j) * 72 + r] = (u16)(w[j] & 0xffffu);
      tile[(8 * c + 2 * j + 1) * 72 + r] = (u16)(w[j] >> 16);
    }
    __syncthreads();
    uint4 y = *reinterpret_cast<const uint4*>(tile + r * 72 + 8 * c);
    u32 yy[4] = {y.x, y.y, y.z, y.w}, uu[4] = {uq.x, uq.y, uq.z, uq.w}, dd[4] = {dq.x, dq.y, dq.z, dq.w};
    u32 oo[4];
#pragma unroll
    for (int j = 0; j < 4; ++j) {
      float o0 = bf2f(yy[j] & 0xffffu) + bfround(bf2f(uu[j] & 0xffffu) * bf2f(dd[j] & 0xffffu));
      float o1 = bf2f(yy[j] >> 16) + bfround(bf2f(uu[j] >> 16) * bf2f(dd[j] >> 16));
      oo[j] = f2bf(o0) | (f2bf(o1) << 16);
    }
    *reinterpret_cast<uint4*>(out + (size_t)(l0 + r) * (2 * DCH) + d0 + 8 * c) = make_uint4(oo[0], oo[1], oo[2], oo[3]);
  }
}

#define SM_CF (TWOFF + 260)

__global__ __launch_bounds__(256, 3) void k1(KParams P, float* Kc, const u16* x, cf* X1T, const u16* wr,
                                            const u16* wi, u32* WT, cf* TW, const u16* u, u16* uT) {
  __shared__ __attribute__((aligned(16))) u32 smw[4 * 32 * KREC];
  cf* sm = reinterpret_cast<cf*>(smw);
  int b = blockIdx.x, tid = threadIdx.x;
  if (b < 768) {
    int q = b / 3, r = b % 3;
    if (r < 2) {
      kgen_block(P, Kc, smw, q * 2 + r, tid);
    } else {
      brow_block(x, X1T, sm, q, tid);
    }
  } else if (b < 768 + 128) {
    int i = b - 768;
    if (i < 8) {
      int j = i * 256 + tid;
      float s, c;
      sincospif(-(float)j / 1024.0f, &s, &c);
      TW[j] = cmk(c, s);
    }
    wt_block(wr, wi, WT, smw, i, tid);
  } else {
    utrans_block(u, uT, reinterpret_cast<u16*>(smw), b - 896, tid);
  }
}

__global__ __launch_bounds__(128, 3) void k2(cf* X1T, const u32* WT, const u32* uT, const float* Kc, const cf* TW,
                                            u32* yT) {
  __shared__ __attribute__((aligned(16))) cf sm[XBUF];
  int b = blockIdx.x, tid = threadIdx.x;
  int q = b / 3, r = b % 3;
  if (r == 2) {
    if (q == 0) bcol_block<true>(X1T, WT, TW, sm, q, tid);
    else bcol_block<false>(X1T, WT, TW, sm, q, tid);
  } else {
    aconv_block(uT, Kc, TW, yT, sm, q * 2 + r, tid);
  }
}

__global__ __launch_bounds__(512, 2) void k3(const cf* X1T, const u16* yT, const u16* u, const u16* Dskip, u16* out) {
  __shared__ __attribute__((aligned(16))) cf sm[SM_CF];
  int b = blockIdx.x, tid = threadIdx.x;
  if (b & 1) {
    browinv_block(X1T, out, sm, b >> 1, tid);
  } else {
    ytrans_block(yT, u, Dskip, out, reinterpret_cast<u16*>(sm), b >> 1, tid);
  }
}

extern "C" void kernel_launch(void* const* d_in, const int* in_sizes, int n_in, void* d_out, int out_size,
                              void* d_ws, size_t ws_size, hipStream_t stream) {
  const u16* u = (const u16*)d_in[0];
  KParams P;
  P.Are = (const u16*)d_in[1];
  P.Aim = (const u16*)d_in[2];
  P.Bre = (const u16*)d_in[3];
  P.Bim = (const u16*)d_in[4];
  P.Cre = (const u16*)d_in[5];
  P.Cim = (const u16*)d_in[6];
  P.logdt = (const u16*)d_in[7];
  const u16* Dskip = (const u16*)d_in[8];
  const u16* x = (const u16*)d_in[9];
  const u16* wr = (const u16*)d_in[10];
  const u16* wi = (const u16*)d_in[11];
  u16* out = (u16*)d_out;
  char* ws = (char*)d_ws;
  cf* X1T = (cf*)ws;
  u32* WT = (u32*)(ws + (size_t)8 * 1024 * 1024);
  float* Kc = (float*)(ws + (size_t)16 * 1024 * 1024);
  cf* TW = (cf*)(ws + (size_t)20 * 1024 * 1024);
  u16* uT = (u16*)(ws + (size_t)24 * 1024 * 1024);
  u16* yT = (u16*)(ws + (size_t)28 * 1024 * 1024);

  k1<<<768 + 128 + 256, 256, 0, stream>>>(P, Kc, x, X1T, wr, wi, WT, TW, u, uT);
  k2<<<1536, 128, 0, stream>>>(X1T, WT, (const u32*)uT, Kc, TW, (u32*)yT);
  k3<<<256, 512, 0, stream>>>(X1T, yT, u, Dskip, out);
}
```

```cpp
#include <hip/hip_runtime.h>
#include <hip/hip_bf16.h>
#include <type_traits>

#define SEQ 2048
#define DCH 1024
#define NST 128
#define DF 513

typedef __attribute__((ext_vector_type(2))) float cf;
typedef unsigned int u32;
typedef unsigned short u16;
using f32x16 = __attribute__((ext_vector_type(16))) float;

template <int I, int N, class F>
__device__ __forceinline__ void static_for(F&& f) {
  if constexpr (I < N) {
    f(std::integral_constant<int, I>{});
    static_for<I + 1, N>(f);
  }
}

__device__ __forceinline__ cf cmk(float x, float y) { cf r; r.x = x; r.y = y; return r; }
__device__ __forceinline__ cf cadd(cf a, cf b) { return a + b; }
__device__ __forceinline__ cf csub(cf a, cf b) { return a - b; }
__device__ __forceinline__ cf add_pi(cf a, cf b) {
  cf r;
  asm("v_pk_add_f32 %0, %1, %2 op_sel:[0,1] op_sel_hi:[1,0] neg_lo:[0,1]" : "=v"(r) : "v"(a), "v"(b));
  return r;
}
__device__ __forceinline__ cf add_mi(cf a, cf b) {
  cf r;
  asm("v_pk_add_f32 %0, %1, %2 op_sel:[0,1] op_sel_hi:[1,0] neg_hi:[0,1]" : "=v"(r) : "v"(a), "v"(b));
  return r;
}
__device__ __forceinline__ cf cmul(cf a, cf b) {
  cf t, r;
  asm("v_pk_mul_f32 %0, %1, %2 op_sel:[0,0] op_sel_hi:[0,1]" : "=v"(t) : "v"(a), "v"(b));
  asm("v_pk_fma_f32 %0, %1, %2, %3 op_sel:[1,1,0] op_sel_hi:[1,0,1] neg_lo:[0,1,0]" : "=v"(r) : "v"(a), "v"(b), "v"(t));
  return r;
}
__device__ __forceinline__ cf cmad(cf a, cf b, cf c) {
  cf t, r;
  asm("v_pk_fma_f32 %0, %1, %2, %3 op_sel:[0,0,0] op_sel_hi:[0,1,1]" : "=v"(t) : "v"(a), "v"(b), "v"(c));
  asm("v_pk_fma_f32 %0, %1, %2, %3 op_sel:[1,1,0] op_sel_hi:[1,0,1] neg_lo:[0,1,0]" : "=v"(r) : "v"(a), "v"(b), "v"(t));
  return r;
}
__device__ __forceinline__ cf cmulc(cf a, cf b) {
  cf t, r;
  asm("v_pk_mul_f32 %0, %1, %2 op_sel:[0,0] op_sel_hi:[1,0]" : "=v"(t) : "v"(a), "v"(b));
  asm("v_pk_fma_f32 %0, %1, %2, %3 op_sel:[1,1,0] op_sel_hi:[0,1,1] neg_hi:[1,0,0]" : "=v"(r) : "v"(a), "v"(b), "v"(t));
  return r;
}
__device__ __forceinline__ cf cmadc(cf a, cf b, cf c) {
  cf t, r;
  asm("v_pk_fma_f32 %0, %1, %2, %3 op_sel:[0,0,0] op_sel_hi:[1,0,1]" : "=v"(t) : "v"(a), "v"(b), "v"(c));
  asm("v_pk_fma_f32 %0, %1, %2, %3 op_sel:[1,1,0] op_sel_hi:[0,1,1] neg_hi:[1,0,0]" : "=v"(r) : "v"(a), "v"(b), "v"(t));
  return r;
}
__device__ __forceinline__ cf addc(cf a, cf b) {
  cf r;
  asm("v_pk_add_f32 %0, %1, %2 neg_hi:[0,1]" : "=v"(r) : "v"(a), "v"(b));
  return r;
}
__device__ __forceinline__ cf oddc(cf a, cf b) {
  cf r;
  asm("v_pk_add_f32 %0, %1, %2 op_sel:[1,1] op_sel_hi:[0,0] neg_hi:[1,0]" : "=v"(r) : "v"(a), "v"(b));
  return r;
}
__device__ __forceinline__ cf conj_add_pi(cf a, cf b) {
  cf r;
  asm("v_pk_add_f32 %0, %1, %2 op_sel:[0,1] op_sel_hi:[1,0] neg_hi:[1,0]" : "=v"(r) : "v"(a), "v"(b));
  return r;
}
__device__ __forceinline__ cf cmulk(cf a, cf cs) {
  cf t, r;
  asm("v_pk_mul_f32 %0, %1, %2 op_sel:[0,0] op_sel_hi:[1,0]" : "=v"(t) : "v"(a), "s"(cs));
  asm("v_pk_fma_f32 %0, %1, %2, %3 op_sel:[1,1,0] op_sel_hi:[0,1,1] neg_lo:[1,0,0]" : "=v"(r) : "v"(a), "s"(cs), "v"(t));
  return r;
}
__device__ __forceinline__ cf csqr(cf a) { return cmk(fmaf(a.x, a.x, -(a.y * a.y)), 2.0f * a.x * a.y); }

__device__ __forceinline__ float bf2f(u32 h) { return __uint_as_float(h << 16); }
__device__ __forceinline__ u32 f2bf(float f) {
  __hip_bfloat16 b = __float2bfloat16(f);
  return (u32)(*reinterpret_cast<u16*>(&b));
}
__device__ __forceinline__ float bfround(float f) { return bf2f(f2bf(f)); }

__device__ __forceinline__ void wave_sync() {
  __builtin_amdgcn_fence(__ATOMIC_SEQ_CST, "wavefront");
  __builtin_amdgcn_wave_barrier();
  __builtin_amdgcn_fence(__ATOMIC_SEQ_CST, "wavefront");
}

constexpr double COS64_TAB[17] = {
    1.0, 0.99518472667219688624, 0.98078528040323044913, 0.95694033573220886494,
    0.92387953251128675613, 0.88192126434835502971, 0.83146961230254523708,
    0.77301045336273696081, 0.70710678118654752440, 0.63439328416364549822,
    0.55557023301960222474, 0.47139673682599764856, 0.38268343236508977173,
    0.29028467725446236764, 0.19509032201612826785, 0.09801714032956060199, 0.0};
constexpr double cos64(int j) {
  j = ((j % 64) + 64) % 64;
  if (j > 32) j = 64 - j;
  return (j <= 16) ? COS64_TAB[j] : -COS64_TAB[32 - j];
}
constexpr double sin64(int j) { return cos64(j - 16); }

template <int J, int DIR>
__device__ __forceinline__ cf tw64(cf a) {
  constexpr int j = ((J % 64) + 64) % 64;
  if constexpr (j == 0) {
    return a;
  } else if constexpr (j == 32) {
    return -a;
  } else if constexpr (j == 16) {
    return (DIR > 0) ? cmk(-a.y, a.x) : cmk(a.y, -a.x);
  } else if constexpr (j == 48) {
    return (DIR > 0) ? cmk(a.y, -a.x) : cmk(-a.y, a.x);
  } else {
    constexpr float c = (float)cos64(j);
    constexpr float s = (float)(DIR * sin64(j));
    return cmulk(a, cmk(c, s));
  }
}

template <int N, int DIR, int S>
__device__ __forceinline__ void fft_rec(const cf* in, cf* out) {
  if constexpr (N == 1) {
    out[0] = in[0];
  } else if constexpr (N == 2) {
    cf a = in[0], b = in[S];
    out[0] = cadd(a, b);
    out[1] = csub(a, b);
  } else {
    constexpr int Q = N / 4;
    cf t0[Q], t1[Q], t2[Q], t3[Q];
    fft_rec<Q, DIR, 4 * S>(in, t0);
    fft_rec<Q, DIR, 4 * S>(in + S, t1);
    fft_rec<Q, DIR, 4 * S>(in + 2 * S, t2);
    fft_rec<Q, DIR, 4 * S>(in + 3 * S, t3);
    static_for<0, Q>([&](auto ic) {
      constexpr int k = decltype(ic)::value;
      constexpr int M = 64 / N;
      cf a = t0[k];
      cf b = tw64<k * M, DIR>(t1[k]);
      cf d = tw64<3 * k * M, DIR>(t3[k]);
      cf s0, s1;
      if constexpr (2 * k * M == 16) {
        s0 = (DIR > 0) ? add_pi(a, t2[k]) : add_mi(a, t2[k]);
        s1 = (DIR > 0) ? add_mi(a, t2[k]) : add_pi(a, t2[k]);
      } else {
        cf c = tw64<2 * k * M, DIR>(t2[k]);
        s0 = cadd(a, c);
        s1 = csub(a, c);
      }
      cf s2 = cadd(b, d), s3 = csub(b, d);
      out[k] = cadd(s0, s2);
      out[k + Q] = (DIR > 0) ? add_pi(s1, s3) : add_mi(s1, s3);
      out[k + 2 * Q] = csub(s0, s2);
      out[k + 3 * Q] = (DIR > 0) ? add_mi(s1, s3) : add_pi(s1, s3);
    });
  }
}

template <int N, int DIR>
__device__ __forceinline__ void fft_reg(cf* v) {
  cf o[N];
  fft_rec<N, DIR, 1>(v, o);
#pragma unroll
  for (int i = 0; i < N; ++i) v[i] = o[i];
}

template <int DIR>
__device__ __forceinline__ void fft16_halfzero(cf* v) {
  cf a[8], b[8], oa[8], ob[8];
  static_for<0, 8>([&](auto ic) {
    constexpr int n = decltype(ic)::value;
    a[n] = v[n];
    b[n] = tw64<4 * n, DIR>(v[n]);
  });
  fft_rec<8, DIR, 1>(a, oa);
  fft_rec<8, DIR, 1>(b, ob);
#pragma unroll
  for (int k = 0; k < 8; ++k) {
    v[2 * k] = oa[k];
    v[2 * k + 1] = ob[k];
  }
}

struct Tw16 { cf w[4]; };

__device__ __forceinline__ Tw16 make_tw16(int n, int logN) {
  Tw16 b;
#pragma unroll
  for (int t = 0; t < 4; ++t) {
    int e = (n << t) & ((1 << logN) - 1);
    float s, c;
    sincospif(-2.0f * (float)e / (float)(1 << logN), &s, &c);
    b.w[t] = cmk(c, s);
  }
  return b;
}

__device__ __forceinline__ Tw16 load_tw16(const cf* __restrict__ TW, int n) {
  Tw16 b;
#pragma unroll
  for (int t = 0; t < 4; ++t) b.w[t] = TW[(n << t) & 2047];
  return b;
}

template <bool CONJ>
__device__ __forceinline__ void apply_tw16(cf* v, const Tw16& b) {
  cf p[16];
  p[1] = b.w[0];
  p[2] = b.w[1];
  p[4] = b.w[2];
  p[8] = b.w[3];
  asm volatile("" : "+v"(p[1]), "+v"(p[2]), "+v"(p[4]), "+v"(p[8]));
  static_for<3, 16>([&](auto ic) {
    constexpr int k = decltype(ic)::value;
    constexpr int top = (k >= 8) ? 8 : (k >= 4) ? 4 : 2;
    if constexpr (k != top) p[k] = cmul(p[k - top], p[top]);
  });
#pragma unroll
  for (int k = 1; k < 16; ++k) v[k] = CONJ ? cmulc(v[k], p[k]) : cmul(v[k], p[k]);
}

__device__ __forceinline__ void swap16(cf& a, cf& b) {
  asm volatile("s_nop 1" : "+v"(a), "+v"(b));
  auto r0 = __builtin_amdgcn_permlane16_swap(__float_as_uint(a.x), __float_as_uint(b.x), false, false);
  auto r1 = __builtin_amdgcn_permlane16_swap(__float_as_uint(a.y), __float_as_uint(b.y), false, false);
  a.x = __uint_as_float(r0[0]);
  b.x = __uint_as_float(r0[1]);
  a.y = __uint_as_float(r1[0]);
  b.y = __uint_as_float(r1[1]);
}

#define XP 152
#define XBUF (16 * XP)
typedef volatile __attribute__((address_space(3))) cf* ldsp;
__device__ __forceinline__ cf lds_ld(const cf* p) { return *(ldsp)(p); }
__device__ __forceinline__ void lds_st(cf* p, cf a) { *(ldsp)(p) = a; }

struct FCtx {
  Tw16 ta, tb;
  int t, offA, offB, kA, kB;
};
__device__ __forceinline__ FCtx make_fctx(const cf* __restrict__ TW, int t) {
  FCtx f;
  int k0 = t >> 3, c = t & 7;
  f.t = t;
  f.ta = load_tw16(TW, t);
  f.tb = load_tw16(TW, 16 * c);
  int k0m = (16 - k0) & 15;
  int k1m = (k0 != 0) ? (15 - c) : ((c != 0) ? (16 - c) : 8);
  f.offA = k0 * XP + 9 * c;
  f.offB = k0m * XP + 9 * k1m;
  f.kA = k0 + 16 * c;
  f.kB = k0m + 16 * k1m;
  return f;
}

__device__ __forceinline__ void fft2048_fwd_rest(cf* v, cf* buf, const FCtx& f) {
  apply_tw16<false>(v, f.ta);
  __syncthreads();
#pragma unroll
  for (int k0 = 0; k0 < 16; ++k0) lds_st(buf + k0 * XP + f.t, v[k0]);
  __syncthreads();
  cf* rb = buf + (f.t >> 3) * XP + (f.t & 7);
#pragma unroll
  for (int b = 0; b < 16; ++b) v[b] = lds_ld(rb + 8 * b);
  fft_reg<16, -1>(v);
  apply_tw16<false>(v, f.tb);
#pragma unroll
  for (int k1 = 0; k1 < 16; ++k1) lds_st(rb + 9 * k1, v[k1]);
  __syncthreads();
#pragma unroll
  for (int j = 0; j < 8; ++j) v[j] = lds_ld(buf + f.offA + j);
#pragma unroll
  for (int j = 0; j < 8; ++j) v[8 + j] = lds_ld(buf + f.offB + j);
  fft_reg<8, -1>(v);
  fft_reg<8, -1>(v + 8);
}

__device__ __forceinline__ void fft2048_inv_first(cf* v, cf* buf, const FCtx& f) {
  fft_reg<8, 1>(v);
  fft_reg<8, 1>(v + 8);
  __syncthreads();
#pragma unroll
  for (int j = 0; j < 8; ++j) lds_st(buf + f.offA + j, v[j]);
#pragma unroll
  for (int j = 0; j < 8; ++j) lds_st(buf + f.offB + j, v[8 + j]);
  __syncthreads();
  cf* rb = buf + (f.t >> 3) * XP + (f.t & 7);
#pragma unroll
  for (int k1 = 0; k1 < 16; ++k1) v[k1] = lds_ld(rb + 9 * k1);
  apply_tw16<true>(v, f.tb);
  fft_reg<16, 1>(v);
#pragma unroll
  for (int b = 0; b < 16; ++b) lds_st(rb + 8 * b, v[b]);
  __syncthreads();
#pragma unroll
  for (int kk = 0; kk < 16; ++kk) v[kk] = lds_ld(buf + kk * XP + f.t);
  apply_tw16<true>(v, f.ta);
}

#define KC_PITCH 648
#define KREC 104
using u32x4 = __attribute__((ext_vector_type(4))) unsigned;
using u32x2 = __attribute__((ext_vector_type(2))) unsigned;
using bf16x8 = __attribute__((ext_vector_type(8))) __bf16;

__device__ __forceinline__ cf cexp_scaled(float ar, float ai, float sc) {
  float e = expf(ar * sc);
  float s, c;
  sincosf(ai * sc, &s, &c);
  return cmk(e * c, e * s);
}
__device__ __forceinline__ u32 cvtpk(float a, float b) {
  u32 r;
  asm("v_cvt_pk_bf16_f32 %0, %1, %2" : "=v"(r) : "v"(a), "v"(b));
  return r;
}
__device__ __forceinline__ u32x2 split2(cf g) {
  u32x2 o;
  o.x = cvtpk(g.x, g.y);
  cf h = cmk(__uint_as_float(o.x << 16), __uint_as_float(o.x & 0xffff0000u));
  cf r = g - h;
  o.y = cvtpk(r.x, r.y);
  return o;
}

struct KParams { const u16 *Are, *Aim, *Bre, *Bim, *Cre, *Cim, *logdt; };

__device__ __forceinline__ void kgen_block(const KParams& P, float* __restrict__ Kc, const u16* __restrict__ u,
                                           u16* __restrict__ uT, u32* lds_all, int bid, int tid) {
  int wave = tid >> 6, lane = tid & 63;
  uint4 uq[2];
  const int ur = tid >> 3, uc = tid & 7;
  const int ul0 = (bid >> 4) * 64, ud0 = (bid & 15) * 64;
#pragma unroll
  for (int h = 0; h < 2; ++h) uq[h] = *reinterpret_cast<const uint4*>(u + (size_t)(ul0 + ur + 32 * h) * DCH + ud0 + 8 * uc);
  int d = bid * 2 + (wave >> 1);
  u32* lds = lds_all + wave * 32 * KREC;
  float dt = bfround(expf(bf2f(P.logdt[d])));
  int idx = d * NST + (wave & 1) * 64 + lane;
  float ar = dt * bf2f(P.Are[idx]), ai = dt * bf2f(P.Aim[idx]);
  cf dB = cmk(dt * bf2f(P.Bre[idx]), dt * bf2f(P.Bim[idx]));
  cf C = cmk(bf2f(P.Cre[idx]), bf2f(P.Cim[idx]));
  cf c0 = cmul(C, dB);
  cf e[12];
  e[0] = cexp_scaled(ar, ai, 1.0f);
  e[4] = cexp_scaled(ar, ai, 16.0f);
  e[8] = cexp_scaled(ar, ai, 256.0f);
  e[1] = csqr(e[0]); e[2] = csqr(e[1]); e[3] = csqr(e[2]);
  e[5] = csqr(e[4]); e[6] = csqr(e[5]); e[7] = csqr(e[6]);
  e[9] = csqr(e[8]); e[10] = csqr(e[9]); e[11] = csqr(e[10]);
  float s2048 = fmaf(c0.x, e[11].x, -(c0.y * e[11].y));
  u32x2 Gw[20], Ew[32];
  {
    cf G[20];
    G[0] = cmk(c0.x, -c0.y);
    G[1] = cmulc(G[0], e[0]);
    G[2] = cmulc(G[0], e[1]);
    G[3] = cmulc(G[1], e[1]);
    G[4] = cmulc(G[0], e[2]);
    G[5] = cmulc(G[1], e[2]);
    G[6] = cmulc(G[2], e[2]);
    G[7] = cmulc(G[3], e[2]);
    G[8] = cmulc(G[0], e[8]);
    G[9] = cmulc(G[2], e[8]);
    G[10] = cmulc(G[4], e[8]);
    G[11] = cmulc(G[6], e[8]);
    G[12] = cmulc(G[0], e[9]);
    G[13] = cmulc(G[4], e[9]);
    G[14] = cmulc(G[8], e[9]);
    G[15] = cmulc(G[10], e[9]);
    G[16] = cmulc(G[0], e[10]);
    G[17] = cmulc(G[8], e[10]);
    G[18] = cmulc(G[12], e[10]);
    G[19] = cmulc(G[14], e[10]);
#pragma unroll
    for (int i = 0; i < 20; ++i) Gw[i] = split2(G[i]);
  }
  {
    cf E[32];
    E[0] = cmk(1.f, 0.f);
    E[1] = e[3]; E[2] = e[4]; E[4] = e[5]; E[8] = e[6]; E[16] = e[7];
    static_for<3, 32>([&](auto ic) {
      constexpr int k = decltype(ic)::value;
      constexpr int top = (k >= 16) ? 16 : (k >= 8) ? 8 : (k >= 4) ? 4 : 2;
      if constexpr (k != top) E[k] = cmul(E[k - top], E[top]);
    });
    Ew[0].x = 0x3f80u;
    Ew[0].y = 0u;
#pragma unroll
    for (int i = 1; i < 32; ++i) Ew[i] = split2(E[i]);
  }
#pragma unroll
  for (int o = 32; o >= 1; o >>= 1) s2048 += __shfl_xor(s2048, o);
  f32x16 acc;
#pragma unroll
  for (int i = 0; i < 16; ++i) acc[i] = 0.f;
  typedef volatile __attribute__((address_space(3))) u32x2* lp2;
  for (int ch = 0; ch < 2; ++ch) {
    if ((lane >> 5) == ch) {
      u32x4* rec = reinterpret_cast<u32x4*>(lds + (lane & 31) * KREC);
#pragma unroll
      for (int i = 0; i < 10; ++i) {
        u32x4 q = {Gw[2 * i].x, Gw[2 * i].y, Gw[2 * i + 1].x, Gw[2 * i + 1].y};
        rec[i] = q;
      }
#pragma unroll
      for (int i = 0; i < 16; ++i) {
        u32x4 q = {Ew[2 * i].x, Ew[2 * i].y, Ew[2 * i + 1].x, Ew[2 * i + 1].y};
        rec[10 + i] = q;
      }
    }
    wave_sync();
    const u32* base = lds + (2 * (lane >> 5)) * KREC + 2 * (lane & 31);
#pragma unroll
    for (int m = 0; m < 8; ++m) {
      const u32* r0 = base + 4 * m * KREC;
      u32x2 a0 = *(lp2)(r0), a1 = *(lp2)(r0 + KREC), b0 = *(lp2)(r0 + 40), b1 = *(lp2)(r0 + KREC + 40);
      u32x4 A = {a0.x, a0.y, a1.x, a1.y}, A2 = {a0.y, a0.x, a1.y, a1.x}, B = {b0.x, b0.y, b1.x, b1.y};
      acc = __builtin_amdgcn_mfma_f32_32x32x16_bf16(__builtin_bit_cast(bf16x8, A), __builtin_bit_cast(bf16x8, B), acc, 0, 0, 0);
      acc = __builtin_amdgcn_mfma_f32_32x32x16_bf16(__builtin_bit_cast(bf16x8, A2), __builtin_bit_cast(bf16x8, B), acc, 0, 0, 0);
    }
    wave_sync();
  }
  float* red = reinterpret_cast<float*>(lds_all + (wave | 1) * 32 * KREC);
  if (wave & 1) {
#pragma unroll
    for (int r = 0; r < 16; ++r) red[r * 64 + lane] = acc[r];
    if (lane == 0) red[1024] = s2048;
  }
  __syncthreads();
  u16* tile = reinterpret_cast<u16*>(lds_all);
#pragma unroll
  for (int h = 0; h < 2; ++h) {
    u32 w[4] = {uq[h].x, uq[h].y, uq[h].z, uq[h].w};
#pragma unroll
    for (int j = 0; j < 4; ++j) {
      tile[(8 * uc + 2 * j) * 72 + ur + 32 * h] = (u16)(w[j] & 0xffffu);
      tile[(8 * uc + 2 * j + 1) * 72 + ur + 32 * h] = (u16)(w[j] >> 16);
    }
  }
  if (!(wave & 1)) {
    int col = lane & 31, hh = lane >> 5;
    float* dst = Kc + (size_t)d * KC_PITCH;
#pragma unroll
    for (int r = 0; r < 16; ++r) {
      int row = (r & 3) + 8 * (r >> 2) + 4 * hh;
      int p = -1;
      if (row < 8) p = row + 8 * col;
      else if (row < 12) p = 256 + (row - 8) + 4 * col;
      else if (row < 16) p = 384 + ((row - 12) & 1) + 64 * ((row - 12) >> 1) + 2 * col;
      else if (row < 20) p = 512 + 32 * (row - 16) + col;
      if (p >= 0) dst[p] = acc[r] + red[r * 64 + lane];
    }
    if (lane == 0) dst[640] = s2048 + red[1024];
  }
  __syncthreads();
#pragma unroll
  for (int h = 0; h < 2; ++h) {
    u32x4 o = *reinterpret_cast<const u32x4*>(tile + (ur + 32 * h) * 72 + 8 * uc);
    __builtin_nontemporal_store(o, reinterpret_cast<u32x4*>(uT + (size_t)(ud0 + ur + 32 * h) * SEQ + ul0 + 8 * uc));
  }
}

struct WtRegs { u32 a[17], b[17]; };
__device__ __forceinline__ void wt_load(const u16* __restrict__ wr, const u16* __restrict__ wi, WtRegs& R, int bid, int tid) {
  int s0 = bid * 16;
  const u32* pr = reinterpret_cast<const u32*>(wr + (size_t)s0 * DF);
  const u32* pi = reinterpret_cast<const u32*>(wi + (size_t)s0 * DF);
#pragma unroll
  for (int i = 0; i < 17; ++i) {
    int w = min(tid + 256 * i, 8 * DF - 1);
    R.a[i] = pr[w];
    R.b[i] = pi[w];
  }
}
__device__ __forceinline__ void wt_finish(const WtRegs& R, u32* __restrict__ WT, u32* tile, int bid, int tid) {
  int s0 = bid * 16;
#pragma unroll
  for (int i = 0; i < 17; ++i) {
    int w = tid + 256 * i;
    if (w < 8 * DF) {
      int e0 = 2 * w;
      int r = e0 / DF, k = e0 - r * DF;
      tile[k * 17 + r] = (R.a[i] & 0xffffu) | (R.b[i] << 16);
      int k1 = k + 1, r1 = r;
      if (k1 == DF) { k1 = 0; r1 = r + 1; }
      tile[k1 * 17 + r1] = (R.a[i] >> 16) | (R.b[i] & 0xffff0000u);
    }
  }
  __syncthreads();
  int sx = tid & 15, ky = tid >> 4;
#pragma unroll
  for (int i = 0; i < 33; ++i) {
    int k = ky + 16 * i;
    if (k < DF) __builtin_nontemporal_store(tile[k * 17 + sx], WT + (size_t)k * SEQ + s0 + sx);
  }
}

#define RP 34
#define RRS (16 * RP)
#define ZP 517
#define TWOFF (16 * RRS)

__device__ __forceinline__ void brow_block(const u16* __restrict__ x, cf* __restrict__ X1T, cf* sm, int bid, int tid) {
  cf* tw = sm + 8 * RRS;
  int s0 = bid * 8;
  {
    float s, c;
    sincospif(-(float)tid / 512.0f, &s, &c);
    tw[tid] = cmk(c, s);
    if (tid == 0) tw[256] = cmk(0.f, -1.f);
  }
  int row = tid >> 5, t = tid & 31;
  cf v[16];
  const u32* xr = reinterpret_cast<const u32*>(x + (size_t)(s0 + row) * DCH);
#pragma unroll
  for (int a = 0; a < 16; ++a) {
    u32 w = xr[32 * a + t];
    v[a] = cmk(bf2f(w & 0xffffu), bf2f(w >> 16));
  }
  Tw16 tb = make_tw16(t, 9);
  fft_reg<16, -1>(v);
  apply_tw16<false>(v, tb);
  cf* rb = sm + row * RRS;
  {
    int pos = (t & 1) * 16 + (t >> 1);
#pragma unroll
    for (int k0 = 0; k0 < 16; ++k0) rb[k0 * RP + pos] = v[k0];
  }
  wave_sync();
  int k0 = t & 15, h = t >> 4;
  {
    const float4* p = reinterpret_cast<const float4*>(rb + k0 * RP + h * 16);
#pragma unroll
    for (int m = 0; m < 8; ++m) {
      float4 q = p[m];
      v[2 * m] = cmk(q.x, q.y);
      v[2 * m + 1] = cmk(q.z, q.w);
    }
  }
  fft_reg<16, -1>(v);
  bool hi = h != 0;
  static_for<0, 8>([&](auto ic) {
    constexpr int j = decltype(ic)::value;
    cf a = v[j], b = v[j + 8];
    swap16(a, b);
    cf br = cmk(hi ? b.y : b.x, hi ? -b.x : b.y);
    br = tw64<2 * j, -1>(br);
    v[j] = cadd(a, br);
    v[j + 8] = csub(a, br);
  });
  __syncthreads();
  cf* zs = sm + row * ZP;
#pragma unroll
  for (int r = 0; r < 16; ++r) zs[k0 + 16 * ((r & 7) + 8 * h + 16 * (r >> 3))] = v[r];
  __syncthreads();
  int r = tid & 7, kk = tid >> 3;
  const cf* zr = sm + r * ZP;
#pragma unroll
  for (int i = 0; i < 9; ++i) {
    int k = kk + 32 * i;
    if (k > 256) break;
    cf a = zr[k];
    cf b = zr[(512 - k) & 511];
    if (k == 0) {
      __builtin_nontemporal_store(cmk(a.x + a.y, a.x - a.y), X1T + (size_t)0 * SEQ + s0 + r);
    } else {
      cf w = tw[k];
      cf ev = cmk(0.5f * (a.x + b.x), 0.5f * (a.y - b.y));
      cf od = cmk(0.5f * (a.y + b.y), -0.5f * (a.x - b.x));
      cf wo = cmul(od, w);
      __builtin_nontemporal_store(cadd(ev, wo), X1T + (size_t)k * SEQ + s0 + r);
      if (k != 256) __builtin_nontemporal_store(cmk(ev.x - wo.x, -(ev.y - wo.y)), X1T + (size_t)(512 - k) * SEQ + s0 + r);
    }
  }
}

__device__ __forceinline__ void browinv_block(const cf* __restrict__ X1T, u16* __restrict__ out, cf* sm, int bid, int tid) {
  cf* tw = sm + TWOFF;
  int s0 = bid * 16;
  {
    int r = tid & 15, kk = tid >> 4;
    cf a[9], b[9];
#pragma unroll
    for (int i = 0; i < 9; ++i) {
      int k = kk + 32 * i;
      if (k <= 256) {
        a[i] = X1T[(size_t)k * SEQ + s0 + r];
        b[i] = X1T[(size_t)((512 - k) & 511) * SEQ + s0 + r];
      }
    }
    if (tid <= 256) {
      float s, c;
      sincospif(-(float)tid / 512.0f, &s, &c);
      tw[tid] = cmk(c, s);
    }
    __syncthreads();
    cf* zr = sm + r * ZP;
#pragma unroll
    for (int i = 0; i < 9; ++i) {
      int k = kk + 32 * i;
      if (k <= 256) {
        if (k == 0) {
          zr[0] = cmk(a[i].x + a[i].y, a[i].x - a[i].y);
        } else {
          cf w = tw[k];
          cf ev = cmk(a[i].x + b[i].x, a[i].y - b[i].y);
          cf df = cmk(a[i].x - b[i].x, a[i].y + b[i].y);
          cf od = cmulc(df, w);
          zr[k] = cmk(ev.x - od.y, ev.y + od.x);
          if (k != 256) zr[512 - k] = cmk(ev.x + od.y, -ev.y + od.x);
        }
      }
    }
  }
  __syncthreads();
  int row = tid >> 5, t = tid & 31;
  int k0 = t & 15, h = t >> 4;
  cf v[16];
  {
    const cf* zs = sm + row * ZP;
#pragma unroll
    for (int r = 0; r < 16; ++r) v[r] = zs[k0 + 16 * ((r & 7) + 8 * h + 16 * (r >> 3))];
  }
  __syncthreads();
  bool hi = h != 0;
  static_for<0, 8>([&](auto ic) {
    constexpr int j = decltype(ic)::value;
    cf a = cadd(v[j], v[j + 8]);
    cf d = csub(v[j], v[j + 8]);
    cf dr = cmk(hi ? -d.y : d.x, hi ? d.x : d.y);
    dr = tw64<2 * j, 1>(dr);
    swap16(a, dr);
    v[j] = a;
    v[j + 8] = dr;
  });
  fft_reg<16, 1>(v);
  cf* rb = sm + row * RRS;
  {
    float4* p = reinterpret_cast<float4*>(rb + k0 * RP + h * 16);
#pragma unroll
    for (int m = 0; m < 8; ++m) p[m] = make_float4(v[2 * m].x, v[2 * m].y, v[2 * m + 1].x, v[2 * m + 1].y);
  }
  wave_sync();
  {
    int pos = (t & 1) * 16 + (t >> 1);
#pragma unroll
    for (int kk = 0; kk < 16; ++kk) v[kk] = rb[kk * RP + pos];
  }
  Tw16 tb = make_tw16(t, 9);
  apply_tw16<true>(v, tb);
  fft_reg<16, 1>(v);
  u32* orow = reinterpret_cast<u32*>(out + (size_t)(s0 + row) * (2 * DCH) + DCH);
#pragma unroll
  for (int a = 0; a < 16; ++a) orow[32 * a + t] = f2bf(v[a].x) | (f2bf(v[a].y) << 16);
}

__device__ __forceinline__ cf ldw(const u32* p, int i) {
  u32 w = p[i];
  return cmk(bf2f(w & 0xffffu), bf2f(w >> 16));
}

template <bool SPECIAL>
__device__ __forceinline__ void bcol_block(cf* __restrict__ X1T, const u32* __restrict__ WT, const cf* __restrict__ TW,
                                           cf* buf, int col, int t) {
  cf v[16];
  cf* xc = X1T + (size_t)col * SEQ;
#pragma unroll
  for (int a = 0; a < 16; ++a) v[a] = xc[128 * a + t];
  FCtx f = make_fctx(TW, t);
  const float sc = 1.0f / (2048.0f * 1024.0f);
  const u32* wc = WT + (size_t)col * SEQ;
  u32 wq[16];
  if (!SPECIAL) {
#pragma unroll
    for (int j = 0; j < 8; ++j) {
      wq[j] = wc[f.kA + 256 * j];
      wq[8 + j] = wc[f.kB + 256 * j];
    }
  }
  fft_reg<16, -1>(v);
  fft2048_fwd_rest(v, buf, f);
  if (!SPECIAL) {
#pragma unroll
    for (int r = 0; r < 16; ++r) {
      cf w = cmk(bf2f(wq[r] & 0xffffu) * sc, bf2f(wq[r] >> 16) * sc);
      v[r] = cmul(v[r], w);
    }
  } else {
    __syncthreads();
#pragma unroll
    for (int j = 0; j < 8; ++j) {
      buf[f.kA + 256 * j] = v[j];
      buf[f.kB + 256 * j] = v[8 + j];
    }
    __syncthreads();
    const u32* w0 = WT;
    const u32* w1 = WT + (size_t)512 * SEQ;
#pragma unroll 1
    for (int k = t; k <= 1024; k += 128) {
      int km = (2048 - k) & 2047;
      cf a = buf[k], b = buf[km];
      cf A = cmk(0.5f * (a.x + b.x), 0.5f * (a.y - b.y));
      cf B = cmk(0.5f * (a.y + b.y), -0.5f * (a.x - b.x));
      cf p = ldw(w0, k), q = ldw(w0, km);
      cf wh0 = cmk(0.5f * sc * (p.x + q.x), 0.5f * sc * (p.y - q.y));
      p = ldw(w1, k); q = ldw(w1, km);
      cf wh1 = cmk(0.5f * sc * (p.x + q.x), 0.5f * sc * (p.y - q.y));
      cf h0 = cmul(A, wh0), h1 = cmul(B, wh1);
      buf[km] = cmk(h0.x + h1.y, -h0.y + h1.x);
      buf[k] = cmk(h0.x - h1.y, h0.y + h1.x);
    }
    __syncthreads();
#pragma unroll
    for (int j = 0; j < 8; ++j) {
      v[j] = buf[f.kA + 256 * j];
      v[8 + j] = buf[f.kB + 256 * j];
    }
  }
  fft2048_inv_first(v, buf, f);
  fft_reg<16, 1>(v);
#pragma unroll
  for (int a = 0; a < 16; ++a) __builtin_nontemporal_store(v[a], xc + 128 * a + t);
}

__device__ __forceinline__ int kpos(int l, int s) {
  if (s == 0) return l;
  int half = 1 << (s - 1);
  int q = (l + half - 1 + ((l >> s) & 1)) >> s;
  return 128 * (s + 1) + q - 128;
}
__device__ __forceinline__ cf csel(bool c, cf a, cf b) { return cmk(c ? a.x : b.x, c ? a.y : b.y); }

__device__ __forceinline__ void aconv_block(const u32* __restrict__ uT, const float* __restrict__ Kc,
                                            const cf* __restrict__ TW, u32* __restrict__ yT, cf* buf, int d, int t) {
  cf v[16], kz[16];
  u32 uw[8];
  {
    const float* kl = Kc + (size_t)d * KC_PITCH;
    static_for<0, 8>([&](auto ic) {
      constexpr int a = decltype(ic)::value;
      constexpr int s = (a < 1) ? 0 : (a < 2) ? 1 : (a < 4) ? 2 : 3;
      int l = 256 * a + 2 * t;
      kz[a] = cmk(kl[kpos(l, s)], kl[kpos(l + 1, s)]);
    });
    const u32* ur = uT + (size_t)d * (SEQ / 2);
#pragma unroll
    for (int a = 0; a < 8; ++a) uw[a] = ur[128 * a + t];
  }
  FCtx f = make_fctx(TW, t);
  cf L = TW[f.kA];
  cf L2 = TW[t == 0 ? 128 : f.kA];
  fft16_halfzero<-1>(kz);
  fft2048_fwd_rest(kz, buf, f);
#pragma unroll
  for (int a = 0; a < 8; ++a) v[a] = cmk(bf2f(uw[a] & 0xffffu), bf2f(uw[a] >> 16)) * (0.5f / 2048.0f);
  fft16_halfzero<-1>(v);
  fft2048_fwd_rest(v, buf, f);
  {
    const bool sp = (t == 0);
    cf oP[8], oQ[8];
    static_for<0, 8>([&](auto ic) {
      constexpr int j = decltype(ic)::value;
      cf a, b, ka, kb;
      if constexpr (j == 0) {
        a = v[0]; b = v[15]; ka = kz[0]; kb = kz[15];
      } else if constexpr (j < 4) {
        a = v[j]; ka = kz[j];
        b = csel(sp, v[8 - j], v[15 - j]);
        kb = csel(sp, kz[8 - j], kz[15 - j]);
      } else {
        a = csel(sp, v[8 + j], v[j]);
        ka = csel(sp, kz[8 + j], kz[j]);
        b = v[15 - j]; kb = kz[15 - j];
      }
      cf eu = addc(a, b);
      cf ou = oddc(a, b) * 0.5f;
      cf ek = addc(ka, kb);
      cf ok = oddc(ka, kb);
      cf w = tw64<8 * j, -1>((j < 4) ? L : L2);
      cf wo = cmul(ok, w);
      cf T = add_pi(wo, ek);
      cf T2 = conj_add_pi(wo, ek);
      oP[j] = cmad(ou, T, cmul(eu, ka));
      oQ[j] = cmadc(T2, ou, cmulc(kb, eu));
    });
    cf s0, s4;
    {
      float eu = 2.0f * v[0].x, ou = 2.0f * v[0].y;
      s0 = cmk(fmaf(eu, kz[0].x, ou * kz[0].y), fmaf(eu, kz[0].y, ou * kz[0].x));
      eu = 2.0f * v[4].x; ou = 2.0f * v[4].y;
      s4 = cmk(fmaf(eu, kz[4].x, -(ou * kz[4].y)), fmaf(eu, kz[4].y, ou * kz[4].x));
    }
    v[0] = csel(sp, s0, oP[0]);
    v[1] = oP[1];
    v[2] = oP[2];
    v[3] = oP[3];
    v[4] = csel(sp, s4, oP[4]);
    v[5] = csel(sp, oQ[3], oP[5]);
    v[6] = csel(sp, oQ[2], oP[6]);
    v[7] = csel(sp, oQ[1], oP[7]);
    v[8] = oQ[7];
    v[9] = oQ[6];
    v[10] = oQ[5];
    v[11] = oQ[4];
    v[12] = csel(sp, oP[4], oQ[3]);
    v[13] = csel(sp, oP[5], oQ[2]);
    v[14] = csel(sp, oP[6], oQ[1]);
    v[15] = csel(sp, oP[7], oQ[0]);
  }
  fft2048_inv_first(v, buf, f);
  fft_reg<16, 1>(v);
  u32* yr = yT + (size_t)d * (SEQ / 2);
#pragma unroll
  for (int a = 0; a < 8; ++a) __builtin_nontemporal_store(f2bf(v[a].x) | (f2bf(v[a].y) << 16), yr + 128 * a + t);
}

__device__ __forceinline__ void ytrans_block(const u16* __restrict__ yT, const u16* __restrict__ u,
                                             const u16* __restrict__ Dskip, u16* __restrict__ out, u16* tile,
                                             int bid, int tid) {
#pragma unroll
  for (int it = 0; it < 4; ++it) {
    int tl = bid * 4 + it;
    int l0 = (tl >> 4) * 64, d0 = (tl & 15) * 64;
    int r = tid >> 3, c = tid & 7;
    uint4 q = *reinterpret_cast<const uint4*>(yT + (size_t)(d0 + r) * SEQ + l0 + 8 * c);
    uint4 uq = *reinterpret_cast<const uint4*>(u + (size_t)(l0 + r) * DCH + d0 + 8 * c);
    uint4 dq = *reinterpret_cast<const uint4*>(Dskip + d0 + 8 * c);
    u32 w[4] = {q.x, q.y, q.z, q.w};
    if (it) __syncthreads();
#pragma unroll
    for (int j = 0; j < 4; ++j) {
      tile[(8 * c + 2 * j) * 72 + r] = (u16)(w[j] & 0xffffu);
      tile[(8 * c + 2 * j + 1) * 72 + r] = (u16)(w[j] >> 16);
    }
    __syncthreads();
    uint4 y = *reinterpret_cast<const uint4*>(tile + r * 72 + 8 * c);
    u32 yy[4] = {y.x, y.y, y.z, y.w}, uu[4] = {uq.x, uq.y, uq.z, uq.w}, dd[4] = {dq.x, dq.y, dq.z, dq.w};
    u32 oo[4];
#pragma unroll
    for (int j = 0; j < 4; ++j) {
      float o0 = bf2f(yy[j] & 0xffffu) + bfround(bf2f(uu[j] & 0xffffu) * bf2f(dd[j] & 0xffffu));
      float o1 = bf2f(yy[j] >> 16) + bfround(bf2f(uu[j] >> 16) * bf2f(dd[j] >> 16));
      oo[j] = f2bf(o0) | (f2bf(o1) << 16);
    }
    *reinterpret_cast<uint4*>(out + (size_t)(l0 + r) * (2 * DCH) + d0 + 8 * c) = make_uint4(oo[0], oo[1], oo[2], oo[3]);
  }
}

#define SM_CF (TWOFF + 260)

__global__ __launch_bounds__(256, 3) void k1(KParams P, float* Kc, const u16* x, cf* X1T, const u16* wr,
                                            const u16* wi, u32* WT, cf* TW, const u16* u, u16* uT) {
  __shared__ __attribute__((aligned(16))) u32 smw[4 * 32 * KREC];
  cf* sm = reinterpret_cast<cf*>(smw);
  int b = blockIdx.x, tid = threadIdx.x;
  int q = b / 3, r = b % 3;
  if (r < 2) {
    kgen_block(P, Kc, u, uT, smw, q * 2 + r, tid);
  } else if (q & 1) {
    if (q < 16) {
      int j = (q >> 1) * 256 + tid;
      float s, c;
      sincospif(-(float)j / 1024.0f, &s, &c);
      TW[j] = cmk(c, s);
    }
    brow_block(x, X1T, sm, q, tid);
  } else {
    WtRegs R;
    wt_load(wr, wi, R, q >> 1, tid);
    brow_block(x, X1T, sm, q, tid);
    __syncthreads();
    wt_finish(R, WT, smw, q >> 1, tid);
  }
}

__global__ __launch_bounds__(128, 3) void k2(cf* X1T, const u32* WT, const u32* uT, const float* Kc, const cf* TW,
                                            u32* yT) {
  __shared__ __attribute__((aligned(16))) cf sm[XBUF];
  int b = blockIdx.x, tid = threadIdx.x;
  if (b >= 1024) {
    int q = b - 1024;
    if (q == 0) bcol_block<true>(X1T, WT, TW, sm, q, tid);
    else bcol_block<false>(X1T, WT, TW, sm, q, tid);
  } else {
    aconv_block(uT, Kc, TW, yT, sm, b, tid);
  }
}

__global__ __launch_bounds__(512, 2) void k3(const cf* X1T, const u16* yT, const u16* u, const u16* Dskip, u16* out) {
  __shared__ __attribute__((aligned(16))) cf sm[SM_CF];
  int b = blockIdx.x, tid = threadIdx.x;
  if (b & 1) {
    browinv_block(X1T, out, sm, b >> 1, tid);
  } else {
    ytrans_block(yT, u, Dskip, out, reinterpret_cast<u16*>(sm), b >> 1, tid);
  }
}

extern "C" void kernel_launch(void* const* d_in, const int* in_sizes, int n_in, void* d_out, int out_size,
                              void* d_ws, size_t ws_size, hipStream_t stream) {
  const u16* u = (const u16*)d_in[0];
  KParams P;
  P.Are = (const u16*)d_in[1];
  P.Aim = (const u16*)d_in[2];
  P.Bre = (const u16*)d_in[3];
  P.Bim = (const u16*)d_in[4];
  P.Cre = (const u16*)d_in[5];
  P.Cim = (const u16*)d_in[6];
  P.logdt = (const u16*)d_in[7];
  const u16* Dskip = (const u16*)d_in[8];
  const u16* x = (const u16*)d_in[9];
  const u16* wr = (const u16*)d_in[10];
  const u16* wi = (const u16*)d_in[11];
  u16* out = (u16*)d_out;
  char* ws = (char*)d_ws;
  cf* X1T = (cf*)ws;
  u32* WT = (u32*)(ws + (size_t)8 * 1024 * 1024);
  float* Kc = (float*)(ws + (size_t)16 * 1024 * 1024);
  cf* TW = (cf*)(ws + (size_t)20 * 1024 * 1024);
  u16* uT = (u16*)(ws + (size_t)24 * 1024 * 1024);
  u16* yT = (u16*)(ws + (size_t)28 * 1024 * 1024);

  k1<<<768, 256, 0, stream>>>(P, Kc, x, X1T, wr, wi, WT, TW, u, uT);
  k2<<<1536, 128, 0, stream>>>(X1T, WT, (const u32*)uT, Kc, TW, (u32*)yT);
  k3<<<256, 512, 0, stream>>>(X1T, yT, u, Dskip, out);
}
```

```cpp
#include <hip/hip_runtime.h>
#include <hip/hip_bf16.h>
#include <type_traits>

#define SEQ 2048
#define DCH 1024
#define NST 128
#define DF 513

typedef __attribute__((ext_vector_type(2))) float cf;
typedef unsigned int u32;
typedef unsigned short u16;
using f32x16 = __attribute__((ext_vector_type(16))) float;

template <int I, int N, class F>
__device__ __forceinline__ void static_for(F&& f) {
  if constexpr (I < N) {
    f(std::integral_constant<int, I>{});
    static_for<I + 1, N>(f);
  }
}

__device__ __forceinline__ cf cmk(float x, float y) { cf r; r.x = x; r.y = y; return r; }
__device__ __forceinline__ cf cadd(cf a, cf b) { return a + b; }
__device__ __forceinline__ cf csub(cf a, cf b) { return a - b; }
__device__ __forceinline__ cf add_pi(cf a, cf b) {
  cf r;
  asm("v_pk_add_f32 %0, %1, %2 op_sel:[0,1] op_sel_hi:[1,0] neg_lo:[0,1]" : "=v"(r) : "v"(a), "v"(b));
  return r;
}
__device__ __forceinline__ cf add_mi(cf a, cf b) {
  cf r;
  asm("v_pk_add_f32 %0, %1, %2 op_sel:[0,1] op_sel_hi:[1,0] neg_hi:[0,1]" : "=v"(r) : "v"(a), "v"(b));
  return r;
}
__device__ __forceinline__ cf cmul(cf a, cf b) {
  cf t, r;
  asm("v_pk_mul_f32 %0, %1, %2 op_sel:[0,0] op_sel_hi:[0,1]" : "=v"(t) : "v"(a), "v"(b));
  asm("v_pk_fma_f32 %0, %1, %2, %3 op_sel:[1,1,0] op_sel_hi:[1,0,1] neg_lo:[0,1,0]" : "=v"(r) : "v"(a), "v"(b), "v"(t));
  return r;
}
__device__ __forceinline__ cf cmad(cf a, cf b, cf c) {
  cf t, r;
  asm("v_pk_fma_f32 %0, %1, %2, %3 op_sel:[0,0,0] op_sel_hi:[0,1,1]" : "=v"(t) : "v"(a), "v"(b), "v"(c));
  asm("v_pk_fma_f32 %0, %1, %2, %3 op_sel:[1,1,0] op_sel_hi:[1,0,1] neg_lo:[0,1,0]" : "=v"(r) : "v"(a), "v"(b), "v"(t));
  return r;
}
__device__ __forceinline__ cf cmulc(cf a, cf b) {
  cf t, r;
  asm("v_pk_mul_f32 %0, %1, %2 op_sel:[0,0] op_sel_hi:[1,0]" : "=v"(t) : "v"(a), "v"(b));
  asm("v_pk_fma_f32 %0, %1, %2, %3 op_sel:[1,1,0] op_sel_hi:[0,1,1] neg_hi:[1,0,0]" : "=v"(r) : "v"(a), "v"(b), "v"(t));
  return r;
}
__device__ __forceinline__ cf cmadc(cf a, cf b, cf c) {
  cf t, r;
  asm("v_pk_fma_f32 %0, %1, %2, %3 op_sel:[0,0,0] op_sel_hi:[1,0,1]" : "=v"(t) : "v"(a), "v"(b), "v"(c));
  asm("v_pk_fma_f32 %0, %1, %2, %3 op_sel:[1,1,0] op_sel_hi:[0,1,1] neg_hi:[1,0,0]" : "=v"(r) : "v"(a), "v"(b), "v"(t));
  return r;
}
__device__ __forceinline__ cf addc(cf a, cf b) {
  cf r;
  asm("v_pk_add_f32 %0, %1, %2 neg_hi:[0,1]" : "=v"(r) : "v"(a), "v"(b));
  return r;
}
__device__ __forceinline__ cf oddc(cf a, cf b) {
  cf r;
  asm("v_pk_add_f32 %0, %1, %2 op_sel:[1,1] op_sel_hi:[0,0] neg_hi:[1,0]" : "=v"(r) : "v"(a), "v"(b));
  return r;
}
__device__ __forceinline__ cf conj_add_pi(cf a, cf b) {
  cf r;
  asm("v_pk_add_f32 %0, %1, %2 op_sel:[0,1] op_sel_hi:[1,0] neg_hi:[1,0]" : "=v"(r) : "v"(a), "v"(b));
  return r;
}
__device__ __forceinline__ cf cmulk(cf a, cf cs) {
  cf t, r;
  asm("v_pk_mul_f32 %0, %1, %2 op_sel:[0,0] op_sel_hi:[1,0]" : "=v"(t) : "v"(a), "s"(cs));
  asm("v_pk_fma_f32 %0, %1, %2, %3 op_sel:[1,1,0] op_sel_hi:[0,1,1] neg_lo:[1,0,0]" : "=v"(r) : "v"(a), "s"(cs), "v"(t));
  return r;
}
__device__ __forceinline__ cf csqr(cf a) { return cmk(fmaf(a.x, a.x, -(a.y * a.y)), 2.0f * a.x * a.y); }

__device__ __forceinline__ float bf2f(u32 h) { return __uint_as_float(h << 16); }
__device__ __forceinline__ u32 f2bf(float f) {
  __hip_bfloat16 b = __float2bfloat16(f);
  return (u32)(*reinterpret_cast<u16*>(&b));
}
__device__ __forceinline__ float bfround(float f) { return bf2f(f2bf(f)); }

__device__ __forceinline__ void wave_sync() {
  __builtin_amdgcn_fence(__ATOMIC_SEQ_CST, "wavefront");
  __builtin_amdgcn_wave_barrier();
  __builtin_amdgcn_fence(__ATOMIC_SEQ_CST, "wavefront");
}

constexpr double COS64_TAB[17] = {
    1.0, 0.99518472667219688624, 0.98078528040323044913, 0.95694033573220886494,
    0.92387953251128675613, 0.88192126434835502971, 0.83146961230254523708,
    0.77301045336273696081, 0.70710678118654752440, 0.63439328416364549822,
    0.55557023301960222474, 0.47139673682599764856, 0.38268343236508977173,
    0.29028467725446236764, 0.19509032201612826785, 0.09801714032956060199, 0.0};
constexpr double cos64(int j) {
  j = ((j % 64) + 64) % 64;
  if (j > 32) j = 64 - j;
  return (j <= 16) ? COS64_TAB[j] : -COS64_TAB[32 - j];
}
constexpr double sin64(int j) { return cos64(j - 16); }

template <int J, int DIR>
__device__ __forceinline__ cf tw64(cf a) {
  constexpr int j = ((J % 64) + 64) % 64;
  if constexpr (j == 0) {
    return a;
  } else if constexpr (j == 32) {
    return -a;
  } else if constexpr (j == 16) {
    return (DIR > 0) ? cmk(-a.y, a.x) : cmk(a.y, -a.x);
  } else if constexpr (j == 48) {
    return (DIR > 0) ? cmk(a.y, -a.x) : cmk(-a.y, a.x);
  } else {
    constexpr float c = (float)cos64(j);
    constexpr float s = (float)(DIR * sin64(j));
    return cmulk(a, cmk(c, s));
  }
}

template <int N, int DIR, int S>
__device__ __forceinline__ void fft_rec(const cf* in, cf* out) {
  if constexpr (N == 1) {
    out[0] = in[0];
  } else if constexpr (N == 2) {
    cf a = in[0], b = in[S];
    out[0] = cadd(a, b);
    out[1] = csub(a, b);
  } else {
    constexpr int Q = N / 4;
    cf t0[Q], t1[Q], t2[Q], t3[Q];
    fft_rec<Q, DIR, 4 * S>(in, t0);
    fft_rec<Q, DIR, 4 * S>(in + S, t1);
    fft_rec<Q, DIR, 4 * S>(in + 2 * S, t2);
    fft_rec<Q, DIR, 4 * S>(in + 3 * S, t3);
    static_for<0, Q>([&](auto ic) {
      constexpr int k = decltype(ic)::value;
      constexpr int M = 64 / N;
      cf a = t0[k];
      cf b = tw64<k * M, DIR>(t1[k]);
      cf d = tw64<3 * k * M, DIR>(t3[k]);
      cf s0, s1;
      if constexpr (2 * k * M == 16) {
        s0 = (DIR > 0) ? add_pi(a, t2[k]) : add_mi(a, t2[k]);
        s1 = (DIR > 0) ? add_mi(a, t2[k]) : add_pi(a, t2[k]);
      } else {
        cf c = tw64<2 * k * M, DIR>(t2[k]);
        s0 = cadd(a, c);
        s1 = csub(a, c);
      }
      cf s2 = cadd(b, d), s3 = csub(b, d);
      out[k] = cadd(s0, s2);
      out[k + Q] = (DIR > 0) ? add_pi(s1, s3) : add_mi(s1, s3);
      out[k + 2 * Q] = csub(s0, s2);
      out[k + 3 * Q] = (DIR > 0) ? add_mi(s1, s3) : add_pi(s1, s3);
    });
  }
}

template <int N, int DIR>
__device__ __forceinline__ void fft_reg(cf* v) {
  cf o[N];
  fft_rec<N, DIR, 1>(v, o);
#pragma unroll
  for (int i = 0; i < N; ++i) v[i] = o[i];
}

template <int DIR>
__device__ __forceinline__ void fft16_halfzero(cf* v) {
  cf a[8], b[8], oa[8], ob[8];
  static_for<0, 8>([&](auto ic) {
    constexpr int n = decltype(ic)::value;
    a[n] = v[n];
    b[n] = tw64<4 * n, DIR>(v[n]);
  });
  fft_rec<8, DIR, 1>(a, oa);
  fft_rec<8, DIR, 1>(b, ob);
#pragma unroll
  for (int k = 0; k < 8; ++k) {
    v[2 * k] = oa[k];
    v[2 * k + 1] = ob[k];
  }
}

struct Tw16 { cf w[4]; };

__device__ __forceinline__ Tw16 make_tw16(int n, int logN) {
  Tw16 b;
#pragma unroll
  for (int t = 0; t < 4; ++t) {
    int e = (n << t) & ((1 << logN) - 1);
    float s, c;
    sincospif(-2.0f * (float)e / (float)(1 << logN), &s, &c);
    b.w[t] = cmk(c, s);
  }
  return b;
}

__device__ __forceinline__ Tw16 load_tw16(const cf* __restrict__ TW, int n) {
  Tw16 b;
#pragma unroll
  for (int t = 0; t < 4; ++t) b.w[t] = TW[(n << t) & 2047];
  return b;
}

template <bool CONJ>
__device__ __forceinline__ void apply_tw16(cf* v, const Tw16& b) {
  cf p[16];
  p[1] = b.w[0];
  p[2] = b.w[1];
  p[4] = b.w[2];
  p[8] = b.w[3];
  asm volatile("" : "+v"(p[1]), "+v"(p[2]), "+v"(p[4]), "+v"(p[8]));
  static_for<3, 16>([&](auto ic) {
    constexpr int k = decltype(ic)::value;
    constexpr int top = (k >= 8) ? 8 : (k >= 4) ? 4 : 2;
    if constexpr (k != top) p[k] = cmul(p[k - top], p[top]);
  });
#pragma unroll
  for (int k = 1; k < 16; ++k) v[k] = CONJ ? cmulc(v[k], p[k]) : cmul(v[k], p[k]);
}

__device__ __forceinline__ void swap16(cf& a, cf& b) {
  asm volatile("s_nop 1" : "+v"(a), "+v"(b));
  auto r0 = __builtin_amdgcn_permlane16_swap(__float_as_uint(a.x), __float_as_uint(b.x), false, false);
  auto r1 = __builtin_amdgcn_permlane16_swap(__float_as_uint(a.y), __float_as_uint(b.y), false, false);
  a.x = __uint_as_float(r0[0]);
  b.x = __uint_as_float(r0[1]);
  a.y = __uint_as_float(r1[0]);
  b.y = __uint_as_float(r1[1]);
}

#define XP 152
#define XBUF (16 * XP)
typedef volatile __attribute__((address_space(3))) cf* ldsp;
__device__ __forceinline__ cf lds_ld(const cf* p) { return *(ldsp)(p); }
__device__ __forceinline__ void lds_st(cf* p, cf a) { *(ldsp)(p) = a; }

struct FCtx {
  Tw16 ta, tb;
  int t, offA, offB, kA, kB;
};
__device__ __forceinline__ FCtx make_fctx(const cf* __restrict__ TW, int t) {
  FCtx f;
  int k0 = t >> 3, c = t & 7;
  f.t = t;
  f.ta = load_tw16(TW, t);
  f.tb = load_tw16(TW, 16 * c);
  int k0m = (16 - k0) & 15;
  int k1m = (k0 != 0) ? (15 - c) : ((c != 0) ? (16 - c) : 8);
  f.offA = k0 * XP + 9 * c;
  f.offB = k0m * XP + 9 * k1m;
  f.kA = k0 + 16 * c;
  f.kB = k0m + 16 * k1m;
  return f;
}

__device__ __forceinline__ void fft2048_fwd_rest(cf* v, cf* buf, const FCtx& f) {
  apply_tw16<false>(v, f.ta);
  __syncthreads();
#pragma unroll
  for (int k0 = 0; k0 < 16; ++k0) lds_st(buf + k0 * XP + f.t, v[k0]);
  __syncthreads();
  cf* rb = buf + (f.t >> 3) * XP + (f.t & 7);
#pragma unroll
  for (int b = 0; b < 16; ++b) v[b] = lds_ld(rb + 8 * b);
  fft_reg<16, -1>(v);
  apply_tw16<false>(v, f.tb);
#pragma unroll
  for (int k1 = 0; k1 < 16; ++k1) lds_st(rb + 9 * k1, v[k1]);
  __syncthreads();
#pragma unroll
  for (int j = 0; j < 8; ++j) v[j] = lds_ld(buf + f.offA + j);
#pragma unroll
  for (int j = 0; j < 8; ++j) v[8 + j] = lds_ld(buf + f.offB + j);
  fft_reg<8, -1>(v);
  fft_reg<8, -1>(v + 8);
}

__device__ __forceinline__ void fft2048_inv_first(cf* v, cf* buf, const FCtx& f) {
  fft_reg<8, 1>(v);
  fft_reg<8, 1>(v + 8);
  __syncthreads();
#pragma unroll
  for (int j = 0; j < 8; ++j) lds_st(buf + f.offA + j, v[j]);
#pragma unroll
  for (int j = 0; j < 8; ++j) lds_st(buf + f.offB + j, v[8 + j]);
  __syncthreads();
  cf* rb = buf + (f.t >> 3) * XP + (f.t & 7);
#pragma unroll
  for (int k1 = 0; k1 < 16; ++k1) v[k1] = lds_ld(rb + 9 * k1);
  apply_tw16<true>(v, f.tb);
  fft_reg<16, 1>(v);
#pragma unroll
  for (int b = 0; b < 16; ++b) lds_st(rb + 8 * b, v[b]);
  __syncthreads();
#pragma unroll
  for (int kk = 0; kk < 16; ++kk) v[kk] = lds_ld(buf + kk * XP + f.t);
  apply_tw16<true>(v, f.ta);
}

#define KC_PITCH 648
#define KREC 104
using u32x4 = __attribute__((ext_vector_type(4))) unsigned;
using u32x2 = __attribute__((ext_vector_type(2))) unsigned;
using bf16x8 = __attribute__((ext_vector_type(8))) __bf16;

__device__ __forceinline__ cf cexp_scaled(float ar, float ai, float sc) {
  float e = expf(ar * sc);
  float s, c;
  sincosf(ai * sc, &s, &c);
  return cmk(e * c, e * s);
}
__device__ __forceinline__ u32 cvtpk(float a, float b) {
  u32 r;
  asm("v_cvt_pk_bf16_f32 %0, %1, %2" : "=v"(r) : "v"(a), "v"(b));
  return r;
}
__device__ __forceinline__ u32x2 split2(cf g) {
  u32x2 o;
  o.x = cvtpk(g.x, g.y);
  cf h = cmk(__uint_as_float(o.x << 16), __uint_as_float(o.x & 0xffff0000u));
  cf r = g - h;
  o.y = cvtpk(r.x, r.y);
  return o;
}

struct KParams { const u16 *Are, *Aim, *Bre, *Bim, *Cre, *Cim, *logdt; };

__device__ __forceinline__ void kgen_block(const KParams& P, float* __restrict__ Kc, u32* lds_all, int bid, int tid) {
  int wave = tid >> 6, lane = tid & 63;
  int d = bid * 2 + (wave >> 1);
  u32* lds = lds_all + wave * 32 * KREC;
  float dt = bfround(expf(bf2f(P.logdt[d])));
  int idx = d * NST + (wave & 1) * 64 + lane;
  float ar = dt * bf2f(P.Are[idx]), ai = dt * bf2f(P.Aim[idx]);
  cf dB = cmk(dt * bf2f(P.Bre[idx]), dt * bf2f(P.Bim[idx]));
  cf C = cmk(bf2f(P.Cre[idx]), bf2f(P.Cim[idx]));
  cf c0 = cmul(C, dB);
  cf e[12];
  e[0] = cexp_scaled(ar, ai, 1.0f);
  e[4] = cexp_scaled(ar, ai, 16.0f);
  e[8] = cexp_scaled(ar, ai, 256.0f);
  e[1] = csqr(e[0]); e[2] = csqr(e[1]); e[3] = csqr(e[2]);
  e[5] = csqr(e[4]); e[6] = csqr(e[5]); e[7] = csqr(e[6]);
  e[9] = csqr(e[8]); e[10] = csqr(e[9]); e[11] = csqr(e[10]);
  typedef volatile __attribute__((address_space(3))) u32x2* lp2;
  float s2048 = fmaf(c0.x, e[11].x, -(c0.y * e[11].y));
  {
    auto dpp = [](float x, auto ctl, auto rm) {
      return __int_as_float(__builtin_amdgcn_update_dpp(0, __float_as_int(x), decltype(ctl)::value, decltype(rm)::value, 0xf, true));
    };
    using std::integral_constant;
    s2048 += dpp(s2048, integral_constant<int, 0x111>{}, integral_constant<int, 0xf>{});
    s2048 += dpp(s2048, integral_constant<int, 0x112>{}, integral_constant<int, 0xf>{});
    s2048 += dpp(s2048, integral_constant<int, 0x114>{}, integral_constant<int, 0xf>{});
    s2048 += dpp(s2048, integral_constant<int, 0x118>{}, integral_constant<int, 0xf>{});
    s2048 += dpp(s2048, integral_constant<int, 0x142>{}, integral_constant<int, 0xa>{});
    s2048 += dpp(s2048, integral_constant<int, 0x143>{}, integral_constant<int, 0xc>{});
  }
  u32x2 Gw[20], Ew[32];
  const int rot = 2 * ((lane >> 2) & 3);
  u32* rec0 = lds + (lane & 31) * KREC;
  u32* rec = rec0 + rot;
  {
    cf G[20];
    G[0] = cmk(c0.x, -c0.y);
    G[1] = cmulc(G[0], e[0]);
    G[2] = cmulc(G[0], e[1]);
    G[3] = cmulc(G[1], e[1]);
    G[4] = cmulc(G[0], e[2]);
    G[5] = cmulc(G[1], e[2]);
    G[6] = cmulc(G[2], e[2]);
    G[7] = cmulc(G[3], e[2]);
    G[8] = cmulc(G[0], e[8]);
    G[9] = cmulc(G[2], e[8]);
    G[10] = cmulc(G[4], e[8]);
    G[11] = cmulc(G[6], e[8]);
    G[12] = cmulc(G[0], e[9]);
    G[13] = cmulc(G[4], e[9]);
    G[14] = cmulc(G[8], e[9]);
    G[15] = cmulc(G[10], e[9]);
    G[16] = cmulc(G[0], e[10]);
    G[17] = cmulc(G[8], e[10]);
    G[18] = cmulc(G[12], e[10]);
    G[19] = cmulc(G[14], e[10]);
#pragma unroll
    for (int i = 0; i < 20; ++i) Gw[i] = split2(G[i]);
  }
  if (lane < 32) {
#pragma unroll
    for (int i = 0; i < 20; ++i) *(lp2)(rec + 2 * i) = Gw[i];
  }
  {
    cf E[32];
    E[0] = cmk(1.f, 0.f);
    E[1] = e[3]; E[2] = e[4]; E[4] = e[5]; E[8] = e[6]; E[16] = e[7];
    static_for<3, 32>([&](auto ic) {
      constexpr int k = decltype(ic)::value;
      constexpr int top = (k >= 16) ? 16 : (k >= 8) ? 8 : (k >= 4) ? 4 : 2;
      if constexpr (k != top) E[k] = cmul(E[k - top], E[top]);
    });
    Ew[0].x = 0x3f80u;
    Ew[0].y = 0u;
#pragma unroll
    for (int i = 1; i < 32; ++i) Ew[i] = split2(E[i]);
  }
  f32x16 acc;
#pragma unroll
  for (int i = 0; i < 16; ++i) acc[i] = 0.f;
  const u32* base = lds + (2 * (lane >> 5)) * KREC;
  int offB[4];
#pragma unroll
  for (int q = 0; q < 4; ++q) {
    int w = 40 + 2 * (lane & 31) + 2 * q;
    offB[q] = (w >= KREC) ? w - KREC : w;
  }
#pragma unroll
  for (int ch = 0; ch < 2; ++ch) {
    if ((lane >> 5) == ch) {
      if (ch) {
#pragma unroll
        for (int i = 0; i < 20; ++i) *(lp2)(rec + 2 * i) = Gw[i];
      }
#pragma unroll
      for (int i = 0; i < 29; ++i) *(lp2)(rec + 40 + 2 * i) = Ew[i];
#pragma unroll
      for (int i = 29; i < 32; ++i) {
        int w = 40 + 2 * i + rot;
        *(lp2)(rec0 + ((w >= KREC) ? w - KREC : w)) = Ew[i];
      }
    }
#pragma unroll
    for (int m = 0; m < 8; ++m) {
      const u32* r0 = base + 4 * m * KREC;
      const u32* ra = r0 + 2 * (lane & 31) + 2 * (m & 3);
      const u32* rb = r0 + offB[m & 3];
      u32x2 a0 = *(lp2)(ra), a1 = *(lp2)(ra + KREC), b0 = *(lp2)(rb), b1 = *(lp2)(rb + KREC);
      u32x4 A = {a0.x, a0.y, a1.x, a1.y}, A2 = {a0.y, a0.x, a1.y, a1.x}, B = {b0.x, b0.y, b1.x, b1.y};
      acc = __builtin_amdgcn_mfma_f32_32x32x16_bf16(__builtin_bit_cast(bf16x8, A), __builtin_bit_cast(bf16x8, B), acc, 0, 0, 0);
      acc = __builtin_amdgcn_mfma_f32_32x32x16_bf16(__builtin_bit_cast(bf16x8, A2), __builtin_bit_cast(bf16x8, B), acc, 0, 0, 0);
    }
  }
  float* red = reinterpret_cast<float*>(lds_all + (wave | 1) * 32 * KREC);
  wave_sync();
  if (wave & 1) {
#pragma unroll
    for (int r = 0; r < 16; ++r) red[r * 64 + lane] = acc[r];
    if (lane == 63) red[1024] = s2048;
  }
  __syncthreads();
  if (!(wave & 1)) {
    int col = lane & 31, hh = lane >> 5;
    float* dst = Kc + (size_t)d * KC_PITCH;
#pragma unroll
    for (int r = 0; r < 16; ++r) {
      int row = (r & 3) + 8 * (r >> 2) + 4 * hh;
      int p = -1;
      if (row < 8) p = row + 8 * col;
      else if (row < 12) p = 256 + (row - 8) + 4 * col;
      else if (row < 16) p = 384 + ((row - 12) & 1) + 64 * ((row - 12) >> 1) + 2 * col;
      else if (row < 20) p = 512 + 32 * (row - 16) + col;
      if (p >= 0) dst[p] = acc[r] + red[r * 64 + lane];
    }
    if (lane == 63) dst[640] = s2048 + red[1024];
  }
}

__device__ __forceinline__ void wt_block(const u16* __restrict__ wr, const u16* __restrict__ wi, u32* __restrict__ WT,
                                         u32* tile, int bid, int tid) {
  int s0 = bid * 16;
  const u32* pr = reinterpret_cast<const u32*>(wr + (size_t)s0 * DF);
  const u32* pi = reinterpret_cast<const u32*>(wi + (size_t)s0 * DF);
  u32 a[17], b[17];
#pragma unroll
  for (int i = 0; i < 17; ++i) {
    int w = min(tid + 256 * i, 8 * DF - 1);
    a[i] = pr[w];
    b[i] = pi[w];
  }
#pragma unroll
  for (int i = 0; i < 17; ++i) {
    int w = tid + 256 * i;
    if (w < 8 * DF) {
      int e0 = 2 * w;
      int r = e0 / DF, k = e0 - r * DF;
      tile[k * 17 + r] = (a[i] & 0xffffu) | (b[i] << 16);
      int k1 = k + 1, r1 = r;
      if (k1 == DF) { k1 = 0; r1 = r + 1; }
      tile[k1 * 17 + r1] = (a[i] >> 16) | (b[i] & 0xffff0000u);
    }
  }
  __syncthreads();
  int sx = tid & 15, ky = tid >> 4;
#pragma unroll
  for (int i = 0; i < 33; ++i) {
    int k = ky + 16 * i;
    if (k < DF) __builtin_nontemporal_store(tile[k * 17 + sx], WT + (size_t)k * SEQ + s0 + sx);
  }
}

#define RP 34
#define RRS (16 * RP)
#define ZP 517
#define TWOFF (16 * RRS)

__device__ __forceinline__ void brow_block(const u16* __restrict__ x, cf* __restrict__ X1T, cf* sm, int bid, int tid) {
  cf* tw = sm + 8 * RRS;
  int s0 = bid * 8;
  {
    float s, c;
    sincospif(-(float)tid / 512.0f, &s, &c);
    tw[tid] = cmk(c, s);
    if (tid == 0) tw[256] = cmk(0.f, -1.f);
  }
  int row = tid >> 5, t = tid & 31;
  cf v[16];
  const u32* xr = reinterpret_cast<const u32*>(x + (size_t)(s0 + row) * DCH);
#pragma unroll
  for (int a = 0; a < 16; ++a) {
    u32 w = xr[32 * a + t];
    v[a] = cmk(bf2f(w & 0xffffu), bf2f(w >> 16));
  }
  Tw16 tb = make_tw16(t, 9);
  fft_reg<16, -1>(v);
  apply_tw16<false>(v, tb);
  cf* rb = sm + row * RRS;
  {
    int pos = (t & 1) * 16 + (t >> 1);
#pragma unroll
    for (int k0 = 0; k0 < 16; ++k0) rb[k0 * RP + pos] = v[k0];
  }
  wave_sync();
  int k0 = t & 15, h = t >> 4;
  {
    const float4* p = reinterpret_cast<const float4*>(rb + k0 * RP + h * 16);
#pragma unroll
    for (int m = 0; m < 8; ++m) {
      float4 q = p[m];
      v[2 * m] = cmk(q.x, q.y);
      v[2 * m + 1] = cmk(q.z, q.w);
    }
  }
  fft_reg<16, -1>(v);
  bool hi = h != 0;
  static_for<0, 8>([&](auto ic) {
    constexpr int j = decltype(ic)::value;
    cf a = v[j], b = v[j + 8];
    swap16(a, b);
    cf br = cmk(hi ? b.y : b.x, hi ? -b.x : b.y);
    br = tw64<2 * j, -1>(br);
    v[j] = cadd(a, br);
    v[j + 8] = csub(a, br);
  });
  __syncthreads();
  cf* zs = sm + row * ZP;
#pragma unroll
  for (int r = 0; r < 16; ++r) zs[k0 + 16 * ((r & 7) + 8 * h + 16 * (r >> 3))] = v[r];
  __syncthreads();
  int r = tid & 7, kk = tid >> 3;
  const cf* zr = sm + r * ZP;
#pragma unroll
  for (int i = 0; i < 9; ++i) {
    int k = kk + 32 * i;
    if (k > 256) break;
    cf a = zr[k];
    cf b = zr[(512 - k) & 511];
    if (k == 0) {
      __builtin_nontemporal_store(cmk(a.x + a.y, a.x - a.y), X1T + (size_t)0 * SEQ + s0 + r);
    } else {
      cf w = tw[k];
      cf ev = cmk(0.5f * (a.x + b.x), 0.5f * (a.y - b.y));
      cf od = cmk(0.5f * (a.y + b.y), -0.5f * (a.x - b.x));
      cf wo = cmul(od, w);
      __builtin_nontemporal_store(cadd(ev, wo), X1T + (size_t)k * SEQ + s0 + r);
      if (k != 256) __builtin_nontemporal_store(cmk(ev.x - wo.x, -(ev.y - wo.y)), X1T + (size_t)(512 - k) * SEQ + s0 + r);
    }
  }
}

__device__ __forceinline__ void browinv_block(const cf* __restrict__ X1T, u16* __restrict__ out, cf* sm, int bid, int tid) {
  cf* tw = sm + TWOFF;
  int s0 = bid * 16;
  {
    int r = tid & 15, kk = tid >> 4;
    cf a[9], b[9];
#pragma unroll
    for (int i = 0; i < 9; ++i) {
      int k = kk + 32 * i;
      if (k <= 256) {
        a[i] = X1T[(size_t)k * SEQ + s0 + r];
        b[i] = X1T[(size_t)((512 - k) & 511) * SEQ + s0 + r];
      }
    }
    if (tid <= 256) {
      float s, c;
      sincospif(-(float)tid / 512.0f, &s, &c);
      tw[tid] = cmk(c, s);
    }
    __syncthreads();
    cf* zr = sm + r * ZP;
#pragma unroll
    for (int i = 0; i < 9; ++i) {
      int k = kk + 32 * i;
      if (k <= 256) {
        if (k == 0) {
          zr[0] = cmk(a[i].x + a[i].y, a[i].x - a[i].y);
        } else {
          cf w = tw[k];
          cf ev = cmk(a[i].x + b[i].x, a[i].y - b[i].y);
          cf df = cmk(a[i].x - b[i].x, a[i].y + b[i].y);
          cf od = cmulc(df, w);
          zr[k] = cmk(ev.x - od.y, ev.y + od.x);
          if (k != 256) zr[512 - k] = cmk(ev.x + od.y, -ev.y + od.x);
        }
      }
    }
  }
  __syncthreads();
  int row = tid >> 5, t = tid & 31;
  int k0 = t & 15, h = t >> 4;
  cf v[16];
  {
    const cf* zs = sm + row * ZP;
#pragma unroll
    for (int r = 0; r < 16; ++r) v[r] = zs[k0 + 16 * ((r & 7) + 8 * h + 16 * (r >> 3))];
  }
  __syncthreads();
  bool hi = h != 0;
  static_for<0, 8>([&](auto ic) {
    constexpr int j = decltype(ic)::value;
    cf a = cadd(v[j], v[j + 8]);
    cf d = csub(v[j], v[j + 8]);
    cf dr = cmk(hi ? -d.y : d.x, hi ? d.x : d.y);
    dr = tw64<2 * j, 1>(dr);
    swap16(a, dr);
    v[j] = a;
    v[j + 8] = dr;
  });
  fft_reg<16, 1>(v);
  cf* rb = sm + row * RRS;
  {
    float4* p = reinterpret_cast<float4*>(rb + k0 * RP + h * 16);
#pragma unroll
    for (int m = 0; m < 8; ++m) p[m] = make_float4(v[2 * m].x, v[2 * m].y, v[2 * m + 1].x, v[2 * m + 1].y);
  }
  wave_sync();
  {
    int pos = (t & 1) * 16 + (t >> 1);
#pragma unroll
    for (int kk = 0; kk < 16; ++kk) v[kk] = rb[kk * RP + pos];
  }
  Tw16 tb = make_tw16(t, 9);
  apply_tw16<true>(v, tb);
  fft_reg<16, 1>(v);
  u32* orow = reinterpret_cast<u32*>(out + (size_t)(s0 + row) * (2 * DCH) + DCH);
#pragma unroll
  for (int a = 0; a < 16; ++a) orow[32 * a + t] = f2bf(v[a].x) | (f2bf(v[a].y) << 16);
}

__device__ __forceinline__ cf ldw(const u32* p, int i) {
  u32 w = p[i];
  return cmk(bf2f(w & 0xffffu), bf2f(w >> 16));
}

template <bool SPECIAL>
__device__ __forceinline__ void bcol_block(cf* __restrict__ X1T, const u32* __restrict__ WT, const cf* __restrict__ TW,
                                           cf* buf, int col, int t) {
  cf v[16];
  cf* xc = X1T + (size_t)col * SEQ;
#pragma unroll
  for (int a = 0; a < 16; ++a) v[a] = xc[128 * a + t];
  FCtx f = make_fctx(TW, t);
  const float sc = 1.0f / (2048.0f * 1024.0f);
  const u32* wc = WT + (size_t)col * SEQ;
  u32 wq[16];
  if (!SPECIAL) {
#pragma unroll
    for (int j = 0; j < 8; ++j) {
      wq[j] = wc[f.kA + 256 * j];
      wq[8 + j] = wc[f.kB + 256 * j];
    }
  }
  fft_reg<16, -1>(v);
  fft2048_fwd_rest(v, buf, f);
  if (!SPECIAL) {
#pragma unroll
    for (int r = 0; r < 16; ++r) {
      cf w = cmk(bf2f(wq[r] & 0xffffu) * sc, bf2f(wq[r] >> 16) * sc);
      v[r] = cmul(v[r], w);
    }
  } else {
    __syncthreads();
#pragma unroll
    for (int j = 0; j < 8; ++j) {
      buf[f.kA + 256 * j] = v[j];
      buf[f.kB + 256 * j] = v[8 + j];
    }
    __syncthreads();
    const u32* w0 = WT;
    const u32* w1 = WT + (size_t)512 * SEQ;
#pragma unroll 1
    for (int k = t; k <= 1024; k += 128) {
      int km = (2048 - k) & 2047;
      cf a = buf[k], b = buf[km];
      cf A = cmk(0.5f * (a.x + b.x), 0.5f * (a.y - b.y));
      cf B = cmk(0.5f * (a.y + b.y), -0.5f * (a.x - b.x));
      cf p = ldw(w0, k), q = ldw(w0, km);
      cf wh0 = cmk(0.5f * sc * (p.x + q.x), 0.5f * sc * (p.y - q.y));
      p = ldw(w1, k); q = ldw(w1, km);
      cf wh1 = cmk(0.5f * sc * (p.x + q.x), 0.5f * sc * (p.y - q.y));
      cf h0 = cmul(A, wh0), h1 = cmul(B, wh1);
      buf[km] = cmk(h0.x + h1.y, -h0.y + h1.x);
      buf[k] = cmk(h0.x - h1.y, h0.y + h1.x);
    }
    __syncthreads();
#pragma unroll
    for (int j = 0; j < 8; ++j) {
      v[j] = buf[f.kA + 256 * j];
      v[8 + j] = buf[f.kB + 256 * j];
    }
  }
  fft2048_inv_first(v, buf, f);
  fft_reg<16, 1>(v);
#pragma unroll
  for (int a = 0; a < 16; ++a) __builtin_nontemporal_store(v[a], xc + 128 * a + t);
}

__device__ __forceinline__ int kpos(int l, int s) {
  if (s == 0) return l;
  int half = 1 << (s - 1);
  int q = (l + half - 1 + ((l >> s) & 1)) >> s;
  return 128 * (s + 1) + q - 128;
}
__device__ __forceinline__ cf csel(bool c, cf a, cf b) { return cmk(c ? a.x : b.x, c ? a.y : b.y); }

__device__ __forceinline__ void aconv_block(const u32* __restrict__ uT, const float* __restrict__ Kc,
                                            const cf* __restrict__ TW, u32* __restrict__ yT, cf* buf, int d, int t) {
  cf v[16], kz[16];
  u32 uw[8];
  {
    const float* kl = Kc + (size_t)d * KC_PITCH;
    static_for<0, 8>([&](auto ic) {
      constexpr int a = decltype(ic)::value;
      constexpr int s = (a < 1) ? 0 : (a < 2) ? 1 : (a < 4) ? 2 : 3;
      int l = 256 * a + 2 * t;
      kz[a] = cmk(kl[kpos(l, s)], kl[kpos(l + 1, s)]);
    });
    const u32* ur = uT + (size_t)d * (SEQ / 2);
#pragma unroll
    for (int a = 0; a < 8; ++a) uw[a] = ur[128 * a + t];
  }
  FCtx f = make_fctx(TW, t);
  cf L = TW[f.kA];
  cf L2 = TW[t == 0 ? 128 : f.kA];
  fft16_halfzero<-1>(kz);
  fft2048_fwd_rest(kz, buf, f);
#pragma unroll
  for (int a = 0; a < 8; ++a) v[a] = cmk(bf2f(uw[a] & 0xffffu), bf2f(uw[a] >> 16)) * (0.5f / 2048.0f);
  fft16_halfzero<-1>(v);
  fft2048_fwd_rest(v, buf, f);
  {
    const bool sp = (t == 0);
    cf oP[8], oQ[8];
    static_for<0, 8>([&](auto ic) {
      constexpr int j = decltype(ic)::value;
      cf a, b, ka, kb;
      if constexpr (j == 0) {
        a = v[0]; b = v[15]; ka = kz[0]; kb = kz[15];
      } else if constexpr (j < 4) {
        a = v[j]; ka = kz[j];
        b = csel(sp, v[8 - j], v[15 - j]);
        kb = csel(sp, kz[8 - j], kz[15 - j]);
      } else {
        a = csel(sp, v[8 + j], v[j]);
        ka = csel(sp, kz[8 + j], kz[j]);
        b = v[15 - j]; kb = kz[15 - j];
      }
      cf eu = addc(a, b);
      cf ou = oddc(a, b) * 0.5f;
      cf ek = addc(ka, kb);
      cf ok = oddc(ka, kb);
      cf w = tw64<8 * j, -1>((j < 4) ? L : L2);
      cf wo = cmul(ok, w);
      cf T = add_pi(wo, ek);
      cf T2 = conj_add_pi(wo, ek);
      oP[j] = cmad(ou, T, cmul(eu, ka));
      oQ[j] = cmadc(T2, ou, cmulc(kb, eu));
    });
    cf s0, s4;
    {
      float eu = 2.0f * v[0].x, ou = 2.0f * v[0].y;
      s0 = cmk(fmaf(eu, kz[0].x, ou * kz[0].y), fmaf(eu, kz[0].y, ou * kz[0].x));
      eu = 2.0f * v[4].x; ou = 2.0f * v[4].y;
      s4 = cmk(fmaf(eu, kz[4].x, -(ou * kz[4].y)), fmaf(eu, kz[4].y, ou * kz[4].x));
    }
    v[0] = csel(sp, s0, oP[0]);
    v[1] = oP[1];
    v[2] = oP[2];
    v[3] = oP[3];
    v[4] = csel(sp, s4, oP[4]);
    v[5] = csel(sp, oQ[3], oP[5]);
    v[6] = csel(sp, oQ[2], oP[6]);
    v[7] = csel(sp, oQ[1], oP[7]);
    v[8] = oQ[7];
    v[9] = oQ[6];
    v[10] = oQ[5];
    v[11] = oQ[4];
    v[12] = csel(sp, oP[4], oQ[3]);
    v[13] = csel(sp, oP[5], oQ[2]);
    v[14] = csel(sp, oP[6], oQ[1]);
    v[15] = csel(sp, oP[7], oQ[0]);
  }
  fft2048_inv_first(v, buf, f);
  fft_reg<16, 1>(v);
  u32* yr = yT + (size_t)d * (SEQ / 2);
#pragma unroll
  for (int a = 0; a < 8; ++a) __builtin_nontemporal_store(f2bf(v[a].x) | (f2bf(v[a].y) << 16), yr + 128 * a + t);
}

__device__ __forceinline__ void utrans_block(const u16* __restrict__ u, u16* __restrict__ uT, u16* tile, int bid, int tid) {
  uint4 q[2][2];
  int r = tid >> 3, c = tid & 7;
#pragma unroll
  for (int it = 0; it < 2; ++it) {
    int tl = bid * 2 + it;
    int l0 = (tl >> 4) * 64, d0 = (tl & 15) * 64;
#pragma unroll
    for (int h = 0; h < 2; ++h)
      q[it][h] = *reinterpret_cast<const uint4*>(u + (size_t)(l0 + r + 32 * h) * DCH + d0 + 8 * c);
  }
#pragma unroll
  for (int it = 0; it < 2; ++it) {
    u16* tl_ = tile + it * 64 * 72;
#pragma unroll
    for (int h = 0; h < 2; ++h) {
      u32 w[4] = {q[it][h].x, q[it][h].y, q[it][h].z, q[it][h].w};
#pragma unroll
      for (int j = 0; j < 4; ++j) {
        tl_[(8 * c + 2 * j) * 72 + r + 32 * h] = (u16)(w[j] & 0xffffu);
        tl_[(8 * c + 2 * j + 1) * 72 + r + 32 * h] = (u16)(w[j] >> 16);
      }
    }
  }
  __syncthreads();
#pragma unroll
  for (int it = 0; it < 2; ++it) {
    int tl = bid * 2 + it;
    int l0 = (tl >> 4) * 64, d0 = (tl & 15) * 64;
    const u16* tl_ = tile + it * 64 * 72;
#pragma unroll
    for (int h = 0; h < 2; ++h) {
      uint4 o = *reinterpret_cast<const uint4*>(tl_ + (r + 32 * h) * 72 + 8 * c);
      { u32x4 oo = {o.x,o.y,o.z,o.w}; __builtin_nontemporal_store(oo, reinterpret_cast<u32x4*>(uT + (size_t)(d0 + r + 32 * h) * SEQ + l0 + 8 * c)); }
    }
  }
}

__device__ __forceinline__ void ytrans_block(const u16* __restrict__ yT, const u16* __restrict__ u,
                                             const u16* __restrict__ Dskip, u16* __restrict__ out, u16* tile,
                                             int bid, int tid) {
#pragma unroll
  for (int it = 0; it < 4; ++it) {
    int tl = bid * 4 + it;
    int l0 = (tl >> 4) * 64, d0 = (tl & 15) * 64;
    int r = tid >> 3, c = tid & 7;
    uint4 q = *reinterpret_cast<const uint4*>(yT + (size_t)(d0 + r) * SEQ + l0 + 8 * c);
    uint4 uq = *reinterpret_cast<const uint4*>(u + (size_t)(l0 + r) * DCH + d0 + 8 * c);
    uint4 dq = *reinterpret_cast<const uint4*>(Dskip + d0 + 8 * c);
    u32 w[4] = {q.x, q.y, q.z, q.w};
    if (it) __syncthreads();
#pragma unroll
    for (int j = 0; j < 4; ++j) {
      tile[(8 * c + 2 * j) * 72 + r] = (u16)(w[j] & 0xffffu);
      tile[(8 * c + 2 * j + 1) * 72 + r] = (u16)(w[j] >> 16);
    }
    __syncthreads();
    uint4 y = *reinterpret_cast<const uint4*>(tile + r * 72 + 8 * c);
    u32 yy[4] = {y.x, y.y, y.z, y.w}, uu[4] = {uq.x, uq.y, uq.z, uq.w}, dd[4] = {dq.x, dq.y, dq.z, dq.w};
    u32 oo[4];
#pragma unroll
    for (int j = 0; j < 4; ++j) {
      float o0 = bf2f(yy[j] & 0xffffu) + bfround(bf2f(uu[j] & 0xffffu) * bf2f(dd[j] & 0xffffu));
      float o1 = bf2f(yy[j] >> 16) + bfround(bf2f(uu[j] >> 16) * bf2f(dd[j] >> 16));
      oo[j] = f2bf(o0) | (f2bf(o1) << 16);
    }
    *reinterpret_cast<uint4*>(out + (size_t)(l0 + r) * (2 * DCH) + d0 + 8 * c) = make_uint4(oo[0], oo[1], oo[2], oo[3]);
  }
}

#define SM_CF (TWOFF + 260)

__global__ __launch_bounds__(256, 3) void k1(KParams P, float* Kc, const u16* x, cf* X1T, const u16* wr,
                                            const u16* wi, u32* WT, cf* TW, const u16* u, u16* uT) {
  __shared__ __attribute__((aligned(16))) u32 smw[4 * 32 * KREC];
  cf* sm = reinterpret_cast<cf*>(smw);
  int b = blockIdx.x, tid = threadIdx.x;
  if (b < 768) {
    int q = b / 3, r = b % 3;
    if (r < 2) {
      kgen_block(P, Kc, smw, q * 2 + r, tid);
    } else {
      brow_block(x, X1T, sm, q, tid);
    }
  } else if (b < 768 + 128) {
    int i = b - 768;
    if (i < 8) {
      int j = i * 256 + tid;
      float s, c;
      sincospif(-(float)j / 1024.0f, &s, &c);
      TW[j] = cmk(c, s);
    }
    wt_block(wr, wi, WT, smw, i, tid);
  } else {
    utrans_block(u, uT, reinterpret_cast<u16*>(smw), b - 896, tid);
  }
}

__global__ __launch_bounds__(128, 3) void k2(cf* X1T, const u32* WT, const u32* uT, const float* Kc, const cf* TW,
                                            u32* yT) {
  __shared__ __attribute__((aligned(16))) cf sm[XBUF];
  int b = blockIdx.x, tid = threadIdx.x;
  if (b >= 1024) {
    int q = b - 1024;
    if (q == 0) bcol_block<true>(X1T, WT, TW, sm, q, tid);
    else bcol_block<false>(X1T, WT, TW, sm, q, tid);
  } else {
    aconv_block(uT, Kc, TW, yT, sm, b, tid);
  }
}

__global__ __launch_bounds__(512, 2) void k3(const cf* X1T, const u16* yT, const u16* u, const u16* Dskip, u16* out) {
  __shared__ __attribute__((aligned(16))) cf sm[SM_CF];
  int b = blockIdx.x, tid = threadIdx.x;
  if (b & 1) {
    browinv_block(X1T, out, sm, b >> 1, tid);
  } else {
    ytrans_block(yT, u, Dskip, out, reinterpret_cast<u16*>(sm), b >> 1, tid);
  }
}

extern "C" void kernel_launch(void* const* d_in, const int* in_sizes, int n_in, void* d_out, int out_size,
                              void* d_ws, size_t ws_size, hipStream_t stream) {
  const u16* u = (const u16*)d_in[0];
  KParams P;
  P.Are = (const u16*)d_in[1];
  P.Aim = (const u16*)d_in[2];
  P.Bre = (const u16*)d_in[3];
  P.Bim = (const u16*)d_in[4];
  P.Cre = (const u16*)d_in[5];
  P.Cim = (const u16*)d_in[6];
  P.logdt = (const u16*)d_in[7];
  const u16* Dskip = (const u16*)d_in[8];
  const u16* x = (const u16*)d_in[9];
  const u16* wr = (const u16*)d_in[10];
  const u16* wi = (const u16*)d_in[11];
  u16* out = (u16*)d_out;
  char* ws = (char*)d_ws;
  cf* X1T = (cf*)ws;
  u32* WT = (u32*)(ws + (size_t)8 * 1024 * 1024);
  float* Kc = (float*)(ws + (size_t)16 * 1024 * 1024);
  cf* TW = (cf*)(ws + (size_t)20 * 1024 * 1024);
  u16* uT = (u16*)(ws + (size_t)24 * 1024 * 1024);
  u16* yT = (u16*)(ws + (size_t)28 * 1024 * 1024);

  k1<<<768 + 128 + 256, 256, 0, stream>>>(P, Kc, x, X1T, wr, wi, WT, TW, u, uT);
  k2<<<1536, 128, 0, stream>>>(X1T, WT, (const u32*)uT, Kc, TW, (u32*)yT);
  k3<<<256, 512, 0, stream>>>(X1T, yT, u, Dskip, out);
}
```

```cpp
#include <hip/hip_runtime.h>
#include <hip/hip_bf16.h>
#include <type_traits>

#define SEQ 2048
#define DCH 1024
#define NST 128
#define DF 513

typedef __attribute__((ext_vector_type(2))) float cf;
typedef unsigned int u32;
typedef unsigned short u16;
using f32x16 = __attribute__((ext_vector_type(16))) float;

template <int I, int N, class F>
__device__ __forceinline__ void static_for(F&& f) {
  if constexpr (I < N) {
    f(std::integral_constant<int, I>{});
    static_for<I + 1, N>(f);
  }
}

__device__ __forceinline__ cf cmk(float x, float y) { cf r; r.x = x; r.y = y; return r; }
__device__ __forceinline__ cf cadd(cf a, cf b) { return a + b; }
__device__ __forceinline__ cf csub(cf a, cf b) { return a - b; }
__device__ __forceinline__ cf add_pi(cf a, cf b) {
  cf r;
  asm("v_pk_add_f32 %0, %1, %2 op_sel:[0,1] op_sel_hi:[1,0] neg_lo:[0,1]" : "=v"(r) : "v"(a), "v"(b));
  return r;
}
__device__ __forceinline__ cf add_mi(cf a, cf b) {
  cf r;
  asm("v_pk_add_f32 %0, %1, %2 op_sel:[0,1] op_sel_hi:[1,0] neg_hi:[0,1]" : "=v"(r) : "v"(a), "v"(b));
  return r;
}
__device__ __forceinline__ cf cmul(cf a, cf b) {
  cf t, r;
  asm("v_pk_mul_f32 %0, %1, %2 op_sel:[0,0] op_sel_hi:[0,1]" : "=v"(t) : "v"(a), "v"(b));
  asm("v_pk_fma_f32 %0, %1, %2, %3 op_sel:[1,1,0] op_sel_hi:[1,0,1] neg_lo:[0,1,0]" : "=v"(r) : "v"(a), "v"(b), "v"(t));
  return r;
}
__device__ __forceinline__ cf cmad(cf a, cf b, cf c) {
  cf t, r;
  asm("v_pk_fma_f32 %0, %1, %2, %3 op_sel:[0,0,0] op_sel_hi:[0,1,1]" : "=v"(t) : "v"(a), "v"(b), "v"(c));
  asm("v_pk_fma_f32 %0, %1, %2, %3 op_sel:[1,1,0] op_sel_hi:[1,0,1] neg_lo:[0,1,0]" : "=v"(r) : "v"(a), "v"(b), "v"(t));
  return r;
}
__device__ __forceinline__ cf cmulc(cf a, cf b) {
  cf t, r;
  asm("v_pk_mul_f32 %0, %1, %2 op_sel:[0,0] op_sel_hi:[1,0]" : "=v"(t) : "v"(a), "v"(b));
  asm("v_pk_fma_f32 %0, %1, %2, %3 op_sel:[1,1,0] op_sel_hi:[0,1,1] neg_hi:[1,0,0]" : "=v"(r) : "v"(a), "v"(b), "v"(t));
  return r;
}
__device__ __forceinline__ cf cmadc(cf a, cf b, cf c) {
  cf t, r;
  asm("v_pk_fma_f32 %0, %1, %2, %3 op_sel:[0,0,0] op_sel_hi:[1,0,1]" : "=v"(t) : "v"(a), "v"(b), "v"(c));
  asm("v_pk_fma_f32 %0, %1, %2, %3 op_sel:[1,1,0] op_sel_hi:[0,1,1] neg_hi:[1,0,0]" : "=v"(r) : "v"(a), "v"(b), "v"(t));
  return r;
}
__device__ __forceinline__ cf addc(cf a, cf b) {
  cf r;
  asm("v_pk_add_f32 %0, %1, %2 neg_hi:[0,1]" : "=v"(r) : "v"(a), "v"(b));
  return r;
}
__device__ __forceinline__ cf oddc(cf a, cf b) {
  cf r;
  asm("v_pk_add_f32 %0, %1, %2 op_sel:[1,1] op_sel_hi:[0,0] neg_hi:[1,0]" : "=v"(r) : "v"(a), "v"(b));
  return r;
}
__device__ __forceinline__ cf conj_add_pi(cf a, cf b) {
  cf r;
  asm("v_pk_add_f32 %0, %1, %2 op_sel:[0,1] op_sel_hi:[1,0] neg_hi:[1,0]" : "=v"(r) : "v"(a), "v"(b));
  return r;
}
__device__ __forceinline__ cf cmulk(cf a, cf cs) {
  cf t, r;
  asm("v_pk_mul_f32 %0, %1, %2 op_sel:[0,0] op_sel_hi:[1,0]" : "=v"(t) : "v"(a), "s"(cs));
  asm("v_pk_fma_f32 %0, %1, %2, %3 op_sel:[1,1,0] op_sel_hi:[0,1,1] neg_lo:[1,0,0]" : "=v"(r) : "v"(a), "s"(cs), "v"(t));
  return r;
}
__device__ __forceinline__ cf csqr(cf a) { return cmk(fmaf(a.x, a.x, -(a.y * a.y)), 2.0f * a.x * a.y); }

__device__ __forceinline__ float bf2f(u32 h) { return __uint_as_float(h << 16); }
__device__ __forceinline__ u32 f2bf(float f) {
  __hip_bfloat16 b = __float2bfloat16(f);
  return (u32)(*reinterpret_cast<u16*>(&b));
}
__device__ __forceinline__ float bfround(float f) { return bf2f(f2bf(f)); }

__device__ __forceinline__ void wave_sync() {
  __builtin_amdgcn_fence(__ATOMIC_SEQ_CST, "wavefront");
  __builtin_amdgcn_wave_barrier();
  __builtin_amdgcn_fence(__ATOMIC_SEQ_CST, "wavefront");
}

constexpr double COS64_TAB[17] = {
    1.0, 0.99518472667219688624, 0.98078528040323044913, 0.95694033573220886494,
    0.92387953251128675613, 0.88192126434835502971, 0.83146961230254523708,
    0.77301045336273696081, 0.70710678118654752440, 0.63439328416364549822,
    0.55557023301960222474, 0.47139673682599764856, 0.38268343236508977173,
    0.29028467725446236764, 0.19509032201612826785, 0.09801714032956060199, 0.0};
constexpr double cos64(int j) {
  j = ((j % 64) + 64) % 64;
  if (j > 32) j = 64 - j;
  return (j <= 16) ? COS64_TAB[j] : -COS64_TAB[32 - j];
}
constexpr double sin64(int j) { return cos64(j - 16); }

template <int J, int DIR>
__device__ __forceinline__ cf tw64(cf a) {
  constexpr int j = ((J % 64) + 64) % 64;
  if constexpr (j == 0) {
    return a;
  } else if constexpr (j == 32) {
    return -a;
  } else if constexpr (j == 16) {
    return (DIR > 0) ? cmk(-a.y, a.x) : cmk(a.y, -a.x);
  } else if constexpr (j == 48) {
    return (DIR > 0) ? cmk(a.y, -a.x) : cmk(-a.y, a.x);
  } else {
    constexpr float c = (float)cos64(j);
    constexpr float s = (float)(DIR * sin64(j));
    return cmulk(a, cmk(c, s));
  }
}

template <int N, int DIR, int S>
__device__ __forceinline__ void fft_rec(const cf* in, cf* out) {
  if constexpr (N == 1) {
    out[0] = in[0];
  } else if constexpr (N == 2) {
    cf a = in[0], b = in[S];
    out[0] = cadd(a, b);
    out[1] = csub(a, b);
  } else {
    constexpr int Q = N / 4;
    cf t0[Q], t1[Q], t2[Q], t3[Q];
    fft_rec<Q, DIR, 4 * S>(in, t0);
    fft_rec<Q, DIR, 4 * S>(in + S, t1);
    fft_rec<Q, DIR, 4 * S>(in + 2 * S, t2);
    fft_rec<Q, DIR, 4 * S>(in + 3 * S, t3);
    static_for<0, Q>([&](auto ic) {
      constexpr int k = decltype(ic)::value;
      constexpr int M = 64 / N;
      cf a = t0[k];
      cf b = tw64<k * M, DIR>(t1[k]);
      cf d = tw64<3 * k * M, DIR>(t3[k]);
      cf s0, s1;
      if constexpr (2 * k * M == 16) {
        s0 = (DIR > 0) ? add_pi(a, t2[k]) : add_mi(a, t2[k]);
        s1 = (DIR > 0) ? add_mi(a, t2[k]) : add_pi(a, t2[k]);
      } else {
        cf c = tw64<2 * k * M, DIR>(t2[k]);
        s0 = cadd(a, c);
        s1 = csub(a, c);
      }
      cf s2 = cadd(b, d), s3 = csub(b, d);
      out[k] = cadd(s0, s2);
      out[k + Q] = (DIR > 0) ? add_pi(s1, s3) : add_mi(s1, s3);
      out[k + 2 * Q] = csub(s0, s2);
      out[k + 3 * Q] = (DIR > 0) ? add_mi(s1, s3) : add_pi(s1, s3);
    });
  }
}

template <int N, int DIR>
__device__ __forceinline__ void fft_reg(cf* v) {
  cf o[N];
  fft_rec<N, DIR, 1>(v, o);
#pragma unroll
  for (int i = 0; i < N; ++i) v[i] = o[i];
}

template <int DIR>
__device__ __forceinline__ void fft16_halfzero(cf* v) {
  cf a[8], b[8], oa[8], ob[8];
  static_for<0, 8>([&](auto ic) {
    constexpr int n = decltype(ic)::value;
    a[n] = v[n];
    b[n] = tw64<4 * n, DIR>(v[n]);
  });
  fft_rec<8, DIR, 1>(a, oa);
  fft_rec<8, DIR, 1>(b, ob);
#pragma unroll
  for (int k = 0; k < 8; ++k) {
    v[2 * k] = oa[k];
    v[2 * k + 1] = ob[k];
  }
}

struct Tw16 { cf w[4]; };

__device__ __forceinline__ Tw16 make_tw16(int n, int logN) {
  Tw16 b;
#pragma unroll
  for (int t = 0; t < 4; ++t) {
    int e = (n << t) & ((1 << logN) - 1);
    float s, c;
    sincospif(-2.0f * (float)e / (float)(1 << logN), &s, &c);
    b.w[t] = cmk(c, s);
  }
  return b;
}

__device__ __forceinline__ Tw16 load_tw16(const cf* __restrict__ TW, int n) {
  Tw16 b;
#pragma unroll
  for (int t = 0; t < 4; ++t) b.w[t] = TW[(n << t) & 2047];
  return b;
}

template <bool CONJ>
__device__ __forceinline__ void apply_tw16(cf* v, const Tw16& b) {
  cf p[16];
  p[1] = b.w[0];
  p[2] = b.w[1];
  p[4] = b.w[2];
  p[8] = b.w[3];
  asm volatile("" : "+v"(p[1]), "+v"(p[2]), "+v"(p[4]), "+v"(p[8]));
  static_for<3, 16>([&](auto ic) {
    constexpr int k = decltype(ic)::value;
    constexpr int top = (k >= 8) ? 8 : (k >= 4) ? 4 : 2;
    if constexpr (k != top) p[k] = cmul(p[k - top], p[top]);
  });
#pragma unroll
  for (int k = 1; k < 16; ++k) v[k] = CONJ ? cmulc(v[k], p[k]) : cmul(v[k], p[k]);
}

__device__ __forceinline__ void swap16(cf& a, cf& b) {
  asm volatile("s_nop 1" : "+v"(a), "+v"(b));
  auto r0 = __builtin_amdgcn_permlane16_swap(__float_as_uint(a.x), __float_as_uint(b.x), false, false);
  auto r1 = __builtin_amdgcn_permlane16_swap(__float_as_uint(a.y), __float_as_uint(b.y), false, false);
  a.x = __uint_as_float(r0[0]);
  b.x = __uint_as_float(r0[1]);
  a.y = __uint_as_float(r1[0]);
  b.y = __uint_as_float(r1[1]);
}

#define XP 152
#define XBUF (16 * XP)
typedef volatile __attribute__((address_space(3))) cf* ldsp;
__device__ __forceinline__ cf lds_ld(const cf* p) { return *(ldsp)(p); }
__device__ __forceinline__ void lds_st(cf* p, cf a) { *(ldsp)(p) = a; }

struct FCtx {
  Tw16 ta, tb;
  int t, offA, offB, kA, kB;
};
__device__ __forceinline__ FCtx make_fctx(const cf* __restrict__ TW, int t) {
  FCtx f;
  int k0 = t >> 3, c = t & 7;
  f.t = t;
  f.ta = load_tw16(TW, t);
  f.tb = load_tw16(TW, 16 * c);
  int k0m = (16 - k0) & 15;
  int k1m = (k0 != 0) ? (15 - c) : ((c != 0) ? (16 - c) : 8);
  f.offA = k0 * XP + 9 * c;
  f.offB = k0m * XP + 9 * k1m;
  f.kA = k0 + 16 * c;
  f.kB = k0m + 16 * k1m;
  return f;
}

__device__ __forceinline__ void fft2048_fwd_rest(cf* v, cf* buf, const FCtx& f) {
  apply_tw16<false>(v, f.ta);
  __syncthreads();
#pragma unroll
  for (int k0 = 0; k0 < 16; ++k0) lds_st(buf + k0 * XP + f.t, v[k0]);
  __syncthreads();
  cf* rb = buf + (f.t >> 3) * XP + (f.t & 7);
#pragma unroll
  for (int b = 0; b < 16; ++b) v[b] = lds_ld(rb + 8 * b);
  fft_reg<16, -1>(v);
  apply_tw16<false>(v, f.tb);
#pragma unroll
  for (int k1 = 0; k1 < 16; ++k1) lds_st(rb + 9 * k1, v[k1]);
  __syncthreads();
#pragma unroll
  for (int j = 0; j < 8; ++j) v[j] = lds_ld(buf + f.offA + j);
#pragma unroll
  for (int j = 0; j < 8; ++j) v[8 + j] = lds_ld(buf + f.offB + j);
  fft_reg<8, -1>(v);
  fft_reg<8, -1>(v + 8);
}

__device__ __forceinline__ void fft2048_inv_first(cf* v, cf* buf, const FCtx& f) {
  fft_reg<8, 1>(v);
  fft_reg<8, 1>(v + 8);
  __syncthreads();
#pragma unroll
  for (int j = 0; j < 8; ++j) lds_st(buf + f.offA + j, v[j]);
#pragma unroll
  for (int j = 0; j < 8; ++j) lds_st(buf + f.offB + j, v[8 + j]);
  __syncthreads();
  cf* rb = buf + (f.t >> 3) * XP + (f.t & 7);
#pragma unroll
  for (int k1 = 0; k1 < 16; ++k1) v[k1] = lds_ld(rb + 9 * k1);
  apply_tw16<true>(v, f.tb);
  fft_reg<16, 1>(v);
#pragma unroll
  for (int b = 0; b < 16; ++b) lds_st(rb + 8 * b, v[b]);
  __syncthreads();
#pragma unroll
  for (int kk = 0; kk < 16; ++kk) v[kk] = lds_ld(buf + kk * XP + f.t);
  apply_tw16<true>(v, f.ta);
}

#define KC_PITCH 648
#define KREC 104
using u32x4 = __attribute__((ext_vector_type(4))) unsigned;
using u32x2 = __attribute__((ext_vector_type(2))) unsigned;
using bf16x8 = __attribute__((ext_vector_type(8))) __bf16;

__device__ __forceinline__ cf cexp_scaled(float ar, float ai, float sc) {
  float e = expf(ar * sc);
  float s, c;
  sincosf(ai * sc, &s, &c);
  return cmk(e * c, e * s);
}
__device__ __forceinline__ u32 cvtpk(float a, float b) {
  u32 r;
  asm("v_cvt_pk_bf16_f32 %0, %1, %2" : "=v"(r) : "v"(a), "v"(b));
  return r;
}
__device__ __forceinline__ u32x2 split2(cf g) {
  u32x2 o;
  o.x = cvtpk(g.x, g.y);
  cf h = cmk(__uint_as_float(o.x << 16), __uint_as_float(o.x & 0xffff0000u));
  cf r = g - h;
  o.y = cvtpk(r.x, r.y);
  return o;
}

struct KParams { const u16 *Are, *Aim, *Bre, *Bim, *Cre, *Cim, *logdt; };

__device__ __forceinline__ void kgen_block(const KParams& P, float* Kst, u32* lds_all, int bid, int tid) {
  int wave = tid >> 6, lane = tid & 63;
  int d = bid * 2 + (wave >> 1);
  u32* lds = lds_all + wave * 32 * KREC;
  float dt = bfround(expf(bf2f(P.logdt[d])));
  int idx = d * NST + (wave & 1) * 64 + lane;
  float ar = dt * bf2f(P.Are[idx]), ai = dt * bf2f(P.Aim[idx]);
  cf dB = cmk(dt * bf2f(P.Bre[idx]), dt * bf2f(P.Bim[idx]));
  cf C = cmk(bf2f(P.Cre[idx]), bf2f(P.Cim[idx]));
  cf c0 = cmul(C, dB);
  cf e[12];
  e[0] = cexp_scaled(ar, ai, 1.0f);
  e[4] = cexp_scaled(ar, ai, 16.0f);
  e[8] = cexp_scaled(ar, ai, 256.0f);
  e[1] = csqr(e[0]); e[2] = csqr(e[1]); e[3] = csqr(e[2]);
  e[5] = csqr(e[4]); e[6] = csqr(e[5]); e[7] = csqr(e[6]);
  e[9] = csqr(e[8]); e[10] = csqr(e[9]); e[11] = csqr(e[10]);
  typedef volatile __attribute__((address_space(3))) u32x2* lp2;
  float s2048 = fmaf(c0.x, e[11].x, -(c0.y * e[11].y));
  {
    auto dpp = [](float x, auto ctl, auto rm) {
      return __int_as_float(__builtin_amdgcn_update_dpp(0, __float_as_int(x), decltype(ctl)::value, decltype(rm)::value, 0xf, true));
    };
    using std::integral_constant;
    s2048 += dpp(s2048, integral_constant<int, 0x111>{}, integral_constant<int, 0xf>{});
    s2048 += dpp(s2048, integral_constant<int, 0x112>{}, integral_constant<int, 0xf>{});
    s2048 += dpp(s2048, integral_constant<int, 0x114>{}, integral_constant<int, 0xf>{});
    s2048 += dpp(s2048, integral_constant<int, 0x118>{}, integral_constant<int, 0xf>{});
    s2048 += dpp(s2048, integral_constant<int, 0x142>{}, integral_constant<int, 0xa>{});
    s2048 += dpp(s2048, integral_constant<int, 0x143>{}, integral_constant<int, 0xc>{});
  }
  u32x2 Gw[20], Ew[32];
  const int rot = 2 * ((lane >> 2) & 3);
  u32* rec0 = lds + (lane & 31) * KREC;
  u32* rec = rec0 + rot;
  {
    cf G[20];
    G[0] = cmk(c0.x, -c0.y);
    G[1] = cmulc(G[0], e[0]);
    G[2] = cmulc(G[0], e[1]);
    G[3] = cmulc(G[1], e[1]);
    G[4] = cmulc(G[0], e[2]);
    G[5] = cmulc(G[1], e[2]);
    G[6] = cmulc(G[2], e[2]);
    G[7] = cmulc(G[3], e[2]);
    G[8] = cmulc(G[0], e[8]);
    G[9] = cmulc(G[2], e[8]);
    G[10] = cmulc(G[4], e[8]);
    G[11] = cmulc(G[6], e[8]);
    G[12] = cmulc(G[0], e[9]);
    G[13] = cmulc(G[4], e[9]);
    G[14] = cmulc(G[8], e[9]);
    G[15] = cmulc(G[10], e[9]);
    G[16] = cmulc(G[0], e[10]);
    G[17] = cmulc(G[8], e[10]);
    G[18] = cmulc(G[12], e[10]);
    G[19] = cmulc(G[14], e[10]);
#pragma unroll
    for (int i = 0; i < 20; ++i) Gw[i] = split2(G[i]);
  }
  if (lane < 32) {
#pragma unroll
    for (int i = 0; i < 20; ++i) *(lp2)(rec + 2 * i) = Gw[i];
  }
  {
    cf E[32];
    E[0] = cmk(1.f, 0.f);
    E[1] = e[3]; E[2] = e[4]; E[4] = e[5]; E[8] = e[6]; E[16] = e[7];
    static_for<3, 32>([&](auto ic) {
      constexpr int k = decltype(ic)::value;
      constexpr int top = (k >= 16) ? 16 : (k >= 8) ? 8 : (k >= 4) ? 4 : 2;
      if constexpr (k != top) E[k] = cmul(E[k - top], E[top]);
    });
    Ew[0].x = 0x3f80u;
    Ew[0].y = 0u;
#pragma unroll
    for (int i = 1; i < 32; ++i) Ew[i] = split2(E[i]);
  }
  f32x16 acc;
#pragma unroll
  for (int i = 0; i < 16; ++i) acc[i] = 0.f;
  const u32* base = lds + (2 * (lane >> 5)) * KREC;
  int offB[4];
#pragma unroll
  for (int q = 0; q < 4; ++q) {
    int w = 40 + 2 * (lane & 31) + 2 * q;
    offB[q] = (w >= KREC) ? w - KREC : w;
  }
#pragma unroll
  for (int ch = 0; ch < 2; ++ch) {
    if ((lane >> 5) == ch) {
      if (ch) {
#pragma unroll
        for (int i = 0; i < 20; ++i) *(lp2)(rec + 2 * i) = Gw[i];
      }
#pragma unroll
      for (int i = 0; i < 29; ++i) *(lp2)(rec + 40 + 2 * i) = Ew[i];
#pragma unroll
      for (int i = 29; i < 32; ++i) {
        int w = 40 + 2 * i + rot;
        *(lp2)(rec0 + ((w >= KREC) ? w - KREC : w)) = Ew[i];
      }
    }
#pragma unroll
    for (int m = 0; m < 8; ++m) {
      const u32* r0 = base + 4 * m * KREC;
      const u32* ra = r0 + 2 * (lane & 31) + 2 * (m & 3);
      const u32* rb = r0 + offB[m & 3];
      u32x2 a0 = *(lp2)(ra), a1 = *(lp2)(ra + KREC), b0 = *(lp2)(rb), b1 = *(lp2)(rb + KREC);
      u32x4 A = {a0.x, a0.y, a1.x, a1.y}, A2 = {a0.y, a0.x, a1.y, a1.x}, B = {b0.x, b0.y, b1.x, b1.y};
      acc = __builtin_amdgcn_mfma_f32_32x32x16_bf16(__builtin_bit_cast(bf16x8, A), __builtin_bit_cast(bf16x8, B), acc, 0, 0, 0);
      acc = __builtin_amdgcn_mfma_f32_32x32x16_bf16(__builtin_bit_cast(bf16x8, A2), __builtin_bit_cast(bf16x8, B), acc, 0, 0, 0);
    }
  }
  float* red = reinterpret_cast<float*>(lds_all + (wave | 1) * 32 * KREC);
  wave_sync();
  if (wave & 1) {
#pragma unroll
    for (int r = 0; r < 16; ++r) red[r * 64 + lane] = acc[r];
    if (lane == 63) red[1024] = s2048;
  }
  __syncthreads();
  if (!(wave & 1)) {
    int col = lane & 31, hh = lane >> 5;
    float* dst = Kst + (wave >> 1) * KC_PITCH;
#pragma unroll
    for (int r = 0; r < 16; ++r) {
      int row = (r & 3) + 8 * (r >> 2) + 4 * hh;
      int p = -1;
      if (row < 8) p = row + 8 * col;
      else if (row < 12) p = 256 + (row - 8) + 4 * col;
      else if (row < 16) p = 384 + ((row - 12) & 1) + 64 * ((row - 12) >> 1) + 2 * col;
      else if (row < 20) p = 512 + 32 * (row - 16) + col;
      if (p >= 0) dst[p] = acc[r] + red[r * 64 + lane];
    }
    if (lane == 63) dst[640] = s2048 + red[1024];
  }
}

__device__ __forceinline__ void wt_block(const u16* __restrict__ wr, const u16* __restrict__ wi, u32* __restrict__ WT,
                                         u32* tile, int bid, int tid) {
  int s0 = bid * 16;
  const u32* pr = reinterpret_cast<const u32*>(wr + (size_t)s0 * DF);
  const u32* pi = reinterpret_cast<const u32*>(wi + (size_t)s0 * DF);
  u32 a[17], b[17];
#pragma unroll
  for (int i = 0; i < 17; ++i) {
    int w = min(tid + 256 * i, 8 * DF - 1);
    a[i] = pr[w];
    b[i] = pi[w];
  }
#pragma unroll
  for (int i = 0; i < 17; ++i) {
    int w = tid + 256 * i;
    if (w < 8 * DF) {
      int e0 = 2 * w;
      int r = e0 / DF, k = e0 - r * DF;
      tile[k * 17 + r] = (a[i] & 0xffffu) | (b[i] << 16);
      int k1 = k + 1, r1 = r;
      if (k1 == DF) { k1 = 0; r1 = r + 1; }
      tile[k1 * 17 + r1] = (a[i] >> 16) | (b[i] & 0xffff0000u);
    }
  }
  __syncthreads();
  int sx = tid & 15, ky = tid >> 4;
#pragma unroll
  for (int i = 0; i < 33; ++i) {
    int k = ky + 16 * i;
    if (k < DF) __builtin_nontemporal_store(tile[k * 17 + sx], WT + (size_t)k * SEQ + s0 + sx);
  }
}

#define RP 34
#define RRS (16 * RP)
#define ZP 517
#define TWOFF (16 * RRS)

__device__ __forceinline__ int pos2row(int p) { return 128 * (2 * (p >> 8) + (p & 1)) + ((p & 255) >> 1); }

__device__ __forceinline__ void brow_block(const u16* __restrict__ x, cf* __restrict__ X1T, cf* sm, int bid, int tid) {
  cf* tw = sm + 8 * RRS;
  int s0 = bid * 8;
  {
    float s, c;
    sincospif(-(float)tid / 512.0f, &s, &c);
    tw[tid] = cmk(c, s);
    if (tid == 0) tw[256] = cmk(0.f, -1.f);
  }
  int row = tid >> 5, t = tid & 31;
  cf v[16];
  const u32* xr = reinterpret_cast<const u32*>(x + (size_t)pos2row(s0 + row) * DCH);
#pragma unroll
  for (int a = 0; a < 16; ++a) {
    u32 w = xr[32 * a + t];
    v[a] = cmk(bf2f(w & 0xffffu), bf2f(w >> 16));
  }
  Tw16 tb = make_tw16(t, 9);
  fft_reg<16, -1>(v);
  apply_tw16<false>(v, tb);
  cf* rb = sm + row * RRS;
  {
    int pos = (t & 1) * 16 + (t >> 1);
#pragma unroll
    for (int k0 = 0; k0 < 16; ++k0) rb[k0 * RP + pos] = v[k0];
  }
  wave_sync();
  int k0 = t & 15, h = t >> 4;
  {
    const float4* p = reinterpret_cast<const float4*>(rb + k0 * RP + h * 16);
#pragma unroll
    for (int m = 0; m < 8; ++m) {
      float4 q = p[m];
      v[2 * m] = cmk(q.x, q.y);
      v[2 * m + 1] = cmk(q.z, q.w);
    }
  }
  fft_reg<16, -1>(v);
  bool hi = h != 0;
  static_for<0, 8>([&](auto ic) {
    constexpr int j = decltype(ic)::value;
    cf a = v[j], b = v[j + 8];
    swap16(a, b);
    cf br = cmk(hi ? b.y : b.x, hi ? -b.x : b.y);
    br = tw64<2 * j, -1>(br);
    v[j] = cadd(a, br);
    v[j + 8] = csub(a, br);
  });
  __syncthreads();
  cf* zs = sm + row * ZP;
#pragma unroll
  for (int r = 0; r < 16; ++r) zs[k0 + 16 * ((r & 7) + 8 * h + 16 * (r >> 3))] = v[r];
  __syncthreads();
  int r = tid & 7, kk = tid >> 3;
  const cf* zr = sm + r * ZP;
#pragma unroll
  for (int i = 0; i < 9; ++i) {
    int k = kk + 32 * i;
    if (k > 256) break;
    cf a = zr[k];
    cf b = zr[(512 - k) & 511];
    if (k == 0) {
      __builtin_nontemporal_store(cmk(a.x + a.y, a.x - a.y), X1T + (size_t)0 * SEQ + s0 + r);
    } else {
      cf w = tw[k];
      cf ev = cmk(0.5f * (a.x + b.x), 0.5f * (a.y - b.y));
      cf od = cmk(0.5f * (a.y + b.y), -0.5f * (a.x - b.x));
      cf wo = cmul(od, w);
      __builtin_nontemporal_store(cadd(ev, wo), X1T + (size_t)k * SEQ + s0 + r);
      if (k != 256) __builtin_nontemporal_store(cmk(ev.x - wo.x, -(ev.y - wo.y)), X1T + (size_t)(512 - k) * SEQ + s0 + r);
    }
  }
}

__device__ __forceinline__ void browinv_block(const cf* X2T, const cf* __restrict__ TW, const Tw16& tb,
                                              const cf* wk, u16* __restrict__ out, cf* sm, int bid, int tid) {
  int s0 = bid * 8;
  {
    int rp = tid & 3, kq = tid >> 2;
    float4 a[5], b[5];
#pragma unroll
    for (int i = 0; i < 5; ++i) {
      int k = kq + 64 * i;
      if (k <= 256) {
        a[i] = *reinterpret_cast<const float4*>(X2T + (size_t)k * SEQ + s0 + 2 * rp);
        b[i] = *reinterpret_cast<const float4*>(X2T + (size_t)((512 - k) & 511) * SEQ + s0 + 2 * rp);
      }
    }
#pragma unroll
    for (int i = 0; i < 5; ++i) {
      int k = kq + 64 * i;
      if (k <= 256) {
#pragma unroll
        for (int h = 0; h < 2; ++h) {
          cf* zr = sm + (2 * rp + h) * ZP;
          cf aa = h ? cmk(a[i].z, a[i].w) : cmk(a[i].x, a[i].y);
          cf bb = h ? cmk(b[i].z, b[i].w) : cmk(b[i].x, b[i].y);
          if (k == 0) {
            zr[0] = cmk(aa.x + aa.y, aa.x - aa.y);
          } else {
            cf w = wk[i];
            cf ev = cmk(aa.x + bb.x, aa.y - bb.y);
            cf df = cmk(aa.x - bb.x, aa.y + bb.y);
            cf od = cmulc(df, w);
            zr[k] = cmk(ev.x - od.y, ev.y + od.x);
            if (k != 256) zr[512 - k] = cmk(ev.x + od.y, -ev.y + od.x);
          }
        }
      }
    }
  }
  __syncthreads();
  int row = tid >> 5, t = tid & 31;
  int k0 = t & 15, h = t >> 4;
  cf v[16];
  {
    const cf* zs = sm + row * ZP;
#pragma unroll
    for (int r = 0; r < 16; ++r) v[r] = zs[k0 + 16 * ((r & 7) + 8 * h + 16 * (r >> 3))];
  }
  __syncthreads();
  bool hi = h != 0;
  static_for<0, 8>([&](auto ic) {
    constexpr int j = decltype(ic)::value;
    cf a = cadd(v[j], v[j + 8]);
    cf d = csub(v[j], v[j + 8]);
    cf dr = cmk(hi ? -d.y : d.x, hi ? d.x : d.y);
    dr = tw64<2 * j, 1>(dr);
    swap16(a, dr);
    v[j] = a;
    v[j + 8] = dr;
  });
  fft_reg<16, 1>(v);
  cf* rb = sm + row * RRS;
  {
    float4* p = reinterpret_cast<float4*>(rb + k0 * RP + h * 16);
#pragma unroll
    for (int m = 0; m < 8; ++m) p[m] = make_float4(v[2 * m].x, v[2 * m].y, v[2 * m + 1].x, v[2 * m + 1].y);
  }
  wave_sync();
  {
    int pos = (t & 1) * 16 + (t >> 1);
#pragma unroll
    for (int kk = 0; kk < 16; ++kk) v[kk] = rb[kk * RP + pos];
  }
  apply_tw16<true>(v, tb);
  fft_reg<16, 1>(v);
  u32* orow = reinterpret_cast<u32*>(out + (size_t)pos2row(s0 + row) * (2 * DCH) + DCH);
#pragma unroll
  for (int a = 0; a < 16; ++a) orow[32 * a + t] = cvtpk(v[a].x, v[a].y);
}

__device__ __forceinline__ cf ldw(const u32* p, int i) {
  u32 w = p[i];
  return cmk(bf2f(w & 0xffffu), bf2f(w >> 16));
}

template <bool SPECIAL>
__device__ __forceinline__ void bcol_block(const cf* __restrict__ X1T, cf* __restrict__ X2T, const u32* __restrict__ WT,
                                           const cf* __restrict__ TW, cf* buf, int col, int t) {
  cf v[16];
  const cf* xc = X1T + (size_t)col * SEQ;
#pragma unroll
  for (int a = 0; a < 8; ++a) {
    float4 q = *reinterpret_cast<const float4*>(xc + 256 * a + 2 * t);
    v[2 * a] = cmk(q.x, q.y);
    v[2 * a + 1] = cmk(q.z, q.w);
  }
  FCtx f = make_fctx(TW, t);
  const float sc = 1.0f / (2048.0f * 1024.0f);
  const u32* wc = WT + (size_t)col * SEQ;
  u32 wq[16];
#pragma unroll
  for (int j = 0; j < 8; ++j) {
    wq[j] = wc[f.kA + 256 * j];
    wq[8 + j] = wc[f.kB + 256 * j];
  }
  fft_reg<16, -1>(v);
  fft2048_fwd_rest(v, buf, f);
  const bool sp = SPECIAL && (col == 0);
  if (!sp) {
#pragma unroll
    for (int r = 0; r < 16; ++r) {
      cf w = cmk(bf2f(wq[r] & 0xffffu) * sc, bf2f(wq[r] >> 16) * sc);
      v[r] = cmul(v[r], w);
    }
  }
  if (SPECIAL) {
    __syncthreads();
    if (sp) {
#pragma unroll
      for (int j = 0; j < 8; ++j) {
        buf[f.kA + 256 * j] = v[j];
        buf[f.kB + 256 * j] = v[8 + j];
      }
    }
    __syncthreads();
    if (sp) {
      const u32* w0 = WT;
      const u32* w1 = WT + (size_t)512 * SEQ;
#pragma unroll 1
      for (int k = t; k <= 1024; k += 128) {
        int km = (2048 - k) & 2047;
        cf a = buf[k], b = buf[km];
        cf A = cmk(0.5f * (a.x + b.x), 0.5f * (a.y - b.y));
        cf B = cmk(0.5f * (a.y + b.y), -0.5f * (a.x - b.x));
        cf p = ldw(w0, k), q = ldw(w0, km);
        cf wh0 = cmk(0.5f * sc * (p.x + q.x), 0.5f * sc * (p.y - q.y));
        p = ldw(w1, k); q = ldw(w1, km);
        cf wh1 = cmk(0.5f * sc * (p.x + q.x), 0.5f * sc * (p.y - q.y));
        cf h0 = cmul(A, wh0), h1 = cmul(B, wh1);
        buf[km] = cmk(h0.x + h1.y, -h0.y + h1.x);
        buf[k] = cmk(h0.x - h1.y, h0.y + h1.x);
      }
    }
    __syncthreads();
    if (sp) {
#pragma unroll
      for (int j = 0; j < 8; ++j) {
        v[j] = buf[f.kA + 256 * j];
        v[8 + j] = buf[f.kB + 256 * j];
      }
    }
  }
  fft2048_inv_first(v, buf, f);
  fft_reg<16, 1>(v);
  __amdgpu_buffer_rsrc_t rs = __builtin_amdgcn_make_buffer_rsrc((void*)X2T, 0, 512 * SEQ * 8, 0x00020000);
  int off = (col * SEQ + 2 * t) * 8;
#pragma unroll
  for (int a = 0; a < 8; ++a) {
    u32x4 q = {__float_as_uint(v[2 * a].x), __float_as_uint(v[2 * a].y), __float_as_uint(v[2 * a + 1].x),
               __float_as_uint(v[2 * a + 1].y)};
    __builtin_amdgcn_raw_buffer_store_b128(q, rs, off + 2048 * a, 0, 16);
  }
}

__device__ __forceinline__ int kpos(int l, int s) {
  if (s == 0) return l;
  int half = 1 << (s - 1);
  int q = (l + half - 1 + ((l >> s) & 1)) >> s;
  return 128 * (s + 1) + q - 128;
}
__device__ __forceinline__ cf csel(bool c, cf a, cf b) { return cmk(c ? a.x : b.x, c ? a.y : b.y); }

__device__ __forceinline__ void aconv_block(const u32* __restrict__ uT, const float* kl, const cf* __restrict__ TW,
                                            const u16* __restrict__ Dskip, u32* ost, cf* buf, int d, int t) {
  cf v[16], kz[16];
  u32 uw[8];
  {
    static_for<0, 8>([&](auto ic) {
      constexpr int a = decltype(ic)::value;
      constexpr int s = (a < 1) ? 0 : (a < 2) ? 1 : (a < 4) ? 2 : 3;
      int l = 256 * a + 2 * t;
      kz[a] = cmk(kl[kpos(l, s)], kl[kpos(l + 1, s)]);
    });
    const u32* ur = uT + (size_t)d * (SEQ / 2);
#pragma unroll
    for (int a = 0; a < 8; ++a) uw[a] = ur[128 * a + t];
  }
  FCtx f = make_fctx(TW, t);
  cf L = TW[f.kA];
  cf L2 = TW[t == 0 ? 128 : f.kA];
  fft16_halfzero<-1>(kz);
  fft2048_fwd_rest(kz, buf, f);
#pragma unroll
  for (int a = 0; a < 8; ++a) v[a] = cmk(bf2f(uw[a] & 0xffffu), bf2f(uw[a] >> 16)) * (0.5f / 2048.0f);
  fft16_halfzero<-1>(v);
  fft2048_fwd_rest(v, buf, f);
  {
    const bool sp = (t == 0);
    cf oP[8], oQ[8];
    static_for<0, 8>([&](auto ic) {
      constexpr int j = decltype(ic)::value;
      cf a, b, ka, kb;
      if constexpr (j == 0) {
        a = v[0]; b = v[15]; ka = kz[0]; kb = kz[15];
      } else if constexpr (j < 4) {
        a = v[j]; ka = kz[j];
        b = csel(sp, v[8 - j], v[15 - j]);
        kb = csel(sp, kz[8 - j], kz[15 - j]);
      } else {
        a = csel(sp, v[8 + j], v[j]);
        ka = csel(sp, kz[8 + j], kz[j]);
        b = v[15 - j]; kb = kz[15 - j];
      }
      cf eu = addc(a, b);
      cf ou = oddc(a, b) * 0.5f;
      cf ek = addc(ka, kb);
      cf ok = oddc(ka, kb);
      cf w = tw64<8 * j, -1>((j < 4) ? L : L2);
      cf wo = cmul(ok, w);
      cf T = add_pi(wo, ek);
      cf T2 = conj_add_pi(wo, ek);
      oP[j] = cmad(ou, T, cmul(eu, ka));
      oQ[j] = cmadc(T2, ou, cmulc(kb, eu));
    });
    cf s0, s4;
    {
      float eu = 2.0f * v[0].x, ou = 2.0f * v[0].y;
      s0 = cmk(fmaf(eu, kz[0].x, ou * kz[0].y), fmaf(eu, kz[0].y, ou * kz[0].x));
      eu = 2.0f * v[4].x; ou = 2.0f * v[4].y;
      s4 = cmk(fmaf(eu, kz[4].x, -(ou * kz[4].y)), fmaf(eu, kz[4].y, ou * kz[4].x));
    }
    v[0] = csel(sp, s0, oP[0]);
    v[1] = oP[1];
    v[2] = oP[2];
    v[3] = oP[3];
    v[4] = csel(sp, s4, oP[4]);
    v[5] = csel(sp, oQ[3], oP[5]);
    v[6] = csel(sp, oQ[2], oP[6]);
    v[7] = csel(sp, oQ[1], oP[7]);
    v[8] = oQ[7];
    v[9] = oQ[6];
    v[10] = oQ[5];
    v[11] = oQ[4];
    v[12] = csel(sp, oP[4], oQ[3]);
    v[13] = csel(sp, oP[5], oQ[2]);
    v[14] = csel(sp, oP[6], oQ[1]);
    v[15] = csel(sp, oP[7], oQ[0]);
  }
  fft2048_inv_first(v, buf, f);
  fft_reg<16, 1>(v);
  float D = bf2f(Dskip[d]);
#pragma unroll
  for (int a = 0; a < 8; ++a) {
    u32 yb = cvtpk(v[a].x, v[a].y);
    u32 sb = cvtpk(bf2f(uw[a] & 0xffffu) * D, bf2f(uw[a] >> 16) * D);
    ost[128 * a + t] = cvtpk(bf2f(yb & 0xffffu) + bf2f(sb & 0xffffu), bf2f(yb >> 16) + bf2f(sb >> 16));
  }
}

__device__ __forceinline__ void utrans_block(const u16* __restrict__ u, u16* __restrict__ uT, u16* tile, int bid, int tid) {
  uint4 q[2][2];
  int r = tid >> 3, c = tid & 7;
#pragma unroll
  for (int it = 0; it < 2; ++it) {
    int tl = bid * 2 + it;
    int l0 = (tl >> 4) * 64, d0 = (tl & 15) * 64;
#pragma unroll
    for (int h = 0; h < 2; ++h)
      q[it][h] = *reinterpret_cast<const uint4*>(u + (size_t)(l0 + r + 32 * h) * DCH + d0 + 8 * c);
  }
#pragma unroll
  for (int it = 0; it < 2; ++it) {
    u16* tl_ = tile + it * 64 * 72;
#pragma unroll
    for (int h = 0; h < 2; ++h) {
      u32 w[4] = {q[it][h].x, q[it][h].y, q[it][h].z, q[it][h].w};
#pragma unroll
      for (int j = 0; j < 4; ++j) {
        tl_[(8 * c + 2 * j) * 72 + r + 32 * h] = (u16)(w[j] & 0xffffu);
        tl_[(8 * c + 2 * j + 1) * 72 + r + 32 * h] = (u16)(w[j] >> 16);
      }
    }
  }
  __syncthreads();
#pragma unroll
  for (int it = 0; it < 2; ++it) {
    int tl = bid * 2 + it;
    int l0 = (tl >> 4) * 64, d0 = (tl & 15) * 64;
    const u16* tl_ = tile + it * 64 * 72;
#pragma unroll
    for (int h = 0; h < 2; ++h) {
      uint4 o = *reinterpret_cast<const uint4*>(tl_ + (r + 32 * h) * 72 + 8 * c);
      { u32x4 oo = {o.x,o.y,o.z,o.w}; __builtin_nontemporal_store(oo, reinterpret_cast<u32x4*>(uT + (size_t)(d0 + r + 32 * h) * SEQ + l0 + 8 * c)); }
    }
  }
}

#define NSH 16
#define CSTR 64
typedef __attribute__((address_space(1))) unsigned gu32;

__global__ __launch_bounds__(256, 4) void kA(const u16* x, cf* X1T, const u16* wr, const u16* wi, u32* WT, cf* TW,
                                            const u16* u, u16* uT, u32* cnt) {
  __shared__ __attribute__((aligned(16))) cf sm[8 * RRS + 260];
  int b = blockIdx.x, tid = threadIdx.x;
  if (b < 256) {
    brow_block(x, X1T, sm, b, tid);
  } else if (b < 256 + 128) {
    int i = b - 256;
    if (i < 8) {
      int j = i * 256 + tid;
      float s, c;
      sincospif(-(float)j / 1024.0f, &s, &c);
      TW[j] = cmk(c, s);
    }
    if (i == 8 && tid < NSH) cnt[tid * CSTR] = 0u;
    wt_block(wr, wi, WT, reinterpret_cast<u32*>(sm), i, tid);
  } else {
    utrans_block(u, uT, reinterpret_cast<u16*>(sm), b - 384, tid);
  }
}

__global__ __launch_bounds__(256, 3) void kB(KParams P, const cf* X1T, cf* X2T, const u32* WT, const u32* uT,
                                            const cf* TW, const u16* Dskip, u16* out, u32* cnt) {
  __shared__ __attribute__((aligned(16))) u32 smw[4 * 32 * KREC];
  cf* sm = reinterpret_cast<cf*>(smw);
  int b = blockIdx.x, tid = threadIdx.x;
  int g = tid >> 7, t = tid & 127;
  if (b < 256) {
    __builtin_amdgcn_s_setprio(3);
    if (b == 0) bcol_block<true>(X1T, X2T, WT, TW, sm + g * XBUF, 2 * b + g, t);
    else bcol_block<false>(X1T, X2T, WT, TW, sm + g * XBUF, 2 * b + g, t);
    asm volatile("s_waitcnt vmcnt(0)" ::: "memory");
    __syncthreads();
    gu32* gc = (gu32*)cnt;
    if (tid == 0) __hip_atomic_fetch_add(gc + (b & (NSH - 1)) * CSTR, 1u, __ATOMIC_RELAXED, __HIP_MEMORY_SCOPE_AGENT);
    Tw16 tb = load_tw16(TW, 4 * (tid & 31));
    cf wk[5];
#pragma unroll
    for (int i = 0; i < 5; ++i) wk[i] = TW[(2 * ((tid >> 2) + 64 * i)) & 2047];
    if (tid < 64) {
      for (unsigned spins = 0; spins < (1u << 20); ++spins) {
        unsigned c = (tid < NSH) ? __hip_atomic_load(gc + tid * CSTR, __ATOMIC_RELAXED, __HIP_MEMORY_SCOPE_AGENT) : (256u / NSH);
        if (__all(c == 256u / NSH)) break;
        __builtin_amdgcn_s_sleep(2);
      }
      __builtin_amdgcn_fence(__ATOMIC_ACQUIRE, "agent");
      asm volatile("s_waitcnt vmcnt(0)" ::: "memory");
    }
    __syncthreads();
    browinv_block(X2T, TW, tb, wk, out, sm, b, tid);
  } else {
    int i = b - 256;
    int p = (i & 7) * 64 + (i >> 3);
    float* Kst = reinterpret_cast<float*>(smw + 11136);
    u32* ost = smw + 9728;
    kgen_block(P, Kst, smw, p, tid);
    __syncthreads();
    aconv_block(uT, Kst + g * KC_PITCH, TW, Dskip, ost + g * 1024, sm + g * XBUF, 2 * p + g, t);
    __syncthreads();
    u32* op = reinterpret_cast<u32*>(out + 2 * p);
#pragma unroll
    for (int j = 0; j < 4; ++j) {
      int q = tid + 256 * j;
      u32 w0 = ost[q], w1 = ost[1024 + q];
      op[(size_t)(2 * q) * DCH] = (w0 & 0xffffu) | (w1 << 16);
      op[(size_t)(2 * q + 1) * DCH] = (w0 >> 16) | (w1 & 0xffff0000u);
    }
  }
}

extern "C" void kernel_launch(void* const* d_in, const int* in_sizes, int n_in, void* d_out, int out_size,
                              void* d_ws, size_t ws_size, hipStream_t stream) {
  const u16* u = (const u16*)d_in[0];
  KParams P;
  P.Are = (const u16*)d_in[1];
  P.Aim = (const u16*)d_in[2];
  P.Bre = (const u16*)d_in[3];
  P.Bim = (const u16*)d_in[4];
  P.Cre = (const u16*)d_in[5];
  P.Cim = (const u16*)d_in[6];
  P.logdt = (const u16*)d_in[7];
  const u16* Dskip = (const u16*)d_in[8];
  const u16* x = (const u16*)d_in[9];
  const u16* wr = (const u16*)d_in[10];
  const u16* wi = (const u16*)d_in[11];
  u16* out = (u16*)d_out;
  char* ws = (char*)d_ws;
  cf* X1T = (cf*)ws;
  u32* WT = (u32*)(ws + (size_t)8 * 1024 * 1024);
  cf* X2T = (cf*)(ws + (size_t)16 * 1024 * 1024);
  u16* uT = (u16*)(ws + (size_t)24 * 1024 * 1024);
  cf* TW = (cf*)(ws + (size_t)28 * 1024 * 1024);
  u32* cnt = (u32*)(ws + (size_t)29 * 1024 * 1024);

  kA<<<256 + 128 + 256, 256, 0, stream>>>(x, X1T, wr, wi, WT, TW, u, uT, cnt);
  kB<<<768, 256, 0, stream>>>(P, X1T, X2T, WT, (const u32*)uT, TW, Dskip, out, cnt);
}
```

```cpp
#include <hip/hip_runtime.h>
#include <hip/hip_bf16.h>
#include <type_traits>

#define SEQ 2048
#define DCH 1024
#define NST 128
#define DF 513

typedef __attribute__((ext_vector_type(2))) float cf;
typedef unsigned int u32;
typedef unsigned short u16;
using f32x16 = __attribute__((ext_vector_type(16))) float;

template <int I, int N, class F>
__device__ __forceinline__ void static_for(F&& f) {
  if constexpr (I < N) {
    f(std::integral_constant<int, I>{});
    static_for<I + 1, N>(f);
  }
}

__device__ __forceinline__ cf cmk(float x, float y) { cf r; r.x = x; r.y = y; return r; }
__device__ __forceinline__ cf cadd(cf a, cf b) { return a + b; }
__device__ __forceinline__ cf csub(cf a, cf b) { return a - b; }
__device__ __forceinline__ cf add_pi(cf a, cf b) {
  cf r;
  asm("v_pk_add_f32 %0, %1, %2 op_sel:[0,1] op_sel_hi:[1,0] neg_lo:[0,1]" : "=v"(r) : "v"(a), "v"(b));
  return r;
}
__device__ __forceinline__ cf add_mi(cf a, cf b) {
  cf r;
  asm("v_pk_add_f32 %0, %1, %2 op_sel:[0,1] op_sel_hi:[1,0] neg_hi:[0,1]" : "=v"(r) : "v"(a), "v"(b));
  return r;
}
__device__ __forceinline__ cf cmul(cf a, cf b) {
  cf t, r;
  asm("v_pk_mul_f32 %0, %1, %2 op_sel:[0,0] op_sel_hi:[0,1]" : "=v"(t) : "v"(a), "v"(b));
  asm("v_pk_fma_f32 %0, %1, %2, %3 op_sel:[1,1,0] op_sel_hi:[1,0,1] neg_lo:[0,1,0]" : "=v"(r) : "v"(a), "v"(b), "v"(t));
  return r;
}
__device__ __forceinline__ cf cmad(cf a, cf b, cf c) {
  cf t, r;
  asm("v_pk_fma_f32 %0, %1, %2, %3 op_sel:[0,0,0] op_sel_hi:[0,1,1]" : "=v"(t) : "v"(a), "v"(b), "v"(c));
  asm("v_pk_fma_f32 %0, %1, %2, %3 op_sel:[1,1,0] op_sel_hi:[1,0,1] neg_lo:[0,1,0]" : "=v"(r) : "v"(a), "v"(b), "v"(t));
  return r;
}
__device__ __forceinline__ cf cmulc(cf a, cf b) {
  cf t, r;
  asm("v_pk_mul_f32 %0, %1, %2 op_sel:[0,0] op_sel_hi:[1,0]" : "=v"(t) : "v"(a), "v"(b));
  asm("v_pk_fma_f32 %0, %1, %2, %3 op_sel:[1,1,0] op_sel_hi:[0,1,1] neg_hi:[1,0,0]" : "=v"(r) : "v"(a), "v"(b), "v"(t));
  return r;
}
__device__ __forceinline__ cf cmadc(cf a, cf b, cf c) {
  cf t, r;
  asm("v_pk_fma_f32 %0, %1, %2, %3 op_sel:[0,0,0] op_sel_hi:[1,0,1]" : "=v"(t) : "v"(a), "v"(b), "v"(c));
  asm("v_pk_fma_f32 %0, %1, %2, %3 op_sel:[1,1,0] op_sel_hi:[0,1,1] neg_hi:[1,0,0]" : "=v"(r) : "v"(a), "v"(b), "v"(t));
  return r;
}
__device__ __forceinline__ cf addc(cf a, cf b) {
  cf r;
  asm("v_pk_add_f32 %0, %1, %2 neg_hi:[0,1]" : "=v"(r) : "v"(a), "v"(b));
  return r;
}
__device__ __forceinline__ cf oddc(cf a, cf b) {
  cf r;
  asm("v_pk_add_f32 %0, %1, %2 op_sel:[1,1] op_sel_hi:[0,0] neg_hi:[1,0]" : "=v"(r) : "v"(a), "v"(b));
  return r;
}
__device__ __forceinline__ cf conj_add_pi(cf a, cf b) {
  cf r;
  asm("v_pk_add_f32 %0, %1, %2 op_sel:[0,1] op_sel_hi:[1,0] neg_hi:[1,0]" : "=v"(r) : "v"(a), "v"(b));
  return r;
}
__device__ __forceinline__ cf cmulk(cf a, cf cs) {
  cf t, r;
  asm("v_pk_mul_f32 %0, %1, %2 op_sel:[0,0] op_sel_hi:[1,0]" : "=v"(t) : "v"(a), "s"(cs));
  asm("v_pk_fma_f32 %0, %1, %2, %3 op_sel:[1,1,0] op_sel_hi:[0,1,1] neg_lo:[1,0,0]" : "=v"(r) : "v"(a), "s"(cs), "v"(t));
  return r;
}
__device__ __forceinline__ cf csqr(cf a) { return cmk(fmaf(a.x, a.x, -(a.y * a.y)), 2.0f * a.x * a.y); }

__device__ __forceinline__ float bf2f(u32 h) { return __uint_as_float(h << 16); }
__device__ __forceinline__ u32 f2bf(float f) {
  __hip_bfloat16 b = __float2bfloat16(f);
  return (u32)(*reinterpret_cast<u16*>(&b));
}
__device__ __forceinline__ float bfround(float f) { return bf2f(f2bf(f)); }

__device__ __forceinline__ void wave_sync() {
  __builtin_amdgcn_fence(__ATOMIC_SEQ_CST, "wavefront");
  __builtin_amdgcn_wave_barrier();
  __builtin_amdgcn_fence(__ATOMIC_SEQ_CST, "wavefront");
}

constexpr double COS64_TAB[17] = {
    1.0, 0.99518472667219688624, 0.98078528040323044913, 0.95694033573220886494,
    0.92387953251128675613, 0.88192126434835502971, 0.83146961230254523708,
    0.77301045336273696081, 0.70710678118654752440, 0.63439328416364549822,
    0.55557023301960222474, 0.47139673682599764856, 0.38268343236508977173,
    0.29028467725446236764, 0.19509032201612826785, 0.09801714032956060199, 0.0};
constexpr double cos64(int j) {
  j = ((j % 64) + 64) % 64;
  if (j > 32) j = 64 - j;
  return (j <= 16) ? COS64_TAB[j] : -COS64_TAB[32 - j];
}
constexpr double sin64(int j) { return cos64(j - 16); }

template <int J, int DIR>
__device__ __forceinline__ cf tw64(cf a) {
  constexpr int j = ((J % 64) + 64) % 64;
  if constexpr (j == 0) {
    return a;
  } else if constexpr (j == 32) {
    return -a;
  } else if constexpr (j == 16) {
    return (DIR > 0) ? cmk(-a.y, a.x) : cmk(a.y, -a.x);
  } else if constexpr (j == 48) {
    return (DIR > 0) ? cmk(a.y, -a.x) : cmk(-a.y, a.x);
  } else {
    constexpr float c = (float)cos64(j);
    constexpr float s = (float)(DIR * sin64(j));
    return cmulk(a, cmk(c, s));
  }
}

template <int N, int DIR, int S>
__device__ __forceinline__ void fft_rec(const cf* in, cf* out) {
  if constexpr (N == 1) {
    out[0] = in[0];
  } else if constexpr (N == 2) {
    cf a = in[0], b = in[S];
    out[0] = cadd(a, b);
    out[1] = csub(a, b);
  } else {
    constexpr int Q = N / 4;
    cf t0[Q], t1[Q], t2[Q], t3[Q];
    fft_rec<Q, DIR, 4 * S>(in, t0);
    fft_rec<Q, DIR, 4 * S>(in + S, t1);
    fft_rec<Q, DIR, 4 * S>(in + 2 * S, t2);
    fft_rec<Q, DIR, 4 * S>(in + 3 * S, t3);
    static_for<0, Q>([&](auto ic) {
      constexpr int k = decltype(ic)::value;
      constexpr int M = 64 / N;
      cf a = t0[k];
      cf b = tw64<k * M, DIR>(t1[k]);
      cf d = tw64<3 * k * M, DIR>(t3[k]);
      cf s0, s1;
      if constexpr (2 * k * M == 16) {
        s0 = (DIR > 0) ? add_pi(a, t2[k]) : add_mi(a, t2[k]);
        s1 = (DIR > 0) ? add_mi(a, t2[k]) : add_pi(a, t2[k]);
      } else {
        cf c = tw64<2 * k * M, DIR>(t2[k]);
        s0 = cadd(a, c);
        s1 = csub(a, c);
      }
      cf s2 = cadd(b, d), s3 = csub(b, d);
      out[k] = cadd(s0, s2);
      out[k + Q] = (DIR > 0) ? add_pi(s1, s3) : add_mi(s1, s3);
      out[k + 2 * Q] = csub(s0, s2);
      out[k + 3 * Q] = (DIR > 0) ? add_mi(s1, s3) : add_pi(s1, s3);
    });
  }
}

template <int N, int DIR>
__device__ __forceinline__ void fft_reg(cf* v) {
  cf o[N];
  fft_rec<N, DIR, 1>(v, o);
#pragma unroll
  for (int i = 0; i < N; ++i) v[i] = o[i];
}

template <int DIR>
__device__ __forceinline__ void fft16_halfzero(cf* v) {
  cf a[8], b[8], oa[8], ob[8];
  static_for<0, 8>([&](auto ic) {
    constexpr int n = decltype(ic)::value;
    a[n] = v[n];
    b[n] = tw64<4 * n, DIR>(v[n]);
  });
  fft_rec<8, DIR, 1>(a, oa);
  fft_rec<8, DIR, 1>(b, ob);
#pragma unroll
  for (int k = 0; k < 8; ++k) {
    v[2 * k] = oa[k];
    v[2 * k + 1] = ob[k];
  }
}

struct Tw16 { cf w[4]; };

__device__ __forceinline__ Tw16 make_tw16(int n, int logN) {
  Tw16 b;
#pragma unroll
  for (int t = 0; t < 4; ++t) {
    int e = (n << t) & ((1 << logN) - 1);
    float s, c;
    sincospif(-2.0f * (float)e / (float)(1 << logN), &s, &c);
    b.w[t] = cmk(c, s);
  }
  return b;
}

__device__ __forceinline__ Tw16 load_tw16(const cf* __restrict__ TW, int n) {
  Tw16 b;
#pragma unroll
  for (int t = 0; t < 4; ++t) b.w[t] = TW[(n << t) & 2047];
  return b;
}

template <bool CONJ>
__device__ __forceinline__ void apply_tw16(cf* v, const Tw16& b) {
  cf p[16];
  p[1] = b.w[0];
  p[2] = b.w[1];
  p[4] = b.w[2];
  p[8] = b.w[3];
  asm volatile("" : "+v"(p[1]), "+v"(p[2]), "+v"(p[4]), "+v"(p[8]));
  static_for<3, 16>([&](auto ic) {
    constexpr int k = decltype(ic)::value;
    constexpr int top = (k >= 8) ? 8 : (k >= 4) ? 4 : 2;
    if constexpr (k != top) p[k] = cmul(p[k - top], p[top]);
  });
#pragma unroll
  for (int k = 1; k < 16; ++k) v[k] = CONJ ? cmulc(v[k], p[k]) : cmul(v[k], p[k]);
}

__device__ __forceinline__ void swap16(cf& a, cf& b) {
  asm volatile("s_nop 1" : "+v"(a), "+v"(b));
  auto r0 = __builtin_amdgcn_permlane16_swap(__float_as_uint(a.x), __float_as_uint(b.x), false, false);
  auto r1 = __builtin_amdgcn_permlane16_swap(__float_as_uint(a.y), __float_as_uint(b.y), false, false);
  a.x = __uint_as_float(r0[0]);
  b.x = __uint_as_float(r0[1]);
  a.y = __uint_as_float(r1[0]);
  b.y = __uint_as_float(r1[1]);
}

#define XP 152
#define XBUF (16 * XP)
typedef volatile __attribute__((address_space(3))) cf* ldsp;
__device__ __forceinline__ cf lds_ld(const cf* p) { return *(ldsp)(p); }
__device__ __forceinline__ void lds_st(cf* p, cf a) { *(ldsp)(p) = a; }

struct FCtx {
  Tw16 ta, tb;
  int t, offA, offB, kA, kB;
};
__device__ __forceinline__ FCtx make_fctx(const cf* __restrict__ TW, int t) {
  FCtx f;
  int k0 = t >> 3, c = t & 7;
  f.t = t;
  f.ta = load_tw16(TW, t);
  f.tb = load_tw16(TW, 16 * c);
  int k0m = (16 - k0) & 15;
  int k1m = (k0 != 0) ? (15 - c) : ((c != 0) ? (16 - c) : 8);
  f.offA = k0 * XP + 9 * c;
  f.offB = k0m * XP + 9 * k1m;
  f.kA = k0 + 16 * c;
  f.kB = k0m + 16 * k1m;
  return f;
}

__device__ __forceinline__ void fft2048_fwd_rest(cf* v, cf* buf, const FCtx& f) {
  apply_tw16<false>(v, f.ta);
  __syncthreads();
#pragma unroll
  for (int k0 = 0; k0 < 16; ++k0) lds_st(buf + k0 * XP + f.t, v[k0]);
  __syncthreads();
  cf* rb = buf + (f.t >> 3) * XP + (f.t & 7);
#pragma unroll
  for (int b = 0; b < 16; ++b) v[b] = lds_ld(rb + 8 * b);
  fft_reg<16, -1>(v);
  apply_tw16<false>(v, f.tb);
#pragma unroll
  for (int k1 = 0; k1 < 16; ++k1) lds_st(rb + 9 * k1, v[k1]);
  __syncthreads();
#pragma unroll
  for (int j = 0; j < 8; ++j) v[j] = lds_ld(buf + f.offA + j);
#pragma unroll
  for (int j = 0; j < 8; ++j) v[8 + j] = lds_ld(buf + f.offB + j);
  fft_reg<8, -1>(v);
  fft_reg<8, -1>(v + 8);
}

__device__ __forceinline__ void fft2048_inv_first(cf* v, cf* buf, const FCtx& f) {
  fft_reg<8, 1>(v);
  fft_reg<8, 1>(v + 8);
  __syncthreads();
#pragma unroll
  for (int j = 0; j < 8; ++j) lds_st(buf + f.offA + j, v[j]);
#pragma unroll
  for (int j = 0; j < 8; ++j) lds_st(buf + f.offB + j, v[8 + j]);
  __syncthreads();
  cf* rb = buf + (f.t >> 3) * XP + (f.t & 7);
#pragma unroll
  for (int k1 = 0; k1 < 16; ++k1) v[k1] = lds_ld(rb + 9 * k1);
  apply_tw16<true>(v, f.tb);
  fft_reg<16, 1>(v);
#pragma unroll
  for (int b = 0; b < 16; ++b) lds_st(rb + 8 * b, v[b]);
  __syncthreads();
#pragma unroll
  for (int kk = 0; kk < 16; ++kk) v[kk] = lds_ld(buf + kk * XP + f.t);
  apply_tw16<true>(v, f.ta);
}

#define KC_PITCH 648
#define KREC 104
using u32x4 = __attribute__((ext_vector_type(4))) unsigned;
using u32x2 = __attribute__((ext_vector_type(2))) unsigned;
using bf16x8 = __attribute__((ext_vector_type(8))) __bf16;

__device__ __forceinline__ cf cexp_scaled(float ar, float ai, float sc) {
  float e = expf(ar * sc);
  float s, c;
  sincosf(ai * sc, &s, &c);
  return cmk(e * c, e * s);
}
__device__ __forceinline__ u32 cvtpk(float a, float b) {
  u32 r;
  asm("v_cvt_pk_bf16_f32 %0, %1, %2" : "=v"(r) : "v"(a), "v"(b));
  return r;
}
__device__ __forceinline__ u32x2 split2(cf g) {
  u32x2 o;
  o.x = cvtpk(g.x, g.y);
  cf h = cmk(__uint_as_float(o.x << 16), __uint_as_float(o.x & 0xffff0000u));
  cf r = g - h;
  o.y = cvtpk(r.x, r.y);
  return o;
}

struct KParams { const u16 *Are, *Aim, *Bre, *Bim, *Cre, *Cim, *logdt; };

__device__ __forceinline__ void kgen_block(const KParams& P, float* Kst, u32* lds_all, int bid, int tid) {
  int wave = tid >> 6, lane = tid & 63;
  int d = bid * 2 + (wave >> 1);
  u32* lds = lds_all + wave * 32 * KREC;
  float dt = bfround(expf(bf2f(P.logdt[d])));
  int idx = d * NST + (wave & 1) * 64 + lane;
  float ar = dt * bf2f(P.Are[idx]), ai = dt * bf2f(P.Aim[idx]);
  cf dB = cmk(dt * bf2f(P.Bre[idx]), dt * bf2f(P.Bim[idx]));
  cf C = cmk(bf2f(P.Cre[idx]), bf2f(P.Cim[idx]));
  cf c0 = cmul(C, dB);
  cf e[12];
  e[0] = cexp_scaled(ar, ai, 1.0f);
  e[4] = cexp_scaled(ar, ai, 16.0f);
  e[8] = cexp_scaled(ar, ai, 256.0f);
  e[1] = csqr(e[0]); e[2] = csqr(e[1]); e[3] = csqr(e[2]);
  e[5] = csqr(e[4]); e[6] = csqr(e[5]); e[7] = csqr(e[6]);
  e[9] = csqr(e[8]); e[10] = csqr(e[9]); e[11] = csqr(e[10]);
  typedef volatile __attribute__((address_space(3))) u32x2* lp2;
  float s2048 = fmaf(c0.x, e[11].x, -(c0.y * e[11].y));
  {
    auto dpp = [](float x, auto ctl, auto rm) {
      return __int_as_float(__builtin_amdgcn_update_dpp(0, __float_as_int(x), decltype(ctl)::value, decltype(rm)::value, 0xf, true));
    };
    using std::integral_constant;
    s2048 += dpp(s2048, integral_constant<int, 0x111>{}, integral_constant<int, 0xf>{});
    s2048 += dpp(s2048, integral_constant<int, 0x112>{}, integral_constant<int, 0xf>{});
    s2048 += dpp(s2048, integral_constant<int, 0x114>{}, integral_constant<int, 0xf>{});
    s2048 += dpp(s2048, integral_constant<int, 0x118>{}, integral_constant<int, 0xf>{});
    s2048 += dpp(s2048, integral_constant<int, 0x142>{}, integral_constant<int, 0xa>{});
    s2048 += dpp(s2048, integral_constant<int, 0x143>{}, integral_constant<int, 0xc>{});
  }
  u32x2 Gw[20], Ew[32];
  const int rot = 2 * ((lane >> 2) & 3);
  u32* rec0 = lds + (lane & 31) * KREC;
  u32* rec = rec0 + rot;
  {
    cf G[20];
    G[0] = cmk(c0.x, -c0.y);
    G[1] = cmulc(G[0], e[0]);
    G[2] = cmulc(G[0], e[1]);
    G[3] = cmulc(G[1], e[1]);
    G[4] = cmulc(G[0], e[2]);
    G[5] = cmulc(G[1], e[2]);
    G[6] = cmulc(G[2], e[2]);
    G[7] = cmulc(G[3], e[2]);
    G[8] = cmulc(G[0], e[8]);
    G[9] = cmulc(G[2], e[8]);
    G[10] = cmulc(G[4], e[8]);
    G[11] = cmulc(G[6], e[8]);
    G[12] = cmulc(G[0], e[9]);
    G[13] = cmulc(G[4], e[9]);
    G[14] = cmulc(G[8], e[9]);
    G[15] = cmulc(G[10], e[9]);
    G[16] = cmulc(G[0], e[10]);
    G[17] = cmulc(G[8], e[10]);
    G[18] = cmulc(G[12], e[10]);
    G[19] = cmulc(G[14], e[10]);
#pragma unroll
    for (int i = 0; i < 20; ++i) Gw[i] = split2(G[i]);
  }
  if (lane < 32) {
#pragma unroll
    for (int i = 0; i < 20; ++i) *(lp2)(rec + 2 * i) = Gw[i];
  }
  {
    cf E[32];
    E[0] = cmk(1.f, 0.f);
    E[1] = e[3]; E[2] = e[4]; E[4] = e[5]; E[8] = e[6]; E[16] = e[7];
    static_for<3, 32>([&](auto ic) {
      constexpr int k = decltype(ic)::value;
      constexpr int top = (k >= 16) ? 16 : (k >= 8) ? 8 : (k >= 4) ? 4 : 2;
      if constexpr (k != top) E[k] = cmul(E[k - top], E[top]);
    });
    Ew[0].x = 0x3f80u;
    Ew[0].y = 0u;
#pragma unroll
    for (int i = 1; i < 32; ++i) Ew[i] = split2(E[i]);
  }
  f32x16 acc;
#pragma unroll
  for (int i = 0; i < 16; ++i) acc[i] = 0.f;
  const u32* base = lds + (2 * (lane >> 5)) * KREC;
  int offB[4];
#pragma unroll
  for (int q = 0; q < 4; ++q) {
    int w = 40 + 2 * (lane & 31) + 2 * q;
    offB[q] = (w >= KREC) ? w - KREC : w;
  }
#pragma unroll
  for (int ch = 0; ch < 2; ++ch) {
    if ((lane >> 5) == ch) {
      if (ch) {
#pragma unroll
        for (int i = 0; i < 20; ++i) *(lp2)(rec + 2 * i) = Gw[i];
      }
#pragma unroll
      for (int i = 0; i < 29; ++i) *(lp2)(rec + 40 + 2 * i) = Ew[i];
#pragma unroll
      for (int i = 29; i < 32; ++i) {
        int w = 40 + 2 * i + rot;
        *(lp2)(rec0 + ((w >= KREC) ? w - KREC : w)) = Ew[i];
      }
    }
#pragma unroll
    for (int m = 0; m < 8; ++m) {
      const u32* r0 = base + 4 * m * KREC;
      const u32* ra = r0 + 2 * (lane & 31) + 2 * (m & 3);
      const u32* rb = r0 + offB[m & 3];
      u32x2 a0 = *(lp2)(ra), a1 = *(lp2)(ra + KREC), b0 = *(lp2)(rb), b1 = *(lp2)(rb + KREC);
      u32x4 A = {a0.x, a0.y, a1.x, a1.y}, A2 = {a0.y, a0.x, a1.y, a1.x}, B = {b0.x, b0.y, b1.x, b1.y};
      acc = __builtin_amdgcn_mfma_f32_32x32x16_bf16(__builtin_bit_cast(bf16x8, A), __builtin_bit_cast(bf16x8, B), acc, 0, 0, 0);
      acc = __builtin_amdgcn_mfma_f32_32x32x16_bf16(__builtin_bit_cast(bf16x8, A2), __builtin_bit_cast(bf16x8, B), acc, 0, 0, 0);
    }
  }
  float* red = reinterpret_cast<float*>(lds_all + (wave | 1) * 32 * KREC);
  wave_sync();
  if (wave & 1) {
#pragma unroll
    for (int r = 0; r < 16; ++r) red[r * 64 + lane] = acc[r];
    if (lane == 63) red[1024] = s2048;
  }
  __syncthreads();
  if (!(wave & 1)) {
    int col = lane & 31, hh = lane >> 5;
    float* dst = Kst + (wave >> 1) * KC_PITCH;
#pragma unroll
    for (int r = 0; r < 16; ++r) {
      int row = (r & 3) + 8 * (r >> 2) + 4 * hh;
      int p = -1;
      if (row < 8) p = row + 8 * col;
      else if (row < 12) p = 256 + (row - 8) + 4 * col;
      else if (row < 16) p = 384 + ((row - 12) & 1) + 64 * ((row - 12) >> 1) + 2 * col;
      else if (row < 20) p = 512 + 32 * (row - 16) + col;
      if (p >= 0) dst[p] = acc[r] + red[r * 64 + lane];
    }
    if (lane == 63) dst[640] = s2048 + red[1024];
  }
}

__device__ __forceinline__ void wt_block(const u16* __restrict__ wr, const u16* __restrict__ wi, u32* __restrict__ WT,
                                         u32* tile, int bid, int tid) {
  int s0 = bid * 16;
  const u32* pr = reinterpret_cast<const u32*>(wr + (size_t)s0 * DF);
  const u32* pi = reinterpret_cast<const u32*>(wi + (size_t)s0 * DF);
  u32 a[17], b[17];
#pragma unroll
  for (int i = 0; i < 17; ++i) {
    int w = min(tid + 256 * i, 8 * DF - 1);
    a[i] = pr[w];
    b[i] = pi[w];
  }
#pragma unroll
  for (int i = 0; i < 17; ++i) {
    int w = tid + 256 * i;
    if (w < 8 * DF) {
      int e0 = 2 * w;
      int r = e0 / DF, k = e0 - r * DF;
      tile[k * 17 + r] = (a[i] & 0xffffu) | (b[i] << 16);
      int k1 = k + 1, r1 = r;
      if (k1 == DF) { k1 = 0; r1 = r + 1; }
      tile[k1 * 17 + r1] = (a[i] >> 16) | (b[i] & 0xffff0000u);
    }
  }
  __syncthreads();
  int sx = tid & 15, ky = tid >> 4;
#pragma unroll
  for (int i = 0; i < 33; ++i) {
    int k = ky + 16 * i;
    if (k < DF) __builtin_nontemporal_store(tile[k * 17 + sx], WT + (size_t)k * SEQ + s0 + sx);
  }
}

#define RP 34
#define RRS (16 * RP)
#define ZP 517
#define TWOFF (16 * RRS)

__device__ __forceinline__ int pos2row(int p) { return 128 * (2 * (p >> 8) + (p & 1)) + ((p & 255) >> 1); }

__device__ __forceinline__ void brow_block(const u16* __restrict__ x, cf* __restrict__ X1T, cf* sm, int bid, int tid) {
  cf* tw = sm + 8 * RRS;
  int s0 = bid * 8;
  {
    float s, c;
    sincospif(-(float)tid / 512.0f, &s, &c);
    tw[tid] = cmk(c, s);
    if (tid == 0) tw[256] = cmk(0.f, -1.f);
  }
  int row = tid >> 5, t = tid & 31;
  cf v[16];
  const u32* xr = reinterpret_cast<const u32*>(x + (size_t)pos2row(s0 + row) * DCH);
#pragma unroll
  for (int a = 0; a < 16; ++a) {
    u32 w = xr[32 * a + t];
    v[a] = cmk(bf2f(w & 0xffffu), bf2f(w >> 16));
  }
  Tw16 tb = make_tw16(t, 9);
  fft_reg<16, -1>(v);
  apply_tw16<false>(v, tb);
  cf* rb = sm + row * RRS;
  {
    int pos = (t & 1) * 16 + (t >> 1);
#pragma unroll
    for (int k0 = 0; k0 < 16; ++k0) rb[k0 * RP + pos] = v[k0];
  }
  wave_sync();
  int k0 = t & 15, h = t >> 4;
  {
    const float4* p = reinterpret_cast<const float4*>(rb + k0 * RP + h * 16);
#pragma unroll
    for (int m = 0; m < 8; ++m) {
      float4 q = p[m];
      v[2 * m] = cmk(q.x, q.y);
      v[2 * m + 1] = cmk(q.z, q.w);
    }
  }
  fft_reg<16, -1>(v);
  bool hi = h != 0;
  static_for<0, 8>([&](auto ic) {
    constexpr int j = decltype(ic)::value;
    cf a = v[j], b = v[j + 8];
    swap16(a, b);
    cf br = cmk(hi ? b.y : b.x, hi ? -b.x : b.y);
    br = tw64<2 * j, -1>(br);
    v[j] = cadd(a, br);
    v[j + 8] = csub(a, br);
  });
  __syncthreads();
  cf* zs = sm + row * ZP;
#pragma unroll
  for (int r = 0; r < 16; ++r) zs[k0 + 16 * ((r & 7) + 8 * h + 16 * (r >> 3))] = v[r];
  __syncthreads();
  int r = tid & 7, kk = tid >> 3;
  const cf* zr = sm + r * ZP;
#pragma unroll
  for (int i = 0; i < 9; ++i) {
    int k = kk + 32 * i;
    if (k > 256) break;
    cf a = zr[k];
    cf b = zr[(512 - k) & 511];
    if (k == 0) {
      __builtin_nontemporal_store(cmk(a.x + a.y, a.x - a.y), X1T + (size_t)0 * SEQ + s0 + r);
    } else {
      cf w = tw[k];
      cf ev = cmk(0.5f * (a.x + b.x), 0.5f * (a.y - b.y));
      cf od = cmk(0.5f * (a.y + b.y), -0.5f * (a.x - b.x));
      cf wo = cmul(od, w);
      __builtin_nontemporal_store(cadd(ev, wo), X1T + (size_t)k * SEQ + s0 + r);
      if (k != 256) __builtin_nontemporal_store(cmk(ev.x - wo.x, -(ev.y - wo.y)), X1T + (size_t)(512 - k) * SEQ + s0 + r);
    }
  }
}

__device__ __forceinline__ void browinv_block(const cf* X2T, const cf* __restrict__ TW, const Tw16& tb,
                                              const cf* wk, u16* __restrict__ out, cf* sm, int bid, int tid) {
  int s0 = bid * 8;
  {
    int rp = tid & 3, kq = tid >> 2;
    float4 a[5], b[5];
#pragma unroll
    for (int i = 0; i < 5; ++i) {
      int k = kq + 64 * i;
      if (k <= 256) {
        a[i] = *reinterpret_cast<const float4*>(X2T + (size_t)k * SEQ + s0 + 2 * rp);
        b[i] = *reinterpret_cast<const float4*>(X2T + (size_t)((512 - k) & 511) * SEQ + s0 + 2 * rp);
      }
    }
#pragma unroll
    for (int i = 0; i < 5; ++i) {
      int k = kq + 64 * i;
      if (k <= 256) {
#pragma unroll
        for (int h = 0; h < 2; ++h) {
          cf* zr = sm + (2 * rp + h) * ZP;
          cf aa = h ? cmk(a[i].z, a[i].w) : cmk(a[i].x, a[i].y);
          cf bb = h ? cmk(b[i].z, b[i].w) : cmk(b[i].x, b[i].y);
          if (k == 0) {
            zr[0] = cmk(aa.x + aa.y, aa.x - aa.y);
          } else {
            cf w = wk[i];
            cf ev = cmk(aa.x + bb.x, aa.y - bb.y);
            cf df = cmk(aa.x - bb.x, aa.y + bb.y);
            cf od = cmulc(df, w);
            zr[k] = cmk(ev.x - od.y, ev.y + od.x);
            if (k != 256) zr[512 - k] = cmk(ev.x + od.y, -ev.y + od.x);
          }
        }
      }
    }
  }
  __syncthreads();
  int row = tid >> 5, t = tid & 31;
  int k0 = t & 15, h = t >> 4;
  cf v[16];
  {
    const cf* zs = sm + row * ZP;
#pragma unroll
    for (int r = 0; r < 16; ++r) v[r] = zs[k0 + 16 * ((r & 7) + 8 * h + 16 * (r >> 3))];
  }
  __syncthreads();
  bool hi = h != 0;
  static_for<0, 8>([&](auto ic) {
    constexpr int j = decltype(ic)::value;
    cf a = cadd(v[j], v[j + 8]);
    cf d = csub(v[j], v[j + 8]);
    cf dr = cmk(hi ? -d.y : d.x, hi ? d.x : d.y);
    dr = tw64<2 * j, 1>(dr);
    swap16(a, dr);
    v[j] = a;
    v[j + 8] = dr;
  });
  fft_reg<16, 1>(v);
  cf* rb = sm + row * RRS;
  {
    float4* p = reinterpret_cast<float4*>(rb + k0 * RP + h * 16);
#pragma unroll
    for (int m = 0; m < 8; ++m) p[m] = make_float4(v[2 * m].x, v[2 * m].y, v[2 * m + 1].x, v[2 * m + 1].y);
  }
  wave_sync();
  {
    int pos = (t & 1) * 16 + (t >> 1);
#pragma unroll
    for (int kk = 0; kk < 16; ++kk) v[kk] = rb[kk * RP + pos];
  }
  apply_tw16<true>(v, tb);
  fft_reg<16, 1>(v);
  u32* orow = reinterpret_cast<u32*>(out + (size_t)pos2row(s0 + row) * (2 * DCH) + DCH);
#pragma unroll
  for (int a = 0; a < 16; ++a) orow[32 * a + t] = cvtpk(v[a].x, v[a].y);
}

__device__ __forceinline__ cf ldw(const u32* p, int i) {
  u32 w = p[i];
  return cmk(bf2f(w & 0xffffu), bf2f(w >> 16));
}

template <bool SPECIAL>
__device__ __forceinline__ void bcol_block(const cf* __restrict__ X1T, cf* __restrict__ X2T, const u32* __restrict__ WT,
                                           const cf* __restrict__ TW, cf* buf, int col, int t) {
  cf v[16];
  const cf* xc = X1T + (size_t)col * SEQ;
#pragma unroll
  for (int a = 0; a < 8; ++a) {
    float4 q = *reinterpret_cast<const float4*>(xc + 256 * a + 2 * t);
    v[2 * a] = cmk(q.x, q.y);
    v[2 * a + 1] = cmk(q.z, q.w);
  }
  FCtx f = make_fctx(TW, t);
  const float sc = 1.0f / (2048.0f * 1024.0f);
  const u32* wc = WT + (size_t)col * SEQ;
  u32 wq[16];
#pragma unroll
  for (int j = 0; j < 8; ++j) {
    wq[j] = wc[f.kA + 256 * j];
    wq[8 + j] = wc[f.kB + 256 * j];
  }
  fft_reg<16, -1>(v);
  fft2048_fwd_rest(v, buf, f);
  const bool sp = SPECIAL && (col == 0);
  if (!sp) {
#pragma unroll
    for (int r = 0; r < 16; ++r) {
      cf w = cmk(bf2f(wq[r] & 0xffffu) * sc, bf2f(wq[r] >> 16) * sc);
      v[r] = cmul(v[r], w);
    }
  }
  if (SPECIAL) {
    __syncthreads();
    if (sp) {
#pragma unroll
      for (int j = 0; j < 8; ++j) {
        buf[f.kA + 256 * j] = v[j];
        buf[f.kB + 256 * j] = v[8 + j];
      }
    }
    __syncthreads();
    if (sp) {
      const u32* w0 = WT;
      const u32* w1 = WT + (size_t)512 * SEQ;
#pragma unroll 1
      for (int k = t; k <= 1024; k += 128) {
        int km = (2048 - k) & 2047;
        cf a = buf[k], b = buf[km];
        cf A = cmk(0.5f * (a.x + b.x), 0.5f * (a.y - b.y));
        cf B = cmk(0.5f * (a.y + b.y), -0.5f * (a.x - b.x));
        cf p = ldw(w0, k), q = ldw(w0, km);
        cf wh0 = cmk(0.5f * sc * (p.x + q.x), 0.5f * sc * (p.y - q.y));
        p = ldw(w1, k); q = ldw(w1, km);
        cf wh1 = cmk(0.5f * sc * (p.x + q.x), 0.5f * sc * (p.y - q.y));
        cf h0 = cmul(A, wh0), h1 = cmul(B, wh1);
        buf[km] = cmk(h0.x + h1.y, -h0.y + h1.x);
        buf[k] = cmk(h0.x - h1.y, h0.y + h1.x);
      }
    }
    __syncthreads();
    if (sp) {
#pragma unroll
      for (int j = 0; j < 8; ++j) {
        v[j] = buf[f.kA + 256 * j];
        v[8 + j] = buf[f.kB + 256 * j];
      }
    }
  }
  fft2048_inv_first(v, buf, f);
  fft_reg<16, 1>(v);
  __amdgpu_buffer_rsrc_t rs = __builtin_amdgcn_make_buffer_rsrc((void*)X2T, 0, 512 * SEQ * 8, 0x00020000);
  int off = (col * SEQ + 2 * t) * 8;
#pragma unroll
  for (int a = 0; a < 8; ++a) {
    u32x4 q = {__float_as_uint(v[2 * a].x), __float_as_uint(v[2 * a].y), __float_as_uint(v[2 * a + 1].x),
               __float_as_uint(v[2 * a + 1].y)};
    __builtin_amdgcn_raw_buffer_store_b128(q, rs, off + 2048 * a, 0, 16);
  }
}

__device__ __forceinline__ int kpos(int l, int s) {
  if (s == 0) return l;
  int half = 1 << (s - 1);
  int q = (l + half - 1 + ((l >> s) & 1)) >> s;
  return 128 * (s + 1) + q - 128;
}
__device__ __forceinline__ cf csel(bool c, cf a, cf b) { return cmk(c ? a.x : b.x, c ? a.y : b.y); }

__device__ __forceinline__ void aconv_block(const u32* __restrict__ uT, const float* kl, const cf* __restrict__ TW,
                                            const u16* __restrict__ Dskip, u32* ost, cf* buf, int d, int t) {
  cf v[16], kz[16];
  u32 uw[8];
  {
    static_for<0, 8>([&](auto ic) {
      constexpr int a = decltype(ic)::value;
      constexpr int s = (a < 1) ? 0 : (a < 2) ? 1 : (a < 4) ? 2 : 3;
      int l = 256 * a + 2 * t;
      kz[a] = cmk(kl[kpos(l, s)], kl[kpos(l + 1, s)]);
    });
    const u32* ur = uT + (size_t)d * (SEQ / 2);
#pragma unroll
    for (int a = 0; a < 8; ++a) uw[a] = ur[128 * a + t];
  }
  FCtx f = make_fctx(TW, t);
  cf L = TW[f.kA];
  cf L2 = TW[t == 0 ? 128 : f.kA];
  fft16_halfzero<-1>(kz);
  fft2048_fwd_rest(kz, buf, f);
#pragma unroll
  for (int a = 0; a < 8; ++a) v[a] = cmk(bf2f(uw[a] & 0xffffu), bf2f(uw[a] >> 16)) * (0.5f / 2048.0f);
  fft16_halfzero<-1>(v);
  fft2048_fwd_rest(v, buf, f);
  {
    const bool sp = (t == 0);
    cf oP[8], oQ[8];
    static_for<0, 8>([&](auto ic) {
      constexpr int j = decltype(ic)::value;
      cf a, b, ka, kb;
      if constexpr (j == 0) {
        a = v[0]; b = v[15]; ka = kz[0]; kb = kz[15];
      } else if constexpr (j < 4) {
        a = v[j]; ka = kz[j];
        b = csel(sp, v[8 - j], v[15 - j]);
        kb = csel(sp, kz[8 - j], kz[15 - j]);
      } else {
        a = csel(sp, v[8 + j], v[j]);
        ka = csel(sp, kz[8 + j], kz[j]);
        b = v[15 - j]; kb = kz[15 - j];
      }
      cf eu = addc(a, b);
      cf ou = oddc(a, b) * 0.5f;
      cf ek = addc(ka, kb);
      cf ok = oddc(ka, kb);
      cf w = tw64<8 * j, -1>((j < 4) ? L : L2);
      cf wo = cmul(ok, w);
      cf T = add_pi(wo, ek);
      cf T2 = conj_add_pi(wo, ek);
      oP[j] = cmad(ou, T, cmul(eu, ka));
      oQ[j] = cmadc(T2, ou, cmulc(kb, eu));
    });
    cf s0, s4;
    {
      float eu = 2.0f * v[0].x, ou = 2.0f * v[0].y;
      s0 = cmk(fmaf(eu, kz[0].x, ou * kz[0].y), fmaf(eu, kz[0].y, ou * kz[0].x));
      eu = 2.0f * v[4].x; ou = 2.0f * v[4].y;
      s4 = cmk(fmaf(eu, kz[4].x, -(ou * kz[4].y)), fmaf(eu, kz[4].y, ou * kz[4].x));
    }
    v[0] = csel(sp, s0, oP[0]);
    v[1] = oP[1];
    v[2] = oP[2];
    v[3] = oP[3];
    v[4] = csel(sp, s4, oP[4]);
    v[5] = csel(sp, oQ[3], oP[5]);
    v[6] = csel(sp, oQ[2], oP[6]);
    v[7] = csel(sp, oQ[1], oP[7]);
    v[8] = oQ[7];
    v[9] = oQ[6];
    v[10] = oQ[5];
    v[11] = oQ[4];
    v[12] = csel(sp, oP[4], oQ[3]);
    v[13] = csel(sp, oP[5], oQ[2]);
    v[14] = csel(sp, oP[6], oQ[1]);
    v[15] = csel(sp, oP[7], oQ[0]);
  }
  fft2048_inv_first(v, buf, f);
  fft_reg<16, 1>(v);
  float D = bf2f(Dskip[d]);
#pragma unroll
  for (int a = 0; a < 8; ++a) {
    u32 yb = cvtpk(v[a].x, v[a].y);
    u32 sb = cvtpk(bf2f(uw[a] & 0xffffu) * D, bf2f(uw[a] >> 16) * D);
    ost[128 * a + t] = cvtpk(bf2f(yb & 0xffffu) + bf2f(sb & 0xffffu), bf2f(yb >> 16) + bf2f(sb >> 16));
  }
}

__device__ __forceinline__ void utrans_block(const u16* __restrict__ u, u16* __restrict__ uT, u16* tile, int bid, int tid) {
  uint4 q[2][2];
  int r = tid >> 3, c = tid & 7;
#pragma unroll
  for (int it = 0; it < 2; ++it) {
    int tl = bid * 2 + it;
    int l0 = (tl >> 4) * 64, d0 = (tl & 15) * 64;
#pragma unroll
    for (int h = 0; h < 2; ++h)
      q[it][h] = *reinterpret_cast<const uint4*>(u + (size_t)(l0 + r + 32 * h) * DCH + d0 + 8 * c);
  }
#pragma unroll
  for (int it = 0; it < 2; ++it) {
    u16* tl_ = tile + it * 64 * 72;
#pragma unroll
    for (int h = 0; h < 2; ++h) {
      u32 w[4] = {q[it][h].x, q[it][h].y, q[it][h].z, q[it][h].w};
#pragma unroll
      for (int j = 0; j < 4; ++j) {
        tl_[(8 * c + 2 * j) * 72 + r + 32 * h] = (u16)(w[j] & 0xffffu);
        tl_[(8 * c + 2 * j + 1) * 72 + r + 32 * h] = (u16)(w[j] >> 16);
      }
    }
  }
  __syncthreads();
#pragma unroll
  for (int it = 0; it < 2; ++it) {
    int tl = bid * 2 + it;
    int l0 = (tl >> 4) * 64, d0 = (tl & 15) * 64;
    const u16* tl_ = tile + it * 64 * 72;
#pragma unroll
    for (int h = 0; h < 2; ++h) {
      uint4 o = *reinterpret_cast<const uint4*>(tl_ + (r + 32 * h) * 72 + 8 * c);
      { u32x4 oo = {o.x,o.y,o.z,o.w}; __builtin_nontemporal_store(oo, reinterpret_cast<u32x4*>(uT + (size_t)(d0 + r + 32 * h) * SEQ + l0 + 8 * c)); }
    }
  }
}

#define TS(k)
#define NSH 16
#define CSTR 64
typedef __attribute__((address_space(1))) unsigned gu32;

__global__ __launch_bounds__(256, 4) void kA(const u16* x, cf* X1T, const u16* wr, const u16* wi, u32* WT, cf* TW,
                                            const u16* u, u16* uT, u32* cnt) {
  __shared__ __attribute__((aligned(16))) cf sm[8 * RRS + 260];
  int b = blockIdx.x, tid = threadIdx.x;
  if (b < 256) {
    brow_block(x, X1T, sm, b, tid);
  } else if (b < 256 + 128) {
    int i = b - 256;
    if (i < 8) {
      int j = i * 256 + tid;
      float s, c;
      sincospif(-(float)j / 1024.0f, &s, &c);
      TW[j] = cmk(c, s);
    }
    if (i == 8 && tid < NSH) cnt[tid * CSTR] = 0u;
    wt_block(wr, wi, WT, reinterpret_cast<u32*>(sm), i, tid);
  } else {
    utrans_block(u, uT, reinterpret_cast<u16*>(sm), b - 384, tid);
  }
}

__global__ __launch_bounds__(256, 3) void kB(KParams P, const cf* X1T, cf* X2T, const u32* WT, const u32* uT,
                                            const cf* TW, const u16* Dskip, u16* out, u32* cnt) {
  __shared__ __attribute__((aligned(16))) u32 smw[4 * 32 * KREC];
  cf* sm = reinterpret_cast<cf*>(smw);
  int b = blockIdx.x, tid = threadIdx.x;
  int g = tid >> 7, t = tid & 127;
  TS(0);
  if (b < 256) {
    __builtin_amdgcn_s_setprio(3);
    if (b == 0) bcol_block<true>(X1T, X2T, WT, TW, sm + g * XBUF, 2 * b + g, t);
    else bcol_block<false>(X1T, X2T, WT, TW, sm + g * XBUF, 2 * b + g, t);
    TS(1);
    asm volatile("s_waitcnt vmcnt(0)" ::: "memory");
    __syncthreads();
    TS(2);
    gu32* gc = (gu32*)cnt;
    if (tid == 0) __hip_atomic_fetch_add(gc + (b & (NSH - 1)) * CSTR, 1u, __ATOMIC_RELAXED, __HIP_MEMORY_SCOPE_AGENT);
    Tw16 tb = load_tw16(TW, 4 * (tid & 31));
    cf wk[5];
#pragma unroll
    for (int i = 0; i < 5; ++i) wk[i] = TW[(2 * ((tid >> 2) + 64 * i)) & 2047];
    if (tid < 64) {
      for (unsigned spins = 0; spins < (1u << 20); ++spins) {
        unsigned c = (tid < NSH) ? __hip_atomic_load(gc + tid * CSTR, __ATOMIC_RELAXED, __HIP_MEMORY_SCOPE_AGENT) : (256u / NSH);
        if (__all(c == 256u / NSH)) break;
        __builtin_amdgcn_s_sleep(2);
      }
      TS(3);
      __builtin_amdgcn_fence(__ATOMIC_ACQUIRE, "agent");
      asm volatile("s_waitcnt vmcnt(0)" ::: "memory");
    }
    __syncthreads();
    TS(4);
    browinv_block(X2T, TW, tb, wk, out, sm, b, tid);
    TS(5);
    asm volatile("s_waitcnt vmcnt(0)" ::: "memory");
    TS(6);
  } else {
    int i = b - 256;
    int p = (i & 7) * 64 + (i >> 3);
    float* Kst = reinterpret_cast<float*>(smw + 11136);
    u32* ost = smw + 9728;
    kgen_block(P, Kst, smw, p, tid);
    __syncthreads();
    TS(1);
    aconv_block(uT, Kst + g * KC_PITCH, TW, Dskip, ost + g * 1024, sm + g * XBUF, 2 * p + g, t);
    __syncthreads();
    TS(2);
    u32* op = reinterpret_cast<u32*>(out + 2 * p);
#pragma unroll
    for (int j = 0; j < 4; ++j) {
      int q = tid + 256 * j;
      u32 w0 = ost[q], w1 = ost[1024 + q];
      op[(size_t)(2 * q) * DCH] = (w0 & 0xffffu) | (w1 << 16);
      op[(size_t)(2 * q + 1) * DCH] = (w0 >> 16) | (w1 & 0xffff0000u);
    }
    TS(3);
    TS(4);
  }
}

extern "C" void kernel_launch(void* const* d_in, const int* in_sizes, int n_in, void* d_out, int out_size,
                              void* d_ws, size_t ws_size, hipStream_t stream) {
  const u16* u = (const u16*)d_in[0];
  KParams P;
  P.Are = (const u16*)d_in[1];
  P.Aim = (const u16*)d_in[2];
  P.Bre = (const u16*)d_in[3];
  P.Bim = (const u16*)d_in[4];
  P.Cre = (const u16*)d_in[5];
  P.Cim = (const u16*)d_in[6];
  P.logdt = (const u16*)d_in[7];
  const u16* Dskip = (const u16*)d_in[8];
  const u16* x = (const u16*)d_in[9];
  const u16* wr = (const u16*)d_in[10];
  const u16* wi = (const u16*)d_in[11];
  u16* out = (u16*)d_out;
  char* ws = (char*)d_ws;
  cf* X1T = (cf*)ws;
  u32* WT = (u32*)(ws + (size_t)8 * 1024 * 1024);
  cf* X2T = (cf*)(ws + (size_t)16 * 1024 * 1024);
  u16* uT = (u16*)(ws + (size_t)24 * 1024 * 1024);
  cf* TW = (cf*)(ws + (size_t)28 * 1024 * 1024);
  u32* cnt = (u32*)(ws + (size_t)29 * 1024 * 1024);

  kA<<<256 + 128 + 256, 256, 0, stream>>>(x, X1T, wr, wi, WT, TW, u, uT, cnt);
  kB<<<768, 256, 0, stream>>>(P, X1T, X2T, WT, (const u32*)uT, TW, Dskip, out, cnt);
}
```

```cpp
#include <hip/hip_runtime.h>
#include <hip/hip_bf16.h>
#include <type_traits>

#define SEQ 2048
#define DCH 1024
#define NST 128
#define DF 513

typedef __attribute__((ext_vector_type(2))) float cf;
typedef unsigned int u32;
typedef unsigned short u16;
using f32x16 = __attribute__((ext_vector_type(16))) float;

template <int I, int N, class F>
__device__ __forceinline__ void static_for(F&& f) {
  if constexpr (I < N) {
    f(std::integral_constant<int, I>{});
    static_for<I + 1, N>(f);
  }
}

__device__ __forceinline__ cf cmk(float x, float y) { cf r; r.x = x; r.y = y; return r; }
__device__ __forceinline__ cf cadd(cf a, cf b) { return a + b; }
__device__ __forceinline__ cf csub(cf a, cf b) { return a - b; }
__device__ __forceinline__ cf add_pi(cf a, cf b) {
  cf r;
  asm("v_pk_add_f32 %0, %1, %2 op_sel:[0,1] op_sel_hi:[1,0] neg_lo:[0,1]" : "=v"(r) : "v"(a), "v"(b));
  return r;
}
__device__ __forceinline__ cf add_mi(cf a, cf b) {
  cf r;
  asm("v_pk_add_f32 %0, %1, %2 op_sel:[0,1] op_sel_hi:[1,0] neg_hi:[0,1]" : "=v"(r) : "v"(a), "v"(b));
  return r;
}
__device__ __forceinline__ cf cmul(cf a, cf b) {
  cf t, r;
  asm("v_pk_mul_f32 %0, %1, %2 op_sel:[0,0] op_sel_hi:[0,1]" : "=v"(t) : "v"(a), "v"(b));
  asm("v_pk_fma_f32 %0, %1, %2, %3 op_sel:[1,1,0] op_sel_hi:[1,0,1] neg_lo:[0,1,0]" : "=v"(r) : "v"(a), "v"(b), "v"(t));
  return r;
}
__device__ __forceinline__ cf cmad(cf a, cf b, cf c) {
  cf t, r;
  asm("v_pk_fma_f32 %0, %1, %2, %3 op_sel:[0,0,0] op_sel_hi:[0,1,1]" : "=v"(t) : "v"(a), "v"(b), "v"(c));
  asm("v_pk_fma_f32 %0, %1, %2, %3 op_sel:[1,1,0] op_sel_hi:[1,0,1] neg_lo:[0,1,0]" : "=v"(r) : "v"(a), "v"(b), "v"(t));
  return r;
}
__device__ __forceinline__ cf cmulc(cf a, cf b) {
  cf t, r;
  asm("v_pk_mul_f32 %0, %1, %2 op_sel:[0,0] op_sel_hi:[1,0]" : "=v"(t) : "v"(a), "v"(b));
  asm("v_pk_fma_f32 %0, %1, %2, %3 op_sel:[1,1,0] op_sel_hi:[0,1,1] neg_hi:[1,0,0]" : "=v"(r) : "v"(a), "v"(b), "v"(t));
  return r;
}
__device__ __forceinline__ cf cmadc(cf a, cf b, cf c) {
  cf t, r;
  asm("v_pk_fma_f32 %0, %1, %2, %3 op_sel:[0,0,0] op_sel_hi:[1,0,1]" : "=v"(t) : "v"(a), "v"(b), "v"(c));
  asm("v_pk_fma_f32 %0, %1, %2, %3 op_sel:[1,1,0] op_sel_hi:[0,1,1] neg_hi:[1,0,0]" : "=v"(r) : "v"(a), "v"(b), "v"(t));
  return r;
}
__device__ __forceinline__ cf addc(cf a, cf b) {
  cf r;
  asm("v_pk_add_f32 %0, %1, %2 neg_hi:[0,1]" : "=v"(r) : "v"(a), "v"(b));
  return r;
}
__device__ __forceinline__ cf oddc(cf a, cf b) {
  cf r;
  asm("v_pk_add_f32 %0, %1, %2 op_sel:[1,1] op_sel_hi:[0,0] neg_hi:[1,0]" : "=v"(r) : "v"(a), "v"(b));
  return r;
}
__device__ __forceinline__ cf conj_add_pi(cf a, cf b) {
  cf r;
  asm("v_pk_add_f32 %0, %1, %2 op_sel:[0,1] op_sel_hi:[1,0] neg_hi:[1,0]" : "=v"(r) : "v"(a), "v"(b));
  return r;
}
__device__ __forceinline__ cf cmulk(cf a, cf cs) {
  cf t, r;
  asm("v_pk_mul_f32 %0, %1, %2 op_sel:[0,0] op_sel_hi:[1,0]" : "=v"(t) : "v"(a), "s"(cs));
  asm("v_pk_fma_f32 %0, %1, %2, %3 op_sel:[1,1,0] op_sel_hi:[0,1,1] neg_lo:[1,0,0]" : "=v"(r) : "v"(a), "s"(cs), "v"(t));
  return r;
}
__device__ __forceinline__ cf csqr(cf a) { return cmk(fmaf(a.x, a.x, -(a.y * a.y)), 2.0f * a.x * a.y); }

__device__ __forceinline__ float bf2f(u32 h) { return __uint_as_float(h << 16); }
__device__ __forceinline__ u32 f2bf(float f) {
  __hip_bfloat16 b = __float2bfloat16(f);
  return (u32)(*reinterpret_cast<u16*>(&b));
}
__device__ __forceinline__ float bfround(float f) { return bf2f(f2bf(f)); }

__device__ __forceinline__ void wave_sync() {
  __builtin_amdgcn_fence(__ATOMIC_SEQ_CST, "wavefront");
  __builtin_amdgcn_wave_barrier();
  __builtin_amdgcn_fence(__ATOMIC_SEQ_CST, "wavefront");
}

constexpr double COS64_TAB[17] = {
    1.0, 0.99518472667219688624, 0.98078528040323044913, 0.95694033573220886494,
    0.92387953251128675613, 0.88192126434835502971, 0.83146961230254523708,
    0.77301045336273696081, 0.70710678118654752440, 0.63439328416364549822,
    0.55557023301960222474, 0.47139673682599764856, 0.38268343236508977173,
    0.29028467725446236764, 0.19509032201612826785, 0.09801714032956060199, 0.0};
constexpr double cos64(int j) {
  j = ((j % 64) + 64) % 64;
  if (j > 32) j = 64 - j;
  return (j <= 16) ? COS64_TAB[j] : -COS64_TAB[32 - j];
}
constexpr double sin64(int j) { return cos64(j - 16); }

template <int J, int DIR>
__device__ __forceinline__ cf tw64(cf a) {
  constexpr int j = ((J % 64) + 64) % 64;
  if constexpr (j == 0) {
    return a;
  } else if constexpr (j == 32) {
    return -a;
  } else if constexpr (j == 16) {
    return (DIR > 0) ? cmk(-a.y, a.x) : cmk(a.y, -a.x);
  } else if constexpr (j == 48) {
    return (DIR > 0) ? cmk(a.y, -a.x) : cmk(-a.y, a.x);
  } else {
    constexpr float c = (float)cos64(j);
    constexpr float s = (float)(DIR * sin64(j));
    return cmulk(a, cmk(c, s));
  }
}

template <int N, int DIR, int S>
__device__ __forceinline__ void fft_rec(const cf* in, cf* out) {
  if constexpr (N == 1) {
    out[0] = in[0];
  } else if constexpr (N == 2) {
    cf a = in[0], b = in[S];
    out[0] = cadd(a, b);
    out[1] = csub(a, b);
  } else {
    constexpr int Q = N / 4;
    cf t0[Q], t1[Q], t2[Q], t3[Q];
    fft_rec<Q, DIR, 4 * S>(in, t0);
    fft_rec<Q, DIR, 4 * S>(in + S, t1);
    fft_rec<Q, DIR, 4 * S>(in + 2 * S, t2);
    fft_rec<Q, DIR, 4 * S>(in + 3 * S, t3);
    static_for<0, Q>([&](auto ic) {
      constexpr int k = decltype(ic)::value;
      constexpr int M = 64 / N;
      cf a = t0[k];
      cf b = tw64<k * M, DIR>(t1[k]);
      cf d = tw64<3 * k * M, DIR>(t3[k]);
      cf s0, s1;
      if constexpr (2 * k * M == 16) {
        s0 = (DIR > 0) ? add_pi(a, t2[k]) : add_mi(a, t2[k]);
        s1 = (DIR > 0) ? add_mi(a, t2[k]) : add_pi(a, t2[k]);
      } else {
        cf c = tw64<2 * k * M, DIR>(t2[k]);
        s0 = cadd(a, c);
        s1 = csub(a, c);
      }
      cf s2 = cadd(b, d), s3 = csub(b, d);
      out[k] = cadd(s0, s2);
      out[k + Q] = (DIR > 0) ? add_pi(s1, s3) : add_mi(s1, s3);
      out[k + 2 * Q] = csub(s0, s2);
      out[k + 3 * Q] = (DIR > 0) ? add_mi(s1, s3) : add_pi(s1, s3);
    });
  }
}

template <int N, int DIR>
__device__ __forceinline__ void fft_reg(cf* v) {
  cf o[N];
  fft_rec<N, DIR, 1>(v, o);
#pragma unroll
  for (int i = 0; i < N; ++i) v[i] = o[i];
}

template <int DIR>
__device__ __forceinline__ void fft16_halfzero(cf* v) {
  cf a[8], b[8], oa[8], ob[8];
  static_for<0, 8>([&](auto ic) {
    constexpr int n = decltype(ic)::value;
    a[n] = v[n];
    b[n] = tw64<4 * n, DIR>(v[n]);
  });
  fft_rec<8, DIR, 1>(a, oa);
  fft_rec<8, DIR, 1>(b, ob);
#pragma unroll
  for (int k = 0; k < 8; ++k) {
    v[2 * k] = oa[k];
    v[2 * k + 1] = ob[k];
  }
}

struct Tw16 { cf w[4]; };

__device__ __forceinline__ Tw16 make_tw16(int n, int logN) {
  Tw16 b;
#pragma unroll
  for (int t = 0; t < 4; ++t) {
    int e = (n << t) & ((1 << logN) - 1);
    float s, c;
    sincospif(-2.0f * (float)e / (float)(1 << logN), &s, &c);
    b.w[t] = cmk(c, s);
  }
  return b;
}

__device__ __forceinline__ Tw16 load_tw16(const cf* __restrict__ TW, int n) {
  Tw16 b;
#pragma unroll
  for (int t = 0; t < 4; ++t) b.w[t] = TW[(n << t) & 2047];
  return b;
}

template <bool CONJ>
__device__ __forceinline__ void apply_tw16(cf* v, const Tw16& b) {
  cf p[16];
  p[1] = b.w[0];
  p[2] = b.w[1];
  p[4] = b.w[2];
  p[8] = b.w[3];
  asm volatile("" : "+v"(p[1]), "+v"(p[2]), "+v"(p[4]), "+v"(p[8]));
  static_for<3, 16>([&](auto ic) {
    constexpr int k = decltype(ic)::value;
    constexpr int top = (k >= 8) ? 8 : (k >= 4) ? 4 : 2;
    if constexpr (k != top) p[k] = cmul(p[k - top], p[top]);
  });
#pragma unroll
  for (int k = 1; k < 16; ++k) v[k] = CONJ ? cmulc(v[k], p[k]) : cmul(v[k], p[k]);
}

__device__ __forceinline__ void swap16(cf& a, cf& b) {
  asm volatile("s_nop 1" : "+v"(a), "+v"(b));
  auto r0 = __builtin_amdgcn_permlane16_swap(__float_as_uint(a.x), __float_as_uint(b.x), false, false);
  auto r1 = __builtin_amdgcn_permlane16_swap(__float_as_uint(a.y), __float_as_uint(b.y), false, false);
  a.x = __uint_as_float(r0[0]);
  b.x = __uint_as_float(r0[1]);
  a.y = __uint_as_float(r1[0]);
  b.y = __uint_as_float(r1[1]);
}

#define XP 152
#define XBUF (16 * XP)
typedef volatile __attribute__((address_space(3))) cf* ldsp;
__device__ __forceinline__ cf lds_ld(const cf* p) { return *(ldsp)(p); }
__device__ __forceinline__ void lds_st(cf* p, cf a) { *(ldsp)(p) = a; }

struct FCtx {
  Tw16 ta, tb;
  int t, offA, offB, kA, kB;
};
__device__ __forceinline__ FCtx make_fctx(const cf* __restrict__ TW, int t) {
  FCtx f;
  int k0 = t >> 3, c = t & 7;
  f.t = t;
  f.ta = load_tw16(TW, t);
  f.tb = load_tw16(TW, 16 * c);
  int k0m = (16 - k0) & 15;
  int k1m = (k0 != 0) ? (15 - c) : ((c != 0) ? (16 - c) : 8);
  f.offA = k0 * XP + 9 * c;
  f.offB = k0m * XP + 9 * k1m;
  f.kA = k0 + 16 * c;
  f.kB = k0m + 16 * k1m;
  return f;
}

__device__ __forceinline__ void fft2048_fwd_rest(cf* v, cf* buf, const FCtx& f) {
  apply_tw16<false>(v, f.ta);
  __syncthreads();
#pragma unroll
  for (int k0 = 0; k0 < 16; ++k0) lds_st(buf + k0 * XP + f.t, v[k0]);
  __syncthreads();
  cf* rb = buf + (f.t >> 3) * XP + (f.t & 7);
#pragma unroll
  for (int b = 0; b < 16; ++b) v[b] = lds_ld(rb + 8 * b);
  fft_reg<16, -1>(v);
  apply_tw16<false>(v, f.tb);
#pragma unroll
  for (int k1 = 0; k1 < 16; ++k1) lds_st(rb + 9 * k1, v[k1]);
  __syncthreads();
#pragma unroll
  for (int j = 0; j < 8; ++j) v[j] = lds_ld(buf + f.offA + j);
#pragma unroll
  for (int j = 0; j < 8; ++j) v[8 + j] = lds_ld(buf + f.offB + j);
  fft_reg<8, -1>(v);
  fft_reg<8, -1>(v + 8);
}

__device__ __forceinline__ void fft2048_inv_first(cf* v, cf* buf, const FCtx& f) {
  fft_reg<8, 1>(v);
  fft_reg<8, 1>(v + 8);
  __syncthreads();
#pragma unroll
  for (int j = 0; j < 8; ++j) lds_st(buf + f.offA + j, v[j]);
#pragma unroll
  for (int j = 0; j < 8; ++j) lds_st(buf + f.offB + j, v[8 + j]);
  __syncthreads();
  cf* rb = buf + (f.t >> 3) * XP + (f.t & 7);
#pragma unroll
  for (int k1 = 0; k1 < 16; ++k1) v[k1] = lds_ld(rb + 9 * k1);
  apply_tw16<true>(v, f.tb);
  fft_reg<16, 1>(v);
#pragma unroll
  for (int b = 0; b < 16; ++b) lds_st(rb + 8 * b, v[b]);
  __syncthreads();
#pragma unroll
  for (int kk = 0; kk < 16; ++kk) v[kk] = lds_ld(buf + kk * XP + f.t);
  apply_tw16<true>(v, f.ta);
}

#define KC_PITCH 648
#define KREC 104
using u32x4 = __attribute__((ext_vector_type(4))) unsigned;
using u32x2 = __attribute__((ext_vector_type(2))) unsigned;
using bf16x8 = __attribute__((ext_vector_type(8))) __bf16;

__device__ __forceinline__ cf cexp_scaled(float ar, float ai, float sc) {
  float e = expf(ar * sc);
  float s, c;
  sincosf(ai * sc, &s, &c);
  return cmk(e * c, e * s);
}
__device__ __forceinline__ u32 cvtpk(float a, float b) {
  u32 r;
  asm("v_cvt_pk_bf16_f32 %0, %1, %2" : "=v"(r) : "v"(a), "v"(b));
  return r;
}
__device__ __forceinline__ u32x2 split2(cf g) {
  u32x2 o;
  o.x = cvtpk(g.x, g.y);
  cf h = cmk(__uint_as_float(o.x << 16), __uint_as_float(o.x & 0xffff0000u));
  cf r = g - h;
  o.y = cvtpk(r.x, r.y);
  return o;
}

struct KParams { const u16 *Are, *Aim, *Bre, *Bim, *Cre, *Cim, *logdt; };

__device__ __forceinline__ void kgen_block(const KParams& P, float* __restrict__ Kc, u32* lds_all, int bid, int tid) {
  int wave = tid >> 6, lane = tid & 63;
  int d = bid * 2 + (wave >> 1);
  u32* lds = lds_all + wave * 32 * KREC;
  float dt = bfround(expf(bf2f(P.logdt[d])));
  int idx = d * NST + (wave & 1) * 64 + lane;
  float ar = dt * bf2f(P.Are[idx]), ai = dt * bf2f(P.Aim[idx]);
  cf dB = cmk(dt * bf2f(P.Bre[idx]), dt * bf2f(P.Bim[idx]));
  cf C = cmk(bf2f(P.Cre[idx]), bf2f(P.Cim[idx]));
  cf c0 = cmul(C, dB);
  cf e[12];
  e[0] = cexp_scaled(ar, ai, 1.0f);
  e[4] = cexp_scaled(ar, ai, 16.0f);
  e[8] = cexp_scaled(ar, ai, 256.0f);
  e[1] = csqr(e[0]); e[2] = csqr(e[1]); e[3] = csqr(e[2]);
  e[5] = csqr(e[4]); e[6] = csqr(e[5]); e[7] = csqr(e[6]);
  e[9] = csqr(e[8]); e[10] = csqr(e[9]); e[11] = csqr(e[10]);
  typedef volatile __attribute__((address_space(3))) u32x2* lp2;
  float s2048 = fmaf(c0.x, e[11].x, -(c0.y * e[11].y));
  {
    auto dpp = [](float x, auto ctl, auto rm) {
      return __int_as_float(__builtin_amdgcn_update_dpp(0, __float_as_int(x), decltype(ctl)::value, decltype(rm)::value, 0xf, true));
    };
    using std::integral_constant;
    s2048 += dpp(s2048, integral_constant<int, 0x111>{}, integral_constant<int, 0xf>{});
    s2048 += dpp(s2048, integral_constant<int, 0x112>{}, integral_constant<int, 0xf>{});
    s2048 += dpp(s2048, integral_constant<int, 0x114>{}, integral_constant<int, 0xf>{});
    s2048 += dpp(s2048, integral_constant<int, 0x118>{}, integral_constant<int, 0xf>{});
    s2048 += dpp(s2048, integral_constant<int, 0x142>{}, integral_constant<int, 0xa>{});
    s2048 += dpp(s2048, integral_constant<int, 0x143>{}, integral_constant<int, 0xc>{});
  }
  u32x2 Gw[20], Ew[32];
  const int rot = 2 * ((lane >> 2) & 3);
  u32* rec0 = lds + (lane & 31) * KREC;
  u32* rec = rec0 + rot;
  {
    cf G[20];
    G[0] = cmk(c0.x, -c0.y);
    G[1] = cmulc(G[0], e[0]);
    G[2] = cmulc(G[0], e[1]);
    G[3] = cmulc(G[1], e[1]);
    G[4] = cmulc(G[0], e[2]);
    G[5] = cmulc(G[1], e[2]);
    G[6] = cmulc(G[2], e[2]);
    G[7] = cmulc(G[3], e[2]);
    G[8] = cmulc(G[0], e[8]);
    G[9] = cmulc(G[2], e[8]);
    G[10] = cmulc(G[4], e[8]);
    G[11] = cmulc(G[6], e[8]);
    G[12] = cmulc(G[0], e[9]);
    G[13] = cmulc(G[4], e[9]);
    G[14] = cmulc(G[8], e[9]);
    G[15] = cmulc(G[10], e[9]);
    G[16] = cmulc(G[0], e[10]);
    G[17] = cmulc(G[8], e[10]);
    G[18] = cmulc(G[12], e[10]);
    G[19] = cmulc(G[14], e[10]);
#pragma unroll
    for (int i = 0; i < 20; ++i) Gw[i] = split2(G[i]);
  }
  if (lane < 32) {
#pragma unroll
    for (int i = 0; i < 20; ++i) *(lp2)(rec + 2 * i) = Gw[i];
  }
  {
    cf E[32];
    E[0] = cmk(1.f, 0.f);
    E[1] = e[3]; E[2] = e[4]; E[4] = e[5]; E[8] = e[6]; E[16] = e[7];
    static_for<3, 32>([&](auto ic) {
      constexpr int k = decltype(ic)::value;
      constexpr int top = (k >= 16) ? 16 : (k >= 8) ? 8 : (k >= 4) ? 4 : 2;
      if constexpr (k != top) E[k] = cmul(E[k - top], E[top]);
    });
    Ew[0].x = 0x3f80u;
    Ew[0].y = 0u;
#pragma unroll
    for (int i = 1; i < 32; ++i) Ew[i] = split2(E[i]);
  }
  f32x16 acc;
#pragma unroll
  for (int i = 0; i < 16; ++i) acc[i] = 0.f;
  const u32* base = lds + (2 * (lane >> 5)) * KREC;
  int offB[4];
#pragma unroll
  for (int q = 0; q < 4; ++q) {
    int w = 40 + 2 * (lane & 31) + 2 * q;
    offB[q] = (w >= KREC) ? w - KREC : w;
  }
#pragma unroll
  for (int ch = 0; ch < 2; ++ch) {
    if ((lane >> 5) == ch) {
      if (ch) {
#pragma unroll
        for (int i = 0; i < 20; ++i) *(lp2)(rec + 2 * i) = Gw[i];
      }
#pragma unroll
      for (int i = 0; i < 29; ++i) *(lp2)(rec + 40 + 2 * i) = Ew[i];
#pragma unroll
      for (int i = 29; i < 32; ++i) {
        int w = 40 + 2 * i + rot;
        *(lp2)(rec0 + ((w >= KREC) ? w - KREC : w)) = Ew[i];
      }
    }
#pragma unroll
    for (int m = 0; m < 8; ++m) {
      const u32* r0 = base + 4 * m * KREC;
      const u32* ra = r0 + 2 * (lane & 31) + 2 * (m & 3);
      const u32* rb = r0 + offB[m & 3];
      u32x2 a0 = *(lp2)(ra), a1 = *(lp2)(ra + KREC), b0 = *(lp2)(rb), b1 = *(lp2)(rb + KREC);
      u32x4 A = {a0.x, a0.y, a1.x, a1.y}, A2 = {a0.y, a0.x, a1.y, a1.x}, B = {b0.x, b0.y, b1.x, b1.y};
      acc = __builtin_amdgcn_mfma_f32_32x32x16_bf16(__builtin_bit_cast(bf16x8, A), __builtin_bit_cast(bf16x8, B), acc, 0, 0, 0);
      acc = __builtin_amdgcn_mfma_f32_32x32x16_bf16(__builtin_bit_cast(bf16x8, A2), __builtin_bit_cast(bf16x8, B), acc, 0, 0, 0);
    }
  }
  float* red = reinterpret_cast<float*>(lds_all + (wave | 1) * 32 * KREC);
  wave_sync();
  if (wave & 1) {
#pragma unroll
    for (int r = 0; r < 16; ++r) red[r * 64 + lane] = acc[r];
    if (lane == 63) red[1024] = s2048;
  }
  __syncthreads();
  if (!(wave & 1)) {
    int col = lane & 31, hh = lane >> 5;
    float* dst = Kc + (size_t)d * KC_PITCH;
#pragma unroll
    for (int r = 0; r < 16; ++r) {
      int row = (r & 3) + 8 * (r >> 2) + 4 * hh;
      int p = -1;
      if (row < 8) p = row + 8 * col;
      else if (row < 12) p = 256 + (row - 8) + 4 * col;
      else if (row < 16) p = 384 + ((row - 12) & 1) + 64 * ((row - 12) >> 1) + 2 * col;
      else if (row < 20) p = 512 + 32 * (row - 16) + col;
      if (p >= 0) dst[p] = acc[r] + red[r * 64 + lane];
    }
    if (lane == 63) dst[640] = s2048 + red[1024];
  }
}

__device__ __forceinline__ void wt_block(const u16* __restrict__ wr, const u16* __restrict__ wi, u32* __restrict__ WT,
                                         u32* tile, int bid, int tid) {
  int s0 = bid * 16;
  const u32* pr = reinterpret_cast<const u32*>(wr + (size_t)s0 * DF);
  const u32* pi = reinterpret_cast<const u32*>(wi + (size_t)s0 * DF);
  u32 a[17], b[17];
#pragma unroll
  for (int i = 0; i < 17; ++i) {
    int w = min(tid + 256 * i, 8 * DF - 1);
    a[i] = __builtin_nontemporal_load(pr + w);
    b[i] = __builtin_nontemporal_load(pi + w);
  }
#pragma unroll
  for (int i = 0; i < 17; ++i) {
    int w = tid + 256 * i;
    if (w < 8 * DF) {
      int e0 = 2 * w;
      int r = e0 / DF, k = e0 - r * DF;
      tile[k * 17 + r] = (a[i] & 0xffffu) | (b[i] << 16);
      int k1 = k + 1, r1 = r;
      if (k1 == DF) { k1 = 0; r1 = r + 1; }
      tile[k1 * 17 + r1] = (a[i] >> 16) | (b[i] & 0xffff0000u);
    }
  }
  __syncthreads();
  int sx = tid & 15, ky = tid >> 4;
#pragma unroll
  for (int i = 0; i < 33; ++i) {
    int k = ky + 16 * i;
    if (k < DF) __builtin_nontemporal_store(tile[k * 17 + sx], WT + (size_t)k * SEQ + s0 + sx);
  }
}

#define RP 34
#define RRS (16 * RP)
#define ZP 517
#define TWOFF (16 * RRS)

__device__ __forceinline__ int pos2row(int p) { return 128 * (2 * (p >> 8) + (p & 1)) + ((p & 255) >> 1); }

typedef __amdgpu_buffer_rsrc_t rsrc_t;
__device__ __forceinline__ rsrc_t mkrsrc(const void* p) {
  return __builtin_amdgcn_make_buffer_rsrc(const_cast<void*>(p), 0, 512 * SEQ * 8, 0x00020000);
}
__device__ __forceinline__ void st_sc1(rsrc_t rs, int off, cf a, cf b) {
  u32x4 q = {__float_as_uint(a.x), __float_as_uint(a.y), __float_as_uint(b.x), __float_as_uint(b.y)};
  __builtin_amdgcn_raw_buffer_store_b128(q, rs, off, 0, 16);
}
__device__ __forceinline__ void ld_sc1(rsrc_t rs, int off, cf& a, cf& b) {
  u32x4 q = __builtin_amdgcn_raw_buffer_load_b128(rs, off, 0, 16);
  a = cmk(__uint_as_float(q.x), __uint_as_float(q.y));
  b = cmk(__uint_as_float(q.z), __uint_as_float(q.w));
}

typedef __attribute__((ext_vector_type(4))) float f32x4;
__device__ __forceinline__ void st_nt(cf* p, cf a, cf b) {
  f32x4 q = {a.x, a.y, b.x, b.y};
  __builtin_nontemporal_store(q, reinterpret_cast<f32x4*>(p));
}
__device__ __forceinline__ void brow_block(const u16* __restrict__ x, cf* __restrict__ X1T, cf* sm, int bid, int tid) {
  cf* tw = sm + 8 * RRS;
  int s0 = bid * 8;
  {
    float s, c;
    sincospif(-(float)tid / 512.0f, &s, &c);
    tw[tid] = cmk(c, s);
    if (tid == 0) tw[256] = cmk(0.f, -1.f);
  }
  int row = tid >> 5, t = tid & 31;
  cf v[16];
  const u32* xr = reinterpret_cast<const u32*>(x + (size_t)pos2row(s0 + row) * DCH);
#pragma unroll
  for (int a = 0; a < 16; ++a) {
    u32 w = __builtin_nontemporal_load(xr + 32 * a + t);
    v[a] = cmk(bf2f(w & 0xffffu), bf2f(w >> 16));
  }
  Tw16 tb = make_tw16(t, 9);
  fft_reg<16, -1>(v);
  apply_tw16<false>(v, tb);
  cf* rb = sm + row * RRS;
  {
    int pos = (t & 1) * 16 + (t >> 1);
#pragma unroll
    for (int k0 = 0; k0 < 16; ++k0) rb[k0 * RP + pos] = v[k0];
  }
  wave_sync();
  int k0 = t & 15, h = t >> 4;
  {
    const float4* p = reinterpret_cast<const float4*>(rb + k0 * RP + h * 16);
#pragma unroll
    for (int m = 0; m < 8; ++m) {
      float4 q = p[m];
      v[2 * m] = cmk(q.x, q.y);
      v[2 * m + 1] = cmk(q.z, q.w);
    }
  }
  fft_reg<16, -1>(v);
  bool hi = h != 0;
  static_for<0, 8>([&](auto ic) {
    constexpr int j = decltype(ic)::value;
    cf a = v[j], b = v[j + 8];
    swap16(a, b);
    cf br = cmk(hi ? b.y : b.x, hi ? -b.x : b.y);
    br = tw64<2 * j, -1>(br);
    v[j] = cadd(a, br);
    v[j + 8] = csub(a, br);
  });
  __syncthreads();
  cf* zs = sm + row * ZP;
#pragma unroll
  for (int r = 0; r < 16; ++r) zs[k0 + 16 * ((r & 7) + 8 * h + 16 * (r >> 3))] = v[r];
  __syncthreads();
  int rp = tid & 3, kq = tid >> 2;
#pragma unroll
  for (int i = 0; i < 5; ++i) {
    int k = kq + 64 * i;
    if (k <= 256) {
      cf lo[2], up[2];
#pragma unroll
      for (int hh = 0; hh < 2; ++hh) {
        const cf* zr = sm + (2 * rp + hh) * ZP;
        cf a = zr[k];
        cf b = zr[(512 - k) & 511];
        cf ev = cmk(0.5f * (a.x + b.x), 0.5f * (a.y - b.y));
        cf od = cmk(0.5f * (a.y + b.y), -0.5f * (a.x - b.x));
        cf wo = cmul(od, tw[k]);
        lo[hh] = (k == 0) ? cmk(a.x + a.y, a.x - a.y) : cadd(ev, wo);
        up[hh] = cmk(ev.x - wo.x, -(ev.y - wo.y));
      }
      st_nt(X1T + (size_t)k * SEQ + s0 + 2 * rp, lo[0], lo[1]);
      if (k != 0 && k != 256) st_nt(X1T + (size_t)(512 - k) * SEQ + s0 + 2 * rp, up[0], up[1]);
    }
  }
}

__device__ __forceinline__ void browinv_block(rsrc_t X2T, const Tw16& tb, const cf* wk, u16* __restrict__ out, cf* sm,
                                              int bid, int tid) {
  int s0 = bid * 8;
  {
    int rp = tid & 3, kq = tid >> 2;
    cf a[5][2], b[5][2];
#pragma unroll
    for (int i = 0; i < 5; ++i) {
      int k = kq + 64 * i;
      if (k <= 256) {
        ld_sc1(X2T, (k * SEQ + s0 + 2 * rp) * 8, a[i][0], a[i][1]);
        ld_sc1(X2T, (((512 - k) & 511) * SEQ + s0 + 2 * rp) * 8, b[i][0], b[i][1]);
      }
    }
#pragma unroll
    for (int i = 0; i < 5; ++i) {
      int k = kq + 64 * i;
      if (k <= 256) {
#pragma unroll
        for (int h = 0; h < 2; ++h) {
          cf* zr = sm + (2 * rp + h) * ZP;
          cf aa = a[i][h], bb = b[i][h];
          if (k == 0) {
            zr[0] = cmk(aa.x + aa.y, aa.x - aa.y);
          } else {
            cf w = wk[i];
            cf ev = cmk(aa.x + bb.x, aa.y - bb.y);
            cf df = cmk(aa.x - bb.x, aa.y + bb.y);
            cf od = cmulc(df, w);
            zr[k] = cmk(ev.x - od.y, ev.y + od.x);
            if (k != 256) zr[512 - k] = cmk(ev.x + od.y, -ev.y + od.x);
          }
        }
      }
    }
  }
  __syncthreads();
  int row = tid >> 5, t = tid & 31;
  int k0 = t & 15, h = t >> 4;
  cf v[16];
  {
    const cf* zs = sm + row * ZP;
#pragma unroll
    for (int r = 0; r < 16; ++r) v[r] = zs[k0 + 16 * ((r & 7) + 8 * h + 16 * (r >> 3))];
  }
  __syncthreads();
  bool hi = h != 0;
  static_for<0, 8>([&](auto ic) {
    constexpr int j = decltype(ic)::value;
    cf a = cadd(v[j], v[j + 8]);
    cf d = csub(v[j], v[j + 8]);
    cf dr = cmk(hi ? -d.y : d.x, hi ? d.x : d.y);
    dr = tw64<2 * j, 1>(dr);
    swap16(a, dr);
    v[j] = a;
    v[j + 8] = dr;
  });
  fft_reg<16, 1>(v);
  cf* rb = sm + row * RRS;
  {
    float4* p = reinterpret_cast<float4*>(rb + k0 * RP + h * 16);
#pragma unroll
    for (int m = 0; m < 8; ++m) p[m] = make_float4(v[2 * m].x, v[2 * m].y, v[2 * m + 1].x, v[2 * m + 1].y);
  }
  wave_sync();
  {
    int pos = (t & 1) * 16 + (t >> 1);
#pragma unroll
    for (int kk = 0; kk < 16; ++kk) v[kk] = rb[kk * RP + pos];
  }
  apply_tw16<true>(v, tb);
  fft_reg<16, 1>(v);
  u32* orow = reinterpret_cast<u32*>(out + (size_t)pos2row(s0 + row) * (2 * DCH) + DCH);
#pragma unroll
  for (int a = 0; a < 16; ++a) orow[32 * a + t] = cvtpk(v[a].x, v[a].y);
}

__device__ __forceinline__ cf csel(bool c, cf a, cf b) { return cmk(c ? a.x : b.x, c ? a.y : b.y); }
__device__ __forceinline__ cf unpk(u32 w, float sc) { return cmk(bf2f(w & 0xffffu) * sc, bf2f(w >> 16) * sc); }

template <bool SPECIAL>
__device__ __forceinline__ void bcol_block(const cf* __restrict__ X1T, rsrc_t X2T, const u32* __restrict__ WT,
                                           const cf* __restrict__ TW, cf* buf, int col, int t) {
  cf v[16];
  const cf* xc = X1T + (size_t)col * SEQ;
#pragma unroll
  for (int a = 0; a < 8; ++a) {
    float4 q = *reinterpret_cast<const float4*>(xc + 256 * a + 2 * t);
    v[2 * a] = cmk(q.x, q.y);
    v[2 * a + 1] = cmk(q.z, q.w);
  }
  FCtx f = make_fctx(TW, t);
  const float sc = 1.0f / (2048.0f * 1024.0f);
  const u32* wc = WT + (size_t)col * SEQ;
  u32 wq[16], wn[16];
#pragma unroll
  for (int j = 0; j < 8; ++j) {
    wq[j] = wc[f.kA + 256 * j];
    wq[8 + j] = wc[f.kB + 256 * j];
  }
  if (SPECIAL) {
    const u32* w1 = WT + (size_t)512 * SEQ;
#pragma unroll
    for (int j = 0; j < 8; ++j) {
      wn[j] = w1[f.kA + 256 * j];
      wn[8 + j] = w1[f.kB + 256 * j];
    }
  }
  fft_reg<16, -1>(v);
  fft2048_fwd_rest(v, buf, f);
  if (!SPECIAL || col != 0) {
#pragma unroll
    for (int r = 0; r < 16; ++r) v[r] = cmul(v[r], unpk(wq[r], sc));
  } else {
    const bool sp = (t == 0);
    cf oP[8], oQ[8];
    auto pair = [&](cf a, cf b, u32 pa, u32 pb, u32 qa, u32 qb, cf& hp, cf& hq) {
      cf A = cmk(0.5f * (a.x + b.x), 0.5f * (a.y - b.y));
      cf B = cmk(0.5f * (a.y + b.y), -0.5f * (a.x - b.x));
      cf p = unpk(pa, 1.f), q = unpk(pb, 1.f);
      cf wh0 = cmk(0.5f * sc * (p.x + q.x), 0.5f * sc * (p.y - q.y));
      p = unpk(qa, 1.f); q = unpk(qb, 1.f);
      cf wh1 = cmk(0.5f * sc * (p.x + q.x), 0.5f * sc * (p.y - q.y));
      cf h0 = cmul(A, wh0), h1 = cmul(B, wh1);
      hp = cmk(h0.x - h1.y, h0.y + h1.x);
      hq = cmk(h0.x + h1.y, -h0.y + h1.x);
    };
    static_for<0, 8>([&](auto ic) {
      constexpr int j = decltype(ic)::value;
      constexpr int ia = j, ib = 15 - j;
      constexpr int sa = (j < 4) ? j : 8 + j;
      constexpr int sb = (j == 0) ? 0 : (j < 4) ? 8 - j : 15 - j;
      pair(csel(sp, v[sa], v[ia]), csel(sp, v[sb], v[ib]), sp ? wq[sa] : wq[ia], sp ? wq[sb] : wq[ib],
           sp ? wn[sa] : wn[ia], sp ? wn[sb] : wn[ib], oP[j], oQ[j]);
    });
    cf s4, s4b;
    pair(v[4], v[4], wq[4], wq[4], wn[4], wn[4], s4, s4b);
    v[0] = oP[0];
    v[1] = oP[1];
    v[2] = oP[2];
    v[3] = oP[3];
    v[4] = csel(sp, s4, oP[4]);
    v[5] = csel(sp, oQ[3], oP[5]);
    v[6] = csel(sp, oQ[2], oP[6]);
    v[7] = csel(sp, oQ[1], oP[7]);
    v[8] = oQ[7];
    v[9] = oQ[6];
    v[10] = oQ[5];
    v[11] = oQ[4];
    v[12] = csel(sp, oP[4], oQ[3]);
    v[13] = csel(sp, oP[5], oQ[2]);
    v[14] = csel(sp, oP[6], oQ[1]);
    v[15] = csel(sp, oP[7], oQ[0]);
  }
  fft2048_inv_first(v, buf, f);
  fft_reg<16, 1>(v);
  const int off = (col * SEQ + 2 * t) * 8;
#pragma unroll
  for (int a = 0; a < 8; ++a) st_sc1(X2T, off + 2048 * a, v[2 * a], v[2 * a + 1]);
}

__device__ __forceinline__ int kpos(int l, int s) {
  if (s == 0) return l;
  int half = 1 << (s - 1);
  int q = (l + half - 1 + ((l >> s) & 1)) >> s;
  return 128 * (s + 1) + q - 128;
}

__device__ __forceinline__ void aconv_block(const u32* __restrict__ uT, const float* __restrict__ kl, const cf* __restrict__ TW,
                                            const u16* __restrict__ Dskip, u32* ost, cf* buf, int d, int t) {
  cf v[16], kz[16];
  u32 uw[8];
  {
    static_for<0, 8>([&](auto ic) {
      constexpr int a = decltype(ic)::value;
      constexpr int s = (a < 1) ? 0 : (a < 2) ? 1 : (a < 4) ? 2 : 3;
      int l = 256 * a + 2 * t;
      kz[a] = cmk(kl[kpos(l, s)], kl[kpos(l + 1, s)]);
    });
    const u32* ur = uT + (size_t)d * (SEQ / 2);
#pragma unroll
    for (int a = 0; a < 8; ++a) uw[a] = ur[128 * a + t];
  }
  FCtx f = make_fctx(TW, t);
  cf L = TW[f.kA];
  cf L2 = TW[t == 0 ? 128 : f.kA];
  fft16_halfzero<-1>(kz);
  fft2048_fwd_rest(kz, buf, f);
#pragma unroll
  for (int a = 0; a < 8; ++a) v[a] = cmk(bf2f(uw[a] & 0xffffu), bf2f(uw[a] >> 16)) * (0.5f / 2048.0f);
  fft16_halfzero<-1>(v);
  fft2048_fwd_rest(v, buf, f);
  {
    const bool sp = (t == 0);
    cf oP[8], oQ[8];
    static_for<0, 8>([&](auto ic) {
      constexpr int j = decltype(ic)::value;
      cf a, b, ka, kb;
      if constexpr (j == 0) {
        a = v[0]; b = v[15]; ka = kz[0]; kb = kz[15];
      } else if constexpr (j < 4) {
        a = v[j]; ka = kz[j];
        b = csel(sp, v[8 - j], v[15 - j]);
        kb = csel(sp, kz[8 - j], kz[15 - j]);
      } else {
        a = csel(sp, v[8 + j], v[j]);
        ka = csel(sp, kz[8 + j], kz[j]);
        b = v[15 - j]; kb = kz[15 - j];
      }
      cf eu = addc(a, b);
      cf ou = oddc(a, b) * 0.5f;
      cf ek = addc(ka, kb);
      cf ok = oddc(ka, kb);
      cf w = tw64<8 * j, -1>((j < 4) ? L : L2);
      cf wo = cmul(ok, w);
      cf T = add_pi(wo, ek);
      cf T2 = conj_add_pi(wo, ek);
      oP[j] = cmad(ou, T, cmul(eu, ka));
      oQ[j] = cmadc(T2, ou, cmulc(kb, eu));
    });
    cf s0, s4;
    {
      float eu = 2.0f * v[0].x, ou = 2.0f * v[0].y;
      s0 = cmk(fmaf(eu, kz[0].x, ou * kz[0].y), fmaf(eu, kz[0].y, ou * kz[0].x));
      eu = 2.0f * v[4].x; ou = 2.0f * v[4].y;
      s4 = cmk(fmaf(eu, kz[4].x, -(ou * kz[4].y)), fmaf(eu, kz[4].y, ou * kz[4].x));
    }
    v[0] = csel(sp, s0, oP[0]);
    v[1] = oP[1];
    v[2] = oP[2];
    v[3] = oP[3];
    v[4] = csel(sp, s4, oP[4]);
    v[5] = csel(sp, oQ[3], oP[5]);
    v[6] = csel(sp, oQ[2], oP[6]);
    v[7] = csel(sp, oQ[1], oP[7]);
    v[8] = oQ[7];
    v[9] = oQ[6];
    v[10] = oQ[5];
    v[11] = oQ[4];
    v[12] = csel(sp, oP[4], oQ[3]);
    v[13] = csel(sp, oP[5], oQ[2]);
    v[14] = csel(sp, oP[6], oQ[1]);
    v[15] = csel(sp, oP[7], oQ[0]);
  }
  fft2048_inv_first(v, buf, f);
  fft_reg<16, 1>(v);
  float D = bf2f(Dskip[d]);
#pragma unroll
  for (int a = 0; a < 8; ++a) {
    u32 yb = cvtpk(v[a].x, v[a].y);
    u32 sb = cvtpk(bf2f(uw[a] & 0xffffu) * D, bf2f(uw[a] >> 16) * D);
    ost[128 * a + t] = cvtpk(bf2f(yb & 0xffffu) + bf2f(sb & 0xffffu), bf2f(yb >> 16) + bf2f(sb >> 16));
  }
}

__device__ __forceinline__ void utrans_block(const u16* __restrict__ u, u16* __restrict__ uT, u16* tile, int bid, int tid) {
  uint4 q[2][2];
  int r = tid >> 3, c = tid & 7;
#pragma unroll
  for (int it = 0; it < 2; ++it) {
    int tl = bid * 2 + it;
    int l0 = (tl >> 4) * 64, d0 = (tl & 15) * 64;
#pragma unroll
    for (int h = 0; h < 2; ++h)
    {
      u32x4 z = __builtin_nontemporal_load(reinterpret_cast<const u32x4*>(u + (size_t)(l0 + r + 32 * h) * DCH + d0 + 8 * c));
      q[it][h] = make_uint4(z.x, z.y, z.z, z.w);
    }
  }
#pragma unroll
  for (int it = 0; it < 2; ++it) {
    u16* tl_ = tile + it * 64 * 72;
#pragma unroll
    for (int h = 0; h < 2; ++h) {
      u32 w[4] = {q[it][h].x, q[it][h].y, q[it][h].z, q[it][h].w};
#pragma unroll
      for (int j = 0; j < 4; ++j) {
        tl_[(8 * c + 2 * j) * 72 + r + 32 * h] = (u16)(w[j] & 0xffffu);
        tl_[(8 * c + 2 * j + 1) * 72 + r + 32 * h] = (u16)(w[j] >> 16);
      }
    }
  }
  __syncthreads();
#pragma unroll
  for (int it = 0; it < 2; ++it) {
    int tl = bid * 2 + it;
    int l0 = (tl >> 4) * 64, d0 = (tl & 15) * 64;
    const u16* tl_ = tile + it * 64 * 72;
#pragma unroll
    for (int h = 0; h < 2; ++h) {
      uint4 o = *reinterpret_cast<const uint4*>(tl_ + (r + 32 * h) * 72 + 8 * c);
      { u32x4 oo = {o.x,o.y,o.z,o.w}; __builtin_nontemporal_store(oo, reinterpret_cast<u32x4*>(uT + (size_t)(d0 + r + 32 * h) * SEQ + l0 + 8 * c)); }
    }
  }
}

#define TS(k)
#define NSH 16
#define CSTR 64
typedef __attribute__((address_space(1))) unsigned gu32;

__global__ __launch_bounds__(256, 3) void k1(KParams P, float* Kc, const u16* x, cf* X1T, const u16* wr,
                                            const u16* wi, u32* WT, cf* TW, const u16* u, u16* uT, u32* cnt) {
  __shared__ __attribute__((aligned(16))) u32 smw[4 * 32 * KREC];
  cf* sm = reinterpret_cast<cf*>(smw);
  int b = blockIdx.x, tid = threadIdx.x;
  if (b < 768) {
    int q = b / 3, r = b % 3;
    if (r < 2) {
      kgen_block(P, Kc, smw, q * 2 + r, tid);
    } else {
      brow_block(x, X1T, sm, q, tid);
    }
  } else if (b < 768 + 128) {
    int i = b - 768;
    if (i < 8) {
      int j = i * 256 + tid;
      float s, c;
      sincospif(-(float)j / 1024.0f, &s, &c);
      TW[j] = cmk(c, s);
    }
    if (i == 8 && tid < NSH) cnt[tid * CSTR] = 0u;
    wt_block(wr, wi, WT, smw, i, tid);
  } else {
    utrans_block(u, uT, reinterpret_cast<u16*>(smw), b - 896, tid);
  }
}

__device__ __forceinline__ void handoff(gu32* gc, int b, int tid) {
  asm volatile("s_waitcnt vmcnt(0)" ::: "memory");
  __syncthreads();
  if (tid < 64) {
    if (tid == 0) __hip_atomic_fetch_add(gc + (b & (NSH - 1)) * CSTR, 1u, __ATOMIC_RELAXED, __HIP_MEMORY_SCOPE_AGENT);
    for (unsigned spins = 0; spins < (1u << 16); ++spins) {
      unsigned c = (tid < NSH) ? __hip_atomic_load(gc + tid * CSTR, __ATOMIC_RELAXED, __HIP_MEMORY_SCOPE_AGENT) : (256u / NSH);
      if (__all(c == 256u / NSH)) break;
      __builtin_amdgcn_s_sleep(1);
    }
    __builtin_amdgcn_fence(__ATOMIC_ACQUIRE, "wavefront");
  }
  __syncthreads();
}

__global__ __launch_bounds__(256, 3) void k2(const cf* X1T, cf* X2Tp, const u32* WT, const u32* uT, const float* Kc,
                                            const cf* TW, const u16* Dskip, u16* out, u32* cnt) {
  __shared__ __attribute__((aligned(16))) u32 smw[2 * 2 * XBUF + 2048];
  cf* sm = reinterpret_cast<cf*>(smw);
  int b = blockIdx.x, tid = threadIdx.x;
  int g = tid >> 7, t = tid & 127;
  TS(0);
  if (b < 256) {
    __builtin_amdgcn_s_setprio(3);
    rsrc_t X2T = mkrsrc(X2Tp);
    if (b == 0) bcol_block<true>(X1T, X2T, WT, TW, sm + g * XBUF, 2 * b + g, t);
    else bcol_block<false>(X1T, X2T, WT, TW, sm + g * XBUF, 2 * b + g, t);
    Tw16 tb = load_tw16(TW, 4 * (tid & 31));
    cf wk[5];
#pragma unroll
    for (int i = 0; i < 5; ++i) wk[i] = TW[(2 * ((tid >> 2) + 64 * i)) & 2047];
    TS(1);
    handoff((gu32*)cnt, b, tid);
    TS(2);
    browinv_block(X2T, tb, wk, out, sm, b, tid);
    TS(3);
    TS(4);
  } else {
    int i = b - 256;
    int p = (i & 7) * 64 + (i >> 3);
    u32* ost = smw + 4 * XBUF;
    aconv_block(uT, Kc + (size_t)(2 * p + g) * KC_PITCH, TW, Dskip, ost + g * 1024, sm + g * XBUF, 2 * p + g, t);
    __syncthreads();
    TS(2);
    u32* op = reinterpret_cast<u32*>(out + 2 * p);
#pragma unroll
    for (int j = 0; j < 4; ++j) {
      int q = tid + 256 * j;
      u32 w0 = ost[q], w1 = ost[1024 + q];
      op[(size_t)(2 * q) * DCH] = (w0 & 0xffffu) | (w1 << 16);
      op[(size_t)(2 * q + 1) * DCH] = (w0 >> 16) | (w1 & 0xffff0000u);
    }
    TS(3);
    TS(4);
  }
}

extern "C" void kernel_launch(void* const* d_in, const int* in_sizes, int n_in, void* d_out, int out_size,
                              void* d_ws, size_t ws_size, hipStream_t stream) {
  const u16* u = (const u16*)d_in[0];
  KParams P;
  P.Are = (const u16*)d_in[1];
  P.Aim = (const u16*)d_in[2];
  P.Bre = (const u16*)d_in[3];
  P.Bim = (const u16*)d_in[4];
  P.Cre = (const u16*)d_in[5];
  P.Cim = (const u16*)d_in[6];
  P.logdt = (const u16*)d_in[7];
  const u16* Dskip = (const u16*)d_in[8];
  const u16* x = (const u16*)d_in[9];
  const u16* wr = (const u16*)d_in[10];
  const u16* wi = (const u16*)d_in[11];
  u16* out = (u16*)d_out;
  char* ws = (char*)d_ws;
  cf* X1T = (cf*)ws;
  u32* WT = (u32*)(ws + (size_t)8 * 1024 * 1024);
  cf* X2T = (cf*)(ws + (size_t)16 * 1024 * 1024);
  u16* uT = (u16*)(ws + (size_t)24 * 1024 * 1024);
  cf* TW = (cf*)(ws + (size_t)28 * 1024 * 1024);
  u32* cnt = (u32*)(ws + (size_t)29 * 1024 * 1024);
  float* Kc = (float*)(ws + (size_t)30 * 1024 * 1024);

  k1<<<768 + 128 + 256, 256, 0, stream>>>(P, Kc, x, X1T, wr, wi, WT, TW, u, uT, cnt);
  k2<<<768, 256, 0, stream>>>(X1T, X2T, WT, (const u32*)uT, Kc, TW, Dskip, out, cnt);
}
```

```cpp
#include <hip/hip_runtime.h>
#include <hip/hip_bf16.h>
#include <type_traits>

#define SEQ 2048
#define DCH 1024
#define NST 128
#define DF 513

typedef __attribute__((ext_vector_type(2))) float cf;
typedef unsigned int u32;
typedef unsigned short u16;
using f32x16 = __attribute__((ext_vector_type(16))) float;

template <int I, int N, class F>
__device__ __forceinline__ void static_for(F&& f) {
  if constexpr (I < N) {
    f(std::integral_constant<int, I>{});
    static_for<I + 1, N>(f);
  }
}

__device__ __forceinline__ cf cmk(float x, float y) { cf r; r.x = x; r.y = y; return r; }
__device__ __forceinline__ cf cadd(cf a, cf b) { return a + b; }
__device__ __forceinline__ cf csub(cf a, cf b) { return a - b; }
__device__ __forceinline__ cf add_pi(cf a, cf b) {
  cf r;
  asm("v_pk_add_f32 %0, %1, %2 op_sel:[0,1] op_sel_hi:[1,0] neg_lo:[0,1]" : "=v"(r) : "v"(a), "v"(b));
  return r;
}
__device__ __forceinline__ cf add_mi(cf a, cf b) {
  cf r;
  asm("v_pk_add_f32 %0, %1, %2 op_sel:[0,1] op_sel_hi:[1,0] neg_hi:[0,1]" : "=v"(r) : "v"(a), "v"(b));
  return r;
}
__device__ __forceinline__ cf cmul(cf a, cf b) {
  cf t, r;
  asm("v_pk_mul_f32 %0, %1, %2 op_sel:[0,0] op_sel_hi:[0,1]" : "=v"(t) : "v"(a), "v"(b));
  asm("v_pk_fma_f32 %0, %1, %2, %3 op_sel:[1,1,0] op_sel_hi:[1,0,1] neg_lo:[0,1,0]" : "=v"(r) : "v"(a), "v"(b), "v"(t));
  return r;
}
__device__ __forceinline__ cf cmad(cf a, cf b, cf c) {
  cf t, r;
  asm("v_pk_fma_f32 %0, %1, %2, %3 op_sel:[0,0,0] op_sel_hi:[0,1,1]" : "=v"(t) : "v"(a), "v"(b), "v"(c));
  asm("v_pk_fma_f32 %0, %1, %2, %3 op_sel:[1,1,0] op_sel_hi:[1,0,1] neg_lo:[0,1,0]" : "=v"(r) : "v"(a), "v"(b), "v"(t));
  return r;
}
__device__ __forceinline__ cf cmulc(cf a, cf b) {
  cf t, r;
  asm("v_pk_mul_f32 %0, %1, %2 op_sel:[0,0] op_sel_hi:[1,0]" : "=v"(t) : "v"(a), "v"(b));
  asm("v_pk_fma_f32 %0, %1, %2, %3 op_sel:[1,1,0] op_sel_hi:[0,1,1] neg_hi:[1,0,0]" : "=v"(r) : "v"(a), "v"(b), "v"(t));
  return r;
}
__device__ __forceinline__ cf cmadc(cf a, cf b, cf c) {
  cf t, r;
  asm("v_pk_fma_f32 %0, %1, %2, %3 op_sel:[0,0,0] op_sel_hi:[1,0,1]" : "=v"(t) : "v"(a), "v"(b), "v"(c));
  asm("v_pk_fma_f32 %0, %1, %2, %3 op_sel:[1,1,0] op_sel_hi:[0,1,1] neg_hi:[1,0,0]" : "=v"(r) : "v"(a), "v"(b), "v"(t));
  return r;
}
__device__ __forceinline__ cf addc(cf a, cf b) {
  cf r;
  asm("v_pk_add_f32 %0, %1, %2 neg_hi:[0,1]" : "=v"(r) : "v"(a), "v"(b));
  return r;
}
__device__ __forceinline__ cf oddc(cf a, cf b) {
  cf r;
  asm("v_pk_add_f32 %0, %1, %2 op_sel:[1,1] op_sel_hi:[0,0] neg_hi:[1,0]" : "=v"(r) : "v"(a), "v"(b));
  return r;
}
__device__ __forceinline__ cf conj_add_pi(cf a, cf b) {
  cf r;
  asm("v_pk_add_f32 %0, %1, %2 op_sel:[0,1] op_sel_hi:[1,0] neg_hi:[1,0]" : "=v"(r) : "v"(a), "v"(b));
  return r;
}
__device__ __forceinline__ cf cmulk(cf a, cf cs) {
  cf t, r;
  asm("v_pk_mul_f32 %0, %1, %2 op_sel:[0,0] op_sel_hi:[1,0]" : "=v"(t) : "v"(a), "s"(cs));
  asm("v_pk_fma_f32 %0, %1, %2, %3 op_sel:[1,1,0] op_sel_hi:[0,1,1] neg_lo:[1,0,0]" : "=v"(r) : "v"(a), "s"(cs), "v"(t));
  return r;
}
__device__ __forceinline__ cf csqr(cf a) { return cmk(fmaf(a.x, a.x, -(a.y * a.y)), 2.0f * a.x * a.y); }

__device__ __forceinline__ float bf2f(u32 h) { return __uint_as_float(h << 16); }
__device__ __forceinline__ u32 f2bf(float f) {
  __hip_bfloat16 b = __float2bfloat16(f);
  return (u32)(*reinterpret_cast<u16*>(&b));
}
__device__ __forceinline__ float bfround(float f) { return bf2f(f2bf(f)); }

__device__ __forceinline__ void wave_sync() {
  __builtin_amdgcn_fence(__ATOMIC_SEQ_CST, "wavefront");
  __builtin_amdgcn_wave_barrier();
  __builtin_amdgcn_fence(__ATOMIC_SEQ_CST, "wavefront");
}

constexpr double COS64_TAB[17] = {
    1.0, 0.99518472667219688624, 0.98078528040323044913, 0.95694033573220886494,
    0.92387953251128675613, 0.88192126434835502971, 0.83146961230254523708,
    0.77301045336273696081, 0.70710678118654752440, 0.63439328416364549822,
    0.55557023301960222474, 0.47139673682599764856, 0.38268343236508977173,
    0.29028467725446236764, 0.19509032201612826785, 0.09801714032956060199, 0.0};
constexpr double cos64(int j) {
  j = ((j % 64) + 64) % 64;
  if (j > 32) j = 64 - j;
  return (j <= 16) ? COS64_TAB[j] : -COS64_TAB[32 - j];
}
constexpr double sin64(int j) { return cos64(j - 16); }

template <int J, int DIR>
__device__ __forceinline__ cf tw64(cf a) {
  constexpr int j = ((J % 64) + 64) % 64;
  if constexpr (j == 0) {
    return a;
  } else if constexpr (j == 32) {
    return -a;
  } else if constexpr (j == 16) {
    return (DIR > 0) ? cmk(-a.y, a.x) : cmk(a.y, -a.x);
  } else if constexpr (j == 48) {
    return (DIR > 0) ? cmk(a.y, -a.x) : cmk(-a.y, a.x);
  } else {
    constexpr float c = (float)cos64(j);
    constexpr float s = (float)(DIR * sin64(j));
    return cmulk(a, cmk(c, s));
  }
}

template <int N, int DIR, int S>
__device__ __forceinline__ void fft_rec(const cf* in, cf* out) {
  if constexpr (N == 1) {
    out[0] = in[0];
  } else if constexpr (N == 2) {
    cf a = in[0], b = in[S];
    out[0] = cadd(a, b);
    out[1] = csub(a, b);
  } else {
    constexpr int Q = N / 4;
    cf t0[Q], t1[Q], t2[Q], t3[Q];
    fft_rec<Q, DIR, 4 * S>(in, t0);
    fft_rec<Q, DIR, 4 * S>(in + S, t1);
    fft_rec<Q, DIR, 4 * S>(in + 2 * S, t2);
    fft_rec<Q, DIR, 4 * S>(in + 3 * S, t3);
    static_for<0, Q>([&](auto ic) {
      constexpr int k = decltype(ic)::value;
      constexpr int M = 64 / N;
      cf a = t0[k];
      cf b = tw64<k * M, DIR>(t1[k]);
      cf d = tw64<3 * k * M, DIR>(t3[k]);
      cf s0, s1;
      if constexpr (2 * k * M == 16) {
        s0 = (DIR > 0) ? add_pi(a, t2[k]) : add_mi(a, t2[k]);
        s1 = (DIR > 0) ? add_mi(a, t2[k]) : add_pi(a, t2[k]);
      } else {
        cf c = tw64<2 * k * M, DIR>(t2[k]);
        s0 = cadd(a, c);
        s1 = csub(a, c);
      }
      cf s2 = cadd(b, d), s3 = csub(b, d);
      out[k] = cadd(s0, s2);
      out[k + Q] = (DIR > 0) ? add_pi(s1, s3) : add_mi(s1, s3);
      out[k + 2 * Q] = csub(s0, s2);
      out[k + 3 * Q] = (DIR > 0) ? add_mi(s1, s3) : add_pi(s1, s3);
    });
  }
}

template <int N, int DIR>
__device__ __forceinline__ void fft_reg(cf* v) {
  cf o[N];
  fft_rec<N, DIR, 1>(v, o);
#pragma unroll
  for (int i = 0; i < N; ++i) v[i] = o[i];
}

template <int DIR>
__device__ __forceinline__ void fft16_halfzero(cf* v) {
  cf a[8], b[8], oa[8], ob[8];
  static_for<0, 8>([&](auto ic) {
    constexpr int n = decltype(ic)::value;
    a[n] = v[n];
    b[n] = tw64<4 * n, DIR>(v[n]);
  });
  fft_rec<8, DIR, 1>(a, oa);
  fft_rec<8, DIR, 1>(b, ob);
#pragma unroll
  for (int k = 0; k < 8; ++k) {
    v[2 * k] = oa[k];
    v[2 * k + 1] = ob[k];
  }
}

struct Tw16 { cf w[4]; };

__device__ __forceinline__ Tw16 make_tw16(int n, int logN) {
  Tw16 b;
#pragma unroll
  for (int t = 0; t < 4; ++t) {
    int e = (n << t) & ((1 << logN) - 1);
    float s, c;
    sincospif(-2.0f * (float)e / (float)(1 << logN), &s, &c);
    b.w[t] = cmk(c, s);
  }
  return b;
}

__device__ __forceinline__ Tw16 load_tw16(const cf* __restrict__ TW, int n) {
  Tw16 b;
#pragma unroll
  for (int t = 0; t < 4; ++t) b.w[t] = TW[(n << t) & 2047];
  return b;
}

template <bool CONJ>
__device__ __forceinline__ void apply_tw16(cf* v, const Tw16& b) {
  cf p[16];
  p[1] = b.w[0];
  p[2] = b.w[1];
  p[4] = b.w[2];
  p[8] = b.w[3];
  asm volatile("" : "+v"(p[1]), "+v"(p[2]), "+v"(p[4]), "+v"(p[8]));
  static_for<3, 16>([&](auto ic) {
    constexpr int k = decltype(ic)::value;
    constexpr int top = (k >= 8) ? 8 : (k >= 4) ? 4 : 2;
    if constexpr (k != top) p[k] = cmul(p[k - top], p[top]);
  });
#pragma unroll
  for (int k = 1; k < 16; ++k) v[k] = CONJ ? cmulc(v[k], p[k]) : cmul(v[k], p[k]);
}

__device__ __forceinline__ void swap16(cf& a, cf& b) {
  asm volatile("s_nop 1" : "+v"(a), "+v"(b));
  auto r0 = __builtin_amdgcn_permlane16_swap(__float_as_uint(a.x), __float_as_uint(b.x), false, false);
  auto r1 = __builtin_amdgcn_permlane16_swap(__float_as_uint(a.y), __float_as_uint(b.y), false, false);
  a.x = __uint_as_float(r0[0]);
  b.x = __uint_as_float(r0[1]);
  a.y = __uint_as_float(r1[0]);
  b.y = __uint_as_float(r1[1]);
}

#define XP 152
#define XBUF (16 * XP)
typedef volatile __attribute__((address_space(3))) cf* ldsp;
__device__ __forceinline__ cf lds_ld(const cf* p) { return *(ldsp)(p); }
__device__ __forceinline__ void lds_st(cf* p, cf a) { *(ldsp)(p) = a; }

struct FCtx {
  Tw16 ta, tb;
  int t, offA, offB, kA, kB;
};
__device__ __forceinline__ FCtx make_fctx(const cf* __restrict__ TW, int t) {
  FCtx f;
  int k0 = t >> 3, c = t & 7;
  f.t = t;
  f.ta = load_tw16(TW, t);
  f.tb = load_tw16(TW, 16 * c);
  int k0m = (16 - k0) & 15;
  int k1m = (k0 != 0) ? (15 - c) : ((c != 0) ? (16 - c) : 8);
  f.offA = k0 * XP + 9 * c;
  f.offB = k0m * XP + 9 * k1m;
  f.kA = k0 + 16 * c;
  f.kB = k0m + 16 * k1m;
  return f;
}

__device__ __forceinline__ void fft2048_fwd_rest(cf* v, cf* buf, const FCtx& f) {
  apply_tw16<false>(v, f.ta);
  __syncthreads();
#pragma unroll
  for (int k0 = 0; k0 < 16; ++k0) lds_st(buf + k0 * XP + f.t, v[k0]);
  __syncthreads();
  cf* rb = buf + (f.t >> 3) * XP + (f.t & 7);
#pragma unroll
  for (int b = 0; b < 16; ++b) v[b] = lds_ld(rb + 8 * b);
  fft_reg<16, -1>(v);
  apply_tw16<false>(v, f.tb);
#pragma unroll
  for (int k1 = 0; k1 < 16; ++k1) lds_st(rb + 9 * k1, v[k1]);
  __syncthreads();
#pragma unroll
  for (int j = 0; j < 8; ++j) v[j] = lds_ld(buf + f.offA + j);
#pragma unroll
  for (int j = 0; j < 8; ++j) v[8 + j] = lds_ld(buf + f.offB + j);
  fft_reg<8, -1>(v);
  fft_reg<8, -1>(v + 8);
}

__device__ __forceinline__ void fft2048_inv_first(cf* v, cf* buf, const FCtx& f) {
  fft_reg<8, 1>(v);
  fft_reg<8, 1>(v + 8);
  __syncthreads();
#pragma unroll
  for (int j = 0; j < 8; ++j) lds_st(buf + f.offA + j, v[j]);
#pragma unroll
  for (int j = 0; j < 8; ++j) lds_st(buf + f.offB + j, v[8 + j]);
  __syncthreads();
  cf* rb = buf + (f.t >> 3) * XP + (f.t & 7);
#pragma unroll
  for (int k1 = 0; k1 < 16; ++k1) v[k1] = lds_ld(rb + 9 * k1);
  apply_tw16<true>(v, f.tb);
  fft_reg<16, 1>(v);
#pragma unroll
  for (int b = 0; b < 16; ++b) lds_st(rb + 8 * b, v[b]);
  __syncthreads();
#pragma unroll
  for (int kk = 0; kk < 16; ++kk) v[kk] = lds_ld(buf + kk * XP + f.t);
  apply_tw16<true>(v, f.ta);
}

#define KC_PITCH 648
#define KREC 104
using u32x4 = __attribute__((ext_vector_type(4))) unsigned;
using u32x2 = __attribute__((ext_vector_type(2))) unsigned;
using bf16x8 = __attribute__((ext_vector_type(8))) __bf16;

__device__ __forceinline__ cf cis_rev(float rev) {
  float r = rev - __builtin_rintf(rev);
  return cmk(__builtin_amdgcn_cosf(r), __builtin_amdgcn_sinf(r));
}
__device__ __forceinline__ cf cexp_scaled(float ar2, float aih, float ail, float sc) {
  float e = __builtin_amdgcn_exp2f(ar2 * sc);
  float h = aih * sc;
  float r = (h - __builtin_rintf(h)) + ail * sc;
  cf w = cis_rev(r);
  return cmk(e * w.x, e * w.y);
}
__device__ __forceinline__ u32 cvtpk(float a, float b) {
  u32 r;
  asm("v_cvt_pk_bf16_f32 %0, %1, %2" : "=v"(r) : "v"(a), "v"(b));
  return r;
}
__device__ __forceinline__ u32x2 split2(cf g) {
  u32x2 o;
  o.x = cvtpk(g.x, g.y);
  cf h = cmk(__uint_as_float(o.x << 16), __uint_as_float(o.x & 0xffff0000u));
  cf r = g - h;
  o.y = cvtpk(r.x, r.y);
  return o;
}

struct KParams { const u16 *Are, *Aim, *Bre, *Bim, *Cre, *Cim, *logdt; };

__device__ __forceinline__ void kgen_block(const KParams& P, float* __restrict__ Kc, u32* lds_all, int bid, int tid) {
  int wave = tid >> 6, lane = tid & 63;
  int d = bid * 2 + (wave >> 1);
  u32* lds = lds_all + wave * 32 * KREC;
  float dt = bfround(expf(bf2f(P.logdt[d])));
  int idx = d * NST + (wave & 1) * 64 + lane;
  float ar = dt * bf2f(P.Are[idx]), ai = dt * bf2f(P.Aim[idx]);
  cf dB = cmk(dt * bf2f(P.Bre[idx]), dt * bf2f(P.Bim[idx]));
  cf C = cmk(bf2f(P.Cre[idx]), bf2f(P.Cim[idx]));
  cf c0 = cmul(C, dB);
  cf e[12];
  {
    const float L2E = 1.44269504088896341f, I2PH = 0.15915494309189535f, I2PL = 6.4206383e-09f;
    float ar2 = ar * L2E;
    float aih = ai * I2PH;
    float ail = fmaf(ai, I2PL, fmaf(ai, I2PH, -aih));
    e[0] = cexp_scaled(ar2, aih, ail, 1.0f);
    e[4] = cexp_scaled(ar2, aih, ail, 16.0f);
    e[8] = cexp_scaled(ar2, aih, ail, 256.0f);
  }
  e[1] = csqr(e[0]); e[2] = csqr(e[1]); e[3] = csqr(e[2]);
  e[5] = csqr(e[4]); e[6] = csqr(e[5]); e[7] = csqr(e[6]);
  e[9] = csqr(e[8]); e[10] = csqr(e[9]); e[11] = csqr(e[10]);
  typedef volatile __attribute__((address_space(3))) u32x2* lp2;
  float s2048 = fmaf(c0.x, e[11].x, -(c0.y * e[11].y));
  {
    auto dpp = [](float x, auto ctl, auto rm) {
      return __int_as_float(__builtin_amdgcn_update_dpp(0, __float_as_int(x), decltype(ctl)::value, decltype(rm)::value, 0xf, true));
    };
    using std::integral_constant;
    s2048 += dpp(s2048, integral_constant<int, 0x111>{}, integral_constant<int, 0xf>{});
    s2048 += dpp(s2048, integral_constant<int, 0x112>{}, integral_constant<int, 0xf>{});
    s2048 += dpp(s2048, integral_constant<int, 0x114>{}, integral_constant<int, 0xf>{});
    s2048 += dpp(s2048, integral_constant<int, 0x118>{}, integral_constant<int, 0xf>{});
    s2048 += dpp(s2048, integral_constant<int, 0x142>{}, integral_constant<int, 0xa>{});
    s2048 += dpp(s2048, integral_constant<int, 0x143>{}, integral_constant<int, 0xc>{});
  }
  u32x2 Gw[20], Ew[32];
  const int rot = 2 * ((lane >> 2) & 3);
  u32* rec0 = lds + (lane & 31) * KREC;
  u32* rec = rec0 + rot;
  {
    cf G[20];
    G[0] = cmk(c0.x, -c0.y);
    G[1] = cmulc(G[0], e[0]);
    G[2] = cmulc(G[0], e[1]);
    G[3] = cmulc(G[1], e[1]);
    G[4] = cmulc(G[0], e[2]);
    G[5] = cmulc(G[1], e[2]);
    G[6] = cmulc(G[2], e[2]);
    G[7] = cmulc(G[3], e[2]);
    G[8] = cmulc(G[0], e[8]);
    G[9] = cmulc(G[2], e[8]);
    G[10] = cmulc(G[4], e[8]);
    G[11] = cmulc(G[6], e[8]);
    G[12] = cmulc(G[0], e[9]);
    G[13] = cmulc(G[4], e[9]);
    G[14] = cmulc(G[8], e[9]);
    G[15] = cmulc(G[10], e[9]);
    G[16] = cmulc(G[0], e[10]);
    G[17] = cmulc(G[8], e[10]);
    G[18] = cmulc(G[12], e[10]);
    G[19] = cmulc(G[14], e[10]);
#pragma unroll
    for (int i = 0; i < 20; ++i) Gw[i] = split2(G[i]);
  }
  if (lane < 32) {
#pragma unroll
    for (int i = 0; i < 20; ++i) *(lp2)(rec + 2 * i) = Gw[i];
  }
  {
    cf E[32];
    E[0] = cmk(1.f, 0.f);
    E[1] = e[3]; E[2] = e[4]; E[4] = e[5]; E[8] = e[6]; E[16] = e[7];
    static_for<3, 32>([&](auto ic) {
      constexpr int k = decltype(ic)::value;
      constexpr int top = (k >= 16) ? 16 : (k >= 8) ? 8 : (k >= 4) ? 4 : 2;
      if constexpr (k != top) E[k] = cmul(E[k - top], E[top]);
    });
    Ew[0].x = 0x3f80u;
    Ew[0].y = 0u;
#pragma unroll
    for (int i = 1; i < 32; ++i) Ew[i] = split2(E[i]);
  }
  f32x16 acc;
#pragma unroll
  for (int i = 0; i < 16; ++i) acc[i] = 0.f;
  const u32* base = lds + (2 * (lane >> 5)) * KREC;
  int offB[4];
#pragma unroll
  for (int q = 0; q < 4; ++q) {
    int w = 40 + 2 * (lane & 31) + 2 * q;
    offB[q] = (w >= KREC) ? w - KREC : w;
  }
#pragma unroll
  for (int ch = 0; ch < 2; ++ch) {
    if ((lane >> 5) == ch) {
      if (ch) {
#pragma unroll
        for (int i = 0; i < 20; ++i) *(lp2)(rec + 2 * i) = Gw[i];
      }
#pragma unroll
      for (int i = 0; i < 29; ++i) *(lp2)(rec + 40 + 2 * i) = Ew[i];
#pragma unroll
      for (int i = 29; i < 32; ++i) {
        int w = 40 + 2 * i + rot;
        *(lp2)(rec0 + ((w >= KREC) ? w - KREC : w)) = Ew[i];
      }
    }
#pragma unroll
    for (int m = 0; m < 8; ++m) {
      const u32* r0 = base + 4 * m * KREC;
      const u32* ra = r0 + 2 * (lane & 31) + 2 * (m & 3);
      const u32* rb = r0 + offB[m & 3];
      u32x2 a0 = *(lp2)(ra), a1 = *(lp2)(ra + KREC), b0 = *(lp2)(rb), b1 = *(lp2)(rb + KREC);
      u32x4 A = {a0.x, a0.y, a1.x, a1.y}, A2 = {a0.y, a0.x, a1.y, a1.x}, B = {b0.x, b0.y, b1.x, b1.y};
      acc = __builtin_amdgcn_mfma_f32_32x32x16_bf16(__builtin_bit_cast(bf16x8, A), __builtin_bit_cast(bf16x8, B), acc, 0, 0, 0);
      acc = __builtin_amdgcn_mfma_f32_32x32x16_bf16(__builtin_bit_cast(bf16x8, A2), __builtin_bit_cast(bf16x8, B), acc, 0, 0, 0);
    }
  }
  float* red = reinterpret_cast<float*>(lds_all + (wave | 1) * 32 * KREC);
  wave_sync();
  if (wave & 1) {
#pragma unroll
    for (int r = 0; r < 16; ++r) red[r * 64 + lane] = acc[r];
    if (lane == 63) red[1024] = s2048;
  }
  __syncthreads();
  if (!(wave & 1)) {
    int col = lane & 31, hh = lane >> 5;
    float* dst = Kc + (size_t)d * KC_PITCH;
#pragma unroll
    for (int r = 0; r < 16; ++r) {
      int row = (r & 3) + 8 * (r >> 2) + 4 * hh;
      int p = -1;
      if (row < 8) p = row + 8 * col;
      else if (row < 12) p = 256 + (row - 8) + 4 * col;
      else if (row < 16) p = 384 + ((row - 12) & 1) + 64 * ((row - 12) >> 1) + 2 * col;
      else if (row < 20) p = 512 + 32 * (row - 16) + col;
      if (p >= 0) dst[p] = acc[r] + red[r * 64 + lane];
    }
    if (lane == 63) dst[640] = s2048 + red[1024];
  }
}

#define RP 34
#define RRS (16 * RP)
#define ZP 517
#define TWOFF (16 * RRS)

__device__ __forceinline__ int pos2row(int p) { return 128 * (2 * (p >> 8) + (p & 1)) + ((p & 255) >> 1); }

typedef __amdgpu_buffer_rsrc_t rsrc_t;
__device__ __forceinline__ rsrc_t mkrsrc(const void* p) {
  return __builtin_amdgcn_make_buffer_rsrc(const_cast<void*>(p), 0, 512 * SEQ * 8, 0x00020000);
}
__device__ __forceinline__ void st_sc1(rsrc_t rs, int off, cf a, cf b) {
  u32x4 q = {__float_as_uint(a.x), __float_as_uint(a.y), __float_as_uint(b.x), __float_as_uint(b.y)};
  __builtin_amdgcn_raw_buffer_store_b128(q, rs, off, 0, 16);
}
__device__ __forceinline__ void ld_sc1(rsrc_t rs, int off, cf& a, cf& b) {
  u32x4 q = __builtin_amdgcn_raw_buffer_load_b128(rs, off, 0, 16);
  a = cmk(__uint_as_float(q.x), __uint_as_float(q.y));
  b = cmk(__uint_as_float(q.z), __uint_as_float(q.w));
}

typedef __attribute__((ext_vector_type(4))) float f32x4;
__device__ __forceinline__ void st_nt(cf* p, cf a, cf b) {
  f32x4 q = {a.x, a.y, b.x, b.y};
  __builtin_nontemporal_store(q, reinterpret_cast<f32x4*>(p));
}
__device__ __forceinline__ void brow_block(const u32* xw, cf* __restrict__ X1T, cf* sm, int bid, int tid) {
  cf* tw = sm + 8 * RRS;
  int s0 = bid * 8;
  {
    float s, c;
    sincospif(-(float)tid / 512.0f, &s, &c);
    tw[tid] = cmk(c, s);
    if (tid == 0) tw[256] = cmk(0.f, -1.f);
  }
  int row = tid >> 5, t = tid & 31;
  cf v[16];
#pragma unroll
  for (int a = 0; a < 16; ++a) v[a] = cmk(bf2f(xw[a] & 0xffffu), bf2f(xw[a] >> 16));
  Tw16 tb = make_tw16(t, 9);
  fft_reg<16, -1>(v);
  apply_tw16<false>(v, tb);
  cf* rb = sm + row * RRS;
  {
    int pos = (t & 1) * 16 + (t >> 1);
#pragma unroll
    for (int k0 = 0; k0 < 16; ++k0) rb[k0 * RP + pos] = v[k0];
  }
  wave_sync();
  int k0 = t & 15, h = t >> 4;
  {
    const float4* p = reinterpret_cast<const float4*>(rb + k0 * RP + h * 16);
#pragma unroll
    for (int m = 0; m < 8; ++m) {
      float4 q = p[m];
      v[2 * m] = cmk(q.x, q.y);
      v[2 * m + 1] = cmk(q.z, q.w);
    }
  }
  fft_reg<16, -1>(v);
  bool hi = h != 0;
  static_for<0, 8>([&](auto ic) {
    constexpr int j = decltype(ic)::value;
    cf a = v[j], b = v[j + 8];
    swap16(a, b);
    cf br = cmk(hi ? b.y : b.x, hi ? -b.x : b.y);
    br = tw64<2 * j, -1>(br);
    v[j] = cadd(a, br);
    v[j + 8] = csub(a, br);
  });
  __syncthreads();
  cf* zs = sm + row * ZP;
#pragma unroll
  for (int r = 0; r < 16; ++r) zs[k0 + 16 * ((r & 7) + 8 * h + 16 * (r >> 3))] = v[r];
  __syncthreads();
  int rp = tid & 3, kq = tid >> 2;
#pragma unroll
  for (int i = 0; i < 5; ++i) {
    int k = kq + 64 * i;
    if (k <= 256) {
      cf lo[2], up[2];
#pragma unroll
      for (int hh = 0; hh < 2; ++hh) {
        const cf* zr = sm + (2 * rp + hh) * ZP;
        cf a = zr[k];
        cf b = zr[(512 - k) & 511];
        cf ev = cmk(0.5f * (a.x + b.x), 0.5f * (a.y - b.y));
        cf od = cmk(0.5f * (a.y + b.y), -0.5f * (a.x - b.x));
        cf wo = cmul(od, tw[k]);
        lo[hh] = (k == 0) ? cmk(a.x + a.y, a.x - a.y) : cadd(ev, wo);
        up[hh] = cmk(ev.x - wo.x, -(ev.y - wo.y));
      }
      st_nt(X1T + (size_t)k * SEQ + s0 + 2 * rp, lo[0], lo[1]);
      if (k != 0 && k != 256) st_nt(X1T + (size_t)(512 - k) * SEQ + s0 + 2 * rp, up[0], up[1]);
    }
  }
}

__device__ __forceinline__ void browinv_block(rsrc_t X2T, const Tw16& tb, const cf* wk, u16* __restrict__ out, cf* sm,
                                              int bid, int tid) {
  int s0 = bid * 8;
  {
    int rp = tid & 3, kq = tid >> 2;
    cf a[5][2], b[5][2];
#pragma unroll
    for (int i = 0; i < 5; ++i) {
      int k = kq + 64 * i;
      if (k <= 256) {
        ld_sc1(X2T, (k * SEQ + s0 + 2 * rp) * 8, a[i][0], a[i][1]);
        ld_sc1(X2T, (((512 - k) & 511) * SEQ + s0 + 2 * rp) * 8, b[i][0], b[i][1]);
      }
    }
#pragma unroll
    for (int i = 0; i < 5; ++i) {
      int k = kq + 64 * i;
      if (k <= 256) {
#pragma unroll
        for (int h = 0; h < 2; ++h) {
          cf* zr = sm + (2 * rp + h) * ZP;
          cf aa = a[i][h], bb = b[i][h];
          if (k == 0) {
            zr[0] = cmk(aa.x + aa.y, aa.x - aa.y);
          } else {
            cf w = wk[i];
            cf ev = cmk(aa.x + bb.x, aa.y - bb.y);
            cf df = cmk(aa.x - bb.x, aa.y + bb.y);
            cf od = cmulc(df, w);
            zr[k] = cmk(ev.x - od.y, ev.y + od.x);
            if (k != 256) zr[512 - k] = cmk(ev.x + od.y, -ev.y + od.x);
          }
        }
      }
    }
  }
  __syncthreads();
  int row = tid >> 5, t = tid & 31;
  int k0 = t & 15, h = t >> 4;
  cf v[16];
  {
    const cf* zs = sm + row * ZP;
#pragma unroll
    for (int r = 0; r < 16; ++r) v[r] = zs[k0 + 16 * ((r & 7) + 8 * h + 16 * (r >> 3))];
  }
  __syncthreads();
  bool hi = h != 0;
  static_for<0, 8>([&](auto ic) {
    constexpr int j = decltype(ic)::value;
    cf a = cadd(v[j], v[j + 8]);
    cf d = csub(v[j], v[j + 8]);
    cf dr = cmk(hi ? -d.y : d.x, hi ? d.x : d.y);
    dr = tw64<2 * j, 1>(dr);
    swap16(a, dr);
    v[j] = a;
    v[j + 8] = dr;
  });
  fft_reg<16, 1>(v);
  cf* rb = sm + row * RRS;
  {
    float4* p = reinterpret_cast<float4*>(rb + k0 * RP + h * 16);
#pragma unroll
    for (int m = 0; m < 8; ++m) p[m] = make_float4(v[2 * m].x, v[2 * m].y, v[2 * m + 1].x, v[2 * m + 1].y);
  }
  wave_sync();
  {
    int pos = (t & 1) * 16 + (t >> 1);
#pragma unroll
    for (int kk = 0; kk < 16; ++kk) v[kk] = rb[kk * RP + pos];
  }
  apply_tw16<true>(v, tb);
  fft_reg<16, 1>(v);
  u32* orow = reinterpret_cast<u32*>(out + (size_t)pos2row(s0 + row) * (2 * DCH) + DCH);
#pragma unroll
  for (int a = 0; a < 16; ++a) orow[32 * a + t] = cvtpk(v[a].x, v[a].y);
}

__device__ __forceinline__ cf csel(bool c, cf a, cf b) { return cmk(c ? a.x : b.x, c ? a.y : b.y); }
__device__ __forceinline__ cf unpk(u32 w, float sc) { return cmk(bf2f(w & 0xffffu) * sc, bf2f(w >> 16) * sc); }

template <bool SPECIAL>
__device__ __forceinline__ void bcol_block(const cf* __restrict__ X1T, rsrc_t X2T, const u32* __restrict__ WT,
                                           const cf* __restrict__ TW, cf* buf, int col, int t) {
  cf v[16];
  const cf* xc = X1T + (size_t)col * SEQ;
#pragma unroll
  for (int a = 0; a < 8; ++a) {
    float4 q = *reinterpret_cast<const float4*>(xc + 256 * a + 2 * t);
    v[2 * a] = cmk(q.x, q.y);
    v[2 * a + 1] = cmk(q.z, q.w);
  }
  FCtx f = make_fctx(TW, t);
  const float sc = 1.0f / (2048.0f * 1024.0f);
  const u32* wc = WT + (size_t)col * SEQ;
  u32 wq[16], wn[16];
#pragma unroll
  for (int j = 0; j < 8; ++j) {
    wq[j] = wc[f.kA + 256 * j];
    wq[8 + j] = wc[f.kB + 256 * j];
  }
  if (SPECIAL) {
    const u32* w1 = WT + (size_t)512 * SEQ;
#pragma unroll
    for (int j = 0; j < 8; ++j) {
      wn[j] = w1[f.kA + 256 * j];
      wn[8 + j] = w1[f.kB + 256 * j];
    }
  }
  fft_reg<16, -1>(v);
  fft2048_fwd_rest(v, buf, f);
  if (!SPECIAL || col != 0) {
#pragma unroll
    for (int r = 0; r < 16; ++r) v[r] = cmul(v[r], unpk(wq[r], sc));
  } else {
    const bool sp = (t == 0);
    cf oP[8], oQ[8];
    auto pair = [&](cf a, cf b, u32 pa, u32 pb, u32 qa, u32 qb, cf& hp, cf& hq) {
      cf A = cmk(0.5f * (a.x + b.x), 0.5f * (a.y - b.y));
      cf B = cmk(0.5f * (a.y + b.y), -0.5f * (a.x - b.x));
      cf p = unpk(pa, 1.f), q = unpk(pb, 1.f);
      cf wh0 = cmk(0.5f * sc * (p.x + q.x), 0.5f * sc * (p.y - q.y));
      p = unpk(qa, 1.f); q = unpk(qb, 1.f);
      cf wh1 = cmk(0.5f * sc * (p.x + q.x), 0.5f * sc * (p.y - q.y));
      cf h0 = cmul(A, wh0), h1 = cmul(B, wh1);
      hp = cmk(h0.x - h1.y, h0.y + h1.x);
      hq = cmk(h0.x + h1.y, -h0.y + h1.x);
    };
    static_for<0, 8>([&](auto ic) {
      constexpr int j = decltype(ic)::value;
      constexpr int ia = j, ib = 15 - j;
      constexpr int sa = (j < 4) ? j : 8 + j;
      constexpr int sb = (j == 0) ? 0 : (j < 4) ? 8 - j : 15 - j;
      pair(csel(sp, v[sa], v[ia]), csel(sp, v[sb], v[ib]), sp ? wq[sa] : wq[ia], sp ? wq[sb] : wq[ib],
           sp ? wn[sa] : wn[ia], sp ? wn[sb] : wn[ib], oP[j], oQ[j]);
    });
    cf s4, s4b;
    pair(v[4], v[4], wq[4], wq[4], wn[4], wn[4], s4, s4b);
    v[0] = oP[0];
    v[1] = oP[1];
    v[2] = oP[2];
    v[3] = oP[3];
    v[4] = csel(sp, s4, oP[4]);
    v[5] = csel(sp, oQ[3], oP[5]);
    v[6] = csel(sp, oQ[2], oP[6]);
    v[7] = csel(sp, oQ[1], oP[7]);
    v[8] = oQ[7];
    v[9] = oQ[6];
    v[10] = oQ[5];
    v[11] = oQ[4];
    v[12] = csel(sp, oP[4], oQ[3]);
    v[13] = csel(sp, oP[5], oQ[2]);
    v[14] = csel(sp, oP[6], oQ[1]);
    v[15] = csel(sp, oP[7], oQ[0]);
  }
  fft2048_inv_first(v, buf, f);
  fft_reg<16, 1>(v);
  const int off = (col * SEQ + 2 * t) * 8;
#pragma unroll
  for (int a = 0; a < 8; ++a) st_sc1(X2T, off + 2048 * a, v[2 * a], v[2 * a + 1]);
}

__device__ __forceinline__ int kpos(int l, int s) {
  if (s == 0) return l;
  int half = 1 << (s - 1);
  int q = (l + half - 1 + ((l >> s) & 1)) >> s;
  return 128 * (s + 1) + q - 128;
}

__device__ __forceinline__ void aconv_block(const u32* __restrict__ uT, const float* __restrict__ kl, const cf* __restrict__ TW,
                                            const u16* __restrict__ Dskip, u32* ost, cf* buf, int d, int t) {
  cf v[16], kz[16];
  u32 uw[8];
  {
    static_for<0, 8>([&](auto ic) {
      constexpr int a = decltype(ic)::value;
      constexpr int s = (a < 1) ? 0 : (a < 2) ? 1 : (a < 4) ? 2 : 3;
      int l = 256 * a + 2 * t;
      kz[a] = cmk(kl[kpos(l, s)], kl[kpos(l + 1, s)]);
    });
    const u32* ur = uT + (size_t)d * (SEQ / 2);
#pragma unroll
    for (int a = 0; a < 8; ++a) uw[a] = ur[128 * a + t];
  }
  FCtx f = make_fctx(TW, t);
  cf L = TW[f.kA];
  cf L2 = TW[t == 0 ? 128 : f.kA];
  fft16_halfzero<-1>(kz);
  fft2048_fwd_rest(kz, buf, f);
#pragma unroll
  for (int a = 0; a < 8; ++a) v[a] = cmk(bf2f(uw[a] & 0xffffu), bf2f(uw[a] >> 16)) * (0.5f / 2048.0f);
  fft16_halfzero<-1>(v);
  fft2048_fwd_rest(v, buf, f);
  {
    const bool sp = (t == 0);
    cf oP[8], oQ[8];
    static_for<0, 8>([&](auto ic) {
      constexpr int j = decltype(ic)::value;
      cf a, b, ka, kb;
      if constexpr (j == 0) {
        a = v[0]; b = v[15]; ka = kz[0]; kb = kz[15];
      } else if constexpr (j < 4) {
        a = v[j]; ka = kz[j];
        b = csel(sp, v[8 - j], v[15 - j]);
        kb = csel(sp, kz[8 - j], kz[15 - j]);
      } else {
        a = csel(sp, v[8 + j], v[j]);
        ka = csel(sp, kz[8 + j], kz[j]);
        b = v[15 - j]; kb = kz[15 - j];
      }
      cf eu = addc(a, b);
      cf ou = oddc(a, b) * 0.5f;
      cf ek = addc(ka, kb);
      cf ok = oddc(ka, kb);
      cf w = tw64<8 * j, -1>((j < 4) ? L : L2);
      cf wo = cmul(ok, w);
      cf T = add_pi(wo, ek);
      cf T2 = conj_add_pi(wo, ek);
      oP[j] = cmad(ou, T, cmul(eu, ka));
      oQ[j] = cmadc(T2, ou, cmulc(kb, eu));
    });
    cf s0, s4;
    {
      float eu = 2.0f * v[0].x, ou = 2.0f * v[0].y;
      s0 = cmk(fmaf(eu, kz[0].x, ou * kz[0].y), fmaf(eu, kz[0].y, ou * kz[0].x));
      eu = 2.0f * v[4].x; ou = 2.0f * v[4].y;
      s4 = cmk(fmaf(eu, kz[4].x, -(ou * kz[4].y)), fmaf(eu, kz[4].y, ou * kz[4].x));
    }
    v[0] = csel(sp, s0, oP[0]);
    v[1] = oP[1];
    v[2] = oP[2];
    v[3] = oP[3];
    v[4] = csel(sp, s4, oP[4]);
    v[5] = csel(sp, oQ[3], oP[5]);
    v[6] = csel(sp, oQ[2], oP[6]);
    v[7] = csel(sp, oQ[1], oP[7]);
    v[8] = oQ[7];
    v[9] = oQ[6];
    v[10] = oQ[5];
    v[11] = oQ[4];
    v[12] = csel(sp, oP[4], oQ[3]);
    v[13] = csel(sp, oP[5], oQ[2]);
    v[14] = csel(sp, oP[6], oQ[1]);
    v[15] = csel(sp, oP[7], oQ[0]);
  }
  fft2048_inv_first(v, buf, f);
  fft_reg<16, 1>(v);
  float D = bf2f(Dskip[d]);
#pragma unroll
  for (int a = 0; a < 8; ++a) {
    u32 yb = cvtpk(v[a].x, v[a].y);
    u32 sb = cvtpk(bf2f(uw[a] & 0xffffu) * D, bf2f(uw[a] >> 16) * D);
    ost[128 * a + t] = cvtpk(bf2f(yb & 0xffffu) + bf2f(sb & 0xffffu), bf2f(yb >> 16) + bf2f(sb >> 16));
  }
}

#define TS(k)
#define NSH 16
#define CSTR 64
typedef __attribute__((address_space(1))) unsigned gu32;

__device__ __forceinline__ u32 ld_u32_a2(const u16* p) {
  u32 w;
  __builtin_memcpy(&w, p, 4);
  return w;
}

__global__ __launch_bounds__(256, 3) void k1(KParams P, float* Kc, const u16* x, cf* X1T, const u16* wr,
                                            const u16* wi, u32* WT, cf* TW, const u16* u, u16* uT, u32* cnt) {
  __shared__ __attribute__((aligned(16))) u32 smw[4 * 32 * KREC];
  cf* sm = reinterpret_cast<cf*>(smw);
  int bb = blockIdx.x, tid = threadIdx.x;
  int b = bb / 3, r3 = bb % 3;
  if (r3 < 2) {
    kgen_block(P, Kc, smw, b * 2 + r3, tid);
  } else {
    u32 xw[16];
    {
      const u32* xr = reinterpret_cast<const u32*>(x + (size_t)pos2row(8 * b + (tid >> 5)) * DCH);
#pragma unroll
      for (int a = 0; a < 16; ++a) xw[a] = xr[32 * a + (tid & 31)];
    }
    uint4 uq[2][2];
    const int ur_ = tid >> 3, uc_ = tid & 7;
#pragma unroll
    for (int it = 0; it < 2; ++it) {
      int tl = b * 2 + it;
      int l0 = (tl >> 4) * 64, d0 = (tl & 15) * 64;
#pragma unroll
      for (int h = 0; h < 2; ++h)
        uq[it][h] = *reinterpret_cast<const uint4*>(u + (size_t)(l0 + ur_ + 32 * h) * DCH + d0 + 8 * uc_);
    }
    const int ws0 = (b >> 1) * 16, kb = (b & 1) * 257, nk = 257 - (b & 1);
    u32 wa[9], wb[9];
#pragma unroll
    for (int i = 0; i < 9; ++i) {
      int e = min(tid + 256 * i, 16 * 129 - 1);
      int r = e / 129, c = e - r * 129;
      size_t o = (size_t)(ws0 + r) * DF + kb + min(2 * c, nk - 2);
      wa[i] = ld_u32_a2(wr + o);
      wb[i] = ld_u32_a2(wi + o);
    }
    if (b < 8) {
      int j = b * 256 + tid;
      float s, c;
      sincospif(-(float)j / 1024.0f, &s, &c);
      TW[j] = cmk(c, s);
    }
    if (b == 8 && tid < NSH) cnt[tid * CSTR] = 0u;
    brow_block(xw, X1T, sm, b, tid);
    __syncthreads();
    u16* ut = reinterpret_cast<u16*>(smw);
    u32* wt = smw + 4608;
#pragma unroll
    for (int it = 0; it < 2; ++it) {
      u16* tl_ = ut + it * 64 * 72;
#pragma unroll
      for (int h = 0; h < 2; ++h) {
        u32 w[4] = {uq[it][h].x, uq[it][h].y, uq[it][h].z, uq[it][h].w};
#pragma unroll
        for (int j = 0; j < 4; ++j) {
          tl_[(8 * uc_ + 2 * j) * 72 + ur_ + 32 * h] = (u16)(w[j] & 0xffffu);
          tl_[(8 * uc_ + 2 * j + 1) * 72 + ur_ + 32 * h] = (u16)(w[j] >> 16);
        }
      }
    }
#pragma unroll
    for (int i = 0; i < 9; ++i) {
      int e = min(tid + 256 * i, 16 * 129 - 1);
      int r = e / 129, c = e - r * 129;
      int kk = min(2 * c, nk - 2);
      wt[kk * 17 + r] = (wa[i] & 0xffffu) | (wb[i] << 16);
      wt[(kk + 1) * 17 + r] = (wa[i] >> 16) | (wb[i] & 0xffff0000u);
    }
    __syncthreads();
#pragma unroll
    for (int it = 0; it < 2; ++it) {
      int tl = b * 2 + it;
      int l0 = (tl >> 4) * 64, d0 = (tl & 15) * 64;
      const u16* tl_ = ut + it * 64 * 72;
#pragma unroll
      for (int h = 0; h < 2; ++h) {
        uint4 o = *reinterpret_cast<const uint4*>(tl_ + (ur_ + 32 * h) * 72 + 8 * uc_);
        u32x4 oo = {o.x, o.y, o.z, o.w};
        __builtin_nontemporal_store(oo, reinterpret_cast<u32x4*>(uT + (size_t)(d0 + ur_ + 32 * h) * SEQ + l0 + 8 * uc_));
      }
    }
#pragma unroll
    for (int i = 0; i < 5; ++i) {
      int kk = (tid >> 2) + 64 * i, q = tid & 3;
      if (kk < nk) {
        const u32* p = wt + kk * 17 + 4 * q;
        u32x4 oo = {p[0], p[1], p[2], p[3]};
        __builtin_nontemporal_store(oo, reinterpret_cast<u32x4*>(WT + (size_t)(kb + kk) * SEQ + ws0 + 4 * q));
      }
    }
  }
}

__device__ __forceinline__ void handoff(gu32* gc, int b, int tid) {
  asm volatile("s_waitcnt vmcnt(0)" ::: "memory");
  __syncthreads();
  if (tid < 64) {
    if (tid == 0) __hip_atomic_fetch_add(gc + (b & (NSH - 1)) * CSTR, 1u, __ATOMIC_RELAXED, __HIP_MEMORY_SCOPE_AGENT);
    for (unsigned spins = 0; spins < (1u << 16); ++spins) {
      unsigned c = (tid < NSH) ? __hip_atomic_load(gc + tid * CSTR, __ATOMIC_RELAXED, __HIP_MEMORY_SCOPE_AGENT) : (256u / NSH);
      if (__all(c == 256u / NSH)) break;
      __builtin_amdgcn_s_sleep(1);
    }
    __builtin_amdgcn_fence(__ATOMIC_ACQUIRE, "wavefront");
  }
  __syncthreads();
}

__global__ __launch_bounds__(256, 3) void k2(const cf* X1T, cf* X2Tp, const u32* WT, const u32* uT, const float* Kc,
                                            const cf* TW, const u16* Dskip, u16* out, u32* cnt) {
  __shared__ __attribute__((aligned(16))) u32 smw[2 * 2 * XBUF + 2048];
  cf* sm = reinterpret_cast<cf*>(smw);
  int b = blockIdx.x, tid = threadIdx.x;
  int g = tid >> 7, t = tid & 127;
  TS(0);
  if (b < 256) {
    __builtin_amdgcn_s_setprio(3);
    rsrc_t X2T = mkrsrc(X2Tp);
    if (b == 0) bcol_block<true>(X1T, X2T, WT, TW, sm + g * XBUF, 2 * b + g, t);
    else bcol_block<false>(X1T, X2T, WT, TW, sm + g * XBUF, 2 * b + g, t);
    Tw16 tb = load_tw16(TW, 4 * (tid & 31));
    cf wk[5];
#pragma unroll
    for (int i = 0; i < 5; ++i) wk[i] = TW[(2 * ((tid >> 2) + 64 * i)) & 2047];
    TS(1);
    handoff((gu32*)cnt, b, tid);
    TS(2);
    browinv_block(X2T, tb, wk, out, sm, b, tid);
    TS(3);
    TS(4);
  } else {
    int i = b - 256;
    int p = (i & 7) * 64 + (i >> 3);
    u32* ost = smw + 4 * XBUF;
    aconv_block(uT, Kc + (size_t)(2 * p + g) * KC_PITCH, TW, Dskip, ost + g * 1024, sm + g * XBUF, 2 * p + g, t);
    __syncthreads();
    TS(2);
    u32* op = reinterpret_cast<u32*>(out + 2 * p);
#pragma unroll
    for (int j = 0; j < 4; ++j) {
      int q = tid + 256 * j;
      u32 w0 = ost[q], w1 = ost[1024 + q];
      op[(size_t)(2 * q) * DCH] = (w0 & 0xffffu) | (w1 << 16);
      op[(size_t)(2 * q + 1) * DCH] = (w0 >> 16) | (w1 & 0xffff0000u);
    }
    TS(3);
    TS(4);
  }
}

extern "C" void kernel_launch(void* const* d_in, const int* in_sizes, int n_in, void* d_out, int out_size,
                              void* d_ws, size_t ws_size, hipStream_t stream) {
  const u16* u = (const u16*)d_in[0];
  KParams P;
  P.Are = (const u16*)d_in[1];
  P.Aim = (const u16*)d_in[2];
  P.Bre = (const u16*)d_in[3];
  P.Bim = (const u16*)d_in[4];
  P.Cre = (const u16*)d_in[5];
  P.Cim = (const u16*)d_in[6];
  P.logdt = (const u16*)d_in[7];
  const u16* Dskip = (const u16*)d_in[8];
  const u16* x = (const u16*)d_in[9];
  const u16* wr = (const u16*)d_in[10];
  const u16* wi = (const u16*)d_in[11];
  u16* out = (u16*)d_out;
  char* ws = (char*)d_ws;
  cf* X1T = (cf*)ws;
  u32* WT = (u32*)(ws + (size_t)8 * 1024 * 1024);
  cf* X2T = (cf*)(ws + (size_t)16 * 1024 * 1024);
  u16* uT = (u16*)(ws + (size_t)24 * 1024 * 1024);
  cf* TW = (cf*)(ws + (size_t)28 * 1024 * 1024);
  u32* cnt = (u32*)(ws + (size_t)29 * 1024 * 1024);
  float* Kc = (float*)(ws + (size_t)30 * 1024 * 1024);

  k1<<<768, 256, 0, stream>>>(P, Kc, x, X1T, wr, wi, WT, TW, u, uT, cnt);
  k2<<<768, 256, 0, stream>>>(X1T, X2T, WT, (const u32*)uT, Kc, TW, Dskip, out, cnt);
}
```

```cpp
#include <hip/hip_runtime.h>
#include <hip/hip_bf16.h>
#include <type_traits>

#define SEQ 2048
#define DCH 1024
#define NST 128
#define DF 513

typedef __attribute__((ext_vector_type(2))) float cf;
typedef unsigned int u32;
typedef unsigned short u16;
using f32x16 = __attribute__((ext_vector_type(16))) float;

template <int I, int N, class F>
__device__ __forceinline__ void static_for(F&& f) {
  if constexpr (I < N) {
    f(std::integral_constant<int, I>{});
    static_for<I + 1, N>(f);
  }
}

__device__ __forceinline__ cf cmk(float x, float y) { cf r; r.x = x; r.y = y; return r; }
__device__ __forceinline__ cf cadd(cf a, cf b) { return a + b; }
__device__ __forceinline__ cf csub(cf a, cf b) { return a - b; }
__device__ __forceinline__ cf add_pi(cf a, cf b) {
  cf r;
  asm("v_pk_add_f32 %0, %1, %2 op_sel:[0,1] op_sel_hi:[1,0] neg_lo:[0,1]" : "=v"(r) : "v"(a), "v"(b));
  return r;
}
__device__ __forceinline__ cf add_mi(cf a, cf b) {
  cf r;
  asm("v_pk_add_f32 %0, %1, %2 op_sel:[0,1] op_sel_hi:[1,0] neg_hi:[0,1]" : "=v"(r) : "v"(a), "v"(b));
  return r;
}
__device__ __forceinline__ cf cmul(cf a, cf b) {
  cf t, r;
  asm("v_pk_mul_f32 %0, %1, %2 op_sel:[0,0] op_sel_hi:[0,1]" : "=v"(t) : "v"(a), "v"(b));
  asm("v_pk_fma_f32 %0, %1, %2, %3 op_sel:[1,1,0] op_sel_hi:[1,0,1] neg_lo:[0,1,0]" : "=v"(r) : "v"(a), "v"(b), "v"(t));
  return r;
}
__device__ __forceinline__ cf cmad(cf a, cf b, cf c) {
  cf t, r;
  asm("v_pk_fma_f32 %0, %1, %2, %3 op_sel:[0,0,0] op_sel_hi:[0,1,1]" : "=v"(t) : "v"(a), "v"(b), "v"(c));
  asm("v_pk_fma_f32 %0, %1, %2, %3 op_sel:[1,1,0] op_sel_hi:[1,0,1] neg_lo:[0,1,0]" : "=v"(r) : "v"(a), "v"(b), "v"(t));
  return r;
}
__device__ __forceinline__ cf cmulc(cf a, cf b) {
  cf t, r;
  asm("v_pk_mul_f32 %0, %1, %2 op_sel:[0,0] op_sel_hi:[1,0]" : "=v"(t) : "v"(a), "v"(b));
  asm("v_pk_fma_f32 %0, %1, %2, %3 op_sel:[1,1,0] op_sel_hi:[0,1,1] neg_hi:[1,0,0]" : "=v"(r) : "v"(a), "v"(b), "v"(t));
  return r;
}
__device__ __forceinline__ cf cmadc(cf a, cf b, cf c) {
  cf t, r;
  asm("v_pk_fma_f32 %0, %1, %2, %3 op_sel:[0,0,0] op_sel_hi:[1,0,1]" : "=v"(t) : "v"(a), "v"(b), "v"(c));
  asm("v_pk_fma_f32 %0, %1, %2, %3 op_sel:[1,1,0] op_sel_hi:[0,1,1] neg_hi:[1,0,0]" : "=v"(r) : "v"(a), "v"(b), "v"(t));
  return r;
}
__device__ __forceinline__ cf addc(cf a, cf b) {
  cf r;
  asm("v_pk_add_f32 %0, %1, %2 neg_hi:[0,1]" : "=v"(r) : "v"(a), "v"(b));
  return r;
}
__device__ __forceinline__ cf oddc(cf a, cf b) {
  cf r;
  asm("v_pk_add_f32 %0, %1, %2 op_sel:[1,1] op_sel_hi:[0,0] neg_hi:[1,0]" : "=v"(r) : "v"(a), "v"(b));
  return r;
}
__device__ __forceinline__ cf conj_add_pi(cf a, cf b) {
  cf r;
  asm("v_pk_add_f32 %0, %1, %2 op_sel:[0,1] op_sel_hi:[1,0] neg_hi:[1,0]" : "=v"(r) : "v"(a), "v"(b));
  return r;
}
__device__ __forceinline__ cf cmulk(cf a, cf cs) {
  cf t, r;
  asm("v_pk_mul_f32 %0, %1, %2 op_sel:[0,0] op_sel_hi:[1,0]" : "=v"(t) : "v"(a), "s"(cs));
  asm("v_pk_fma_f32 %0, %1, %2, %3 op_sel:[1,1,0] op_sel_hi:[0,1,1] neg_lo:[1,0,0]" : "=v"(r) : "v"(a), "s"(cs), "v"(t));
  return r;
}
__device__ __forceinline__ cf csqr(cf a) { return cmk(fmaf(a.x, a.x, -(a.y * a.y)), 2.0f * a.x * a.y); }

__device__ __forceinline__ float bf2f(u32 h) { return __uint_as_float(h << 16); }
__device__ __forceinline__ u32 f2bf(float f) {
  __hip_bfloat16 b = __float2bfloat16(f);
  return (u32)(*reinterpret_cast<u16*>(&b));
}
__device__ __forceinline__ float bfround(float f) { return bf2f(f2bf(f)); }

__device__ __forceinline__ void wave_sync() {
  __builtin_amdgcn_fence(__ATOMIC_SEQ_CST, "wavefront");
  __builtin_amdgcn_wave_barrier();
  __builtin_amdgcn_fence(__ATOMIC_SEQ_CST, "wavefront");
}

constexpr double COS64_TAB[17] = {
    1.0, 0.99518472667219688624, 0.98078528040323044913, 0.95694033573220886494,
    0.92387953251128675613, 0.88192126434835502971, 0.83146961230254523708,
    0.77301045336273696081, 0.70710678118654752440, 0.63439328416364549822,
    0.55557023301960222474, 0.47139673682599764856, 0.38268343236508977173,
    0.29028467725446236764, 0.19509032201612826785, 0.09801714032956060199, 0.0};
constexpr double cos64(int j) {
  j = ((j % 64) + 64) % 64;
  if (j > 32) j = 64 - j;
  return (j <= 16) ? COS64_TAB[j] : -COS64_TAB[32 - j];
}
constexpr double sin64(int j) { return cos64(j - 16); }

template <int J, int DIR>
__device__ __forceinline__ cf tw64(cf a) {
  constexpr int j = ((J % 64) + 64) % 64;
  if constexpr (j == 0) {
    return a;
  } else if constexpr (j == 32) {
    return -a;
  } else if constexpr (j == 16) {
    return (DIR > 0) ? cmk(-a.y, a.x) : cmk(a.y, -a.x);
  } else if constexpr (j == 48) {
    return (DIR > 0) ? cmk(a.y, -a.x) : cmk(-a.y, a.x);
  } else {
    constexpr float c = (float)cos64(j);
    constexpr float s = (float)(DIR * sin64(j));
    return cmulk(a, cmk(c, s));
  }
}

template <int N, int DIR, int S>
__device__ __forceinline__ void fft_rec(const cf* in, cf* out) {
  if constexpr (N == 1) {
    out[0] = in[0];
  } else if constexpr (N == 2) {
    cf a = in[0], b = in[S];
    out[0] = cadd(a, b);
    out[1] = csub(a, b);
  } else {
    constexpr int Q = N / 4;
    cf t0[Q], t1[Q], t2[Q], t3[Q];
    fft_rec<Q, DIR, 4 * S>(in, t0);
    fft_rec<Q, DIR, 4 * S>(in + S, t1);
    fft_rec<Q, DIR, 4 * S>(in + 2 * S, t2);
    fft_rec<Q, DIR, 4 * S>(in + 3 * S, t3);
    static_for<0, Q>([&](auto ic) {
      constexpr int k = decltype(ic)::value;
      constexpr int M = 64 / N;
      cf a = t0[k];
      cf b = tw64<k * M, DIR>(t1[k]);
      cf d = tw64<3 * k * M, DIR>(t3[k]);
      cf s0, s1;
      if constexpr (2 * k * M == 16) {
        s0 = (DIR > 0) ? add_pi(a, t2[k]) : add_mi(a, t2[k]);
        s1 = (DIR > 0) ? add_mi(a, t2[k]) : add_pi(a, t2[k]);
      } else {
        cf c = tw64<2 * k * M, DIR>(t2[k]);
        s0 = cadd(a, c);
        s1 = csub(a, c);
      }
      cf s2 = cadd(b, d), s3 = csub(b, d);
      out[k] = cadd(s0, s2);
      out[k + Q] = (DIR > 0) ? add_pi(s1, s3) : add_mi(s1, s3);
      out[k + 2 * Q] = csub(s0, s2);
      out[k + 3 * Q] = (DIR > 0) ? add_mi(s1, s3) : add_pi(s1, s3);
    });
  }
}

template <int N, int DIR>
__device__ __forceinline__ void fft_reg(cf* v) {
  cf o[N];
  fft_rec<N, DIR, 1>(v, o);
#pragma unroll
  for (int i = 0; i < N; ++i) v[i] = o[i];
}

template <int DIR>
__device__ __forceinline__ void fft16_halfzero(cf* v) {
  cf a[8], b[8], oa[8], ob[8];
  static_for<0, 8>([&](auto ic) {
    constexpr int n = decltype(ic)::value;
    a[n] = v[n];
    b[n] = tw64<4 * n, DIR>(v[n]);
  });
  fft_rec<8, DIR, 1>(a, oa);
  fft_rec<8, DIR, 1>(b, ob);
#pragma unroll
  for (int k = 0; k < 8; ++k) {
    v[2 * k] = oa[k];
    v[2 * k + 1] = ob[k];
  }
}

struct Tw16 { cf w[4]; };

__device__ __forceinline__ cf cis_rev(float rev) {
  float r = rev - __builtin_rintf(rev);
  return cmk(__builtin_amdgcn_cosf(r), __builtin_amdgcn_sinf(r));
}

__device__ __forceinline__ Tw16 make_tw16(int n, int logN) {
  Tw16 b;
#pragma unroll
  for (int t = 0; t < 4; ++t) {
    int e = (n << t) & ((1 << logN) - 1);
    b.w[t] = cis_rev(-(float)e / (float)(1 << logN));
  }
  return b;
}

__device__ __forceinline__ Tw16 load_tw16(const cf* __restrict__ TW, int n) {
  Tw16 b;
#pragma unroll
  for (int t = 0; t < 4; ++t) b.w[t] = TW[(n << t) & 2047];
  return b;
}

template <bool CONJ>
__device__ __forceinline__ void apply_tw16(cf* v, const Tw16& b) {
  cf p[16];
  p[1] = b.w[0];
  p[2] = b.w[1];
  p[4] = b.w[2];
  p[8] = b.w[3];
  asm volatile("" : "+v"(p[1]), "+v"(p[2]), "+v"(p[4]), "+v"(p[8]));
  static_for<3, 16>([&](auto ic) {
    constexpr int k = decltype(ic)::value;
    constexpr int top = (k >= 8) ? 8 : (k >= 4) ? 4 : 2;
    if constexpr (k != top) p[k] = cmul(p[k - top], p[top]);
  });
#pragma unroll
  for (int k = 1; k < 16; ++k) v[k] = CONJ ? cmulc(v[k], p[k]) : cmul(v[k], p[k]);
}

__device__ __forceinline__ void swap16(cf& a, cf& b) {
  asm volatile("s_nop 1" : "+v"(a), "+v"(b));
  auto r0 = __builtin_amdgcn_permlane16_swap(__float_as_uint(a.x), __float_as_uint(b.x), false, false);
  auto r1 = __builtin_amdgcn_permlane16_swap(__float_as_uint(a.y), __float_as_uint(b.y), false, false);
  a.x = __uint_as_float(r0[0]);
  b.x = __uint_as_float(r0[1]);
  a.y = __uint_as_float(r1[0]);
  b.y = __uint_as_float(r1[1]);
}

#define XP 152
#define XBUF (16 * XP)
typedef volatile __attribute__((address_space(3))) cf* ldsp;
__device__ __forceinline__ cf lds_ld(const cf* p) { return *(ldsp)(p); }
__device__ __forceinline__ void lds_st(cf* p, cf a) { *(ldsp)(p) = a; }

struct FCtx {
  Tw16 ta, tb;
  int t, offA, offB, kA, kB;
};
__device__ __forceinline__ FCtx make_fctx(const cf* __restrict__ TW, int t) {
  FCtx f;
  int k0 = t >> 3, c = t & 7;
  f.t = t;
  f.ta = load_tw16(TW, t);
  f.tb = load_tw16(TW, 16 * c);
  int k0m = (16 - k0) & 15;
  int k1m = (k0 != 0) ? (15 - c) : ((c != 0) ? (16 - c) : 8);
  f.offA = k0 * XP + 9 * c;
  f.offB = k0m * XP + 9 * k1m;
  f.kA = k0 + 16 * c;
  f.kB = k0m + 16 * k1m;
  return f;
}

__device__ __forceinline__ void fft2048_fwd_rest(cf* v, cf* buf, const FCtx& f) {
  apply_tw16<false>(v, f.ta);
  __syncthreads();
#pragma unroll
  for (int k0 = 0; k0 < 16; ++k0) lds_st(buf + k0 * XP + f.t, v[k0]);
  __syncthreads();
  cf* rb = buf + (f.t >> 3) * XP + (f.t & 7);
#pragma unroll
  for (int b = 0; b < 16; ++b) v[b] = lds_ld(rb + 8 * b);
  fft_reg<16, -1>(v);
  apply_tw16<false>(v, f.tb);
#pragma unroll
  for (int k1 = 0; k1 < 16; ++k1) lds_st(rb + 9 * k1, v[k1]);
  __syncthreads();
#pragma unroll
  for (int j = 0; j < 8; ++j) v[j] = lds_ld(buf + f.offA + j);
#pragma unroll
  for (int j = 0; j < 8; ++j) v[8 + j] = lds_ld(buf + f.offB + j);
  fft_reg<8, -1>(v);
  fft_reg<8, -1>(v + 8);
}

__device__ __forceinline__ void fft2048_inv_first(cf* v, cf* buf, const FCtx& f) {
  fft_reg<8, 1>(v);
  fft_reg<8, 1>(v + 8);
  __syncthreads();
#pragma unroll
  for (int j = 0; j < 8; ++j) lds_st(buf + f.offA + j, v[j]);
#pragma unroll
  for (int j = 0; j < 8; ++j) lds_st(buf + f.offB + j, v[8 + j]);
  __syncthreads();
  cf* rb = buf + (f.t >> 3) * XP + (f.t & 7);
#pragma unroll
  for (int k1 = 0; k1 < 16; ++k1) v[k1] = lds_ld(rb + 9 * k1);
  apply_tw16<true>(v, f.tb);
  fft_reg<16, 1>(v);
#pragma unroll
  for (int b = 0; b < 16; ++b) lds_st(rb + 8 * b, v[b]);
  __syncthreads();
#pragma unroll
  for (int kk = 0; kk < 16; ++kk) v[kk] = lds_ld(buf + kk * XP + f.t);
  apply_tw16<true>(v, f.ta);
}

#define KC_PITCH 648
#define KREC 104
using u32x4 = __attribute__((ext_vector_type(4))) unsigned;
using u32x2 = __attribute__((ext_vector_type(2))) unsigned;
using bf16x8 = __attribute__((ext_vector_type(8))) __bf16;

__device__ __forceinline__ cf cexp_scaled(float ar2, float aih, float ail, float sc) {
  float e = __builtin_amdgcn_exp2f(ar2 * sc);
  float h = aih * sc;
  float r = (h - __builtin_rintf(h)) + ail * sc;
  cf w = cis_rev(r);
  return cmk(e * w.x, e * w.y);
}
__device__ __forceinline__ u32 cvtpk(float a, float b) {
  u32 r;
  asm("v_cvt_pk_bf16_f32 %0, %1, %2" : "=v"(r) : "v"(a), "v"(b));
  return r;
}
__device__ __forceinline__ u32x2 split2(cf g) {
  u32x2 o;
  o.x = cvtpk(g.x, g.y);
  cf h = cmk(__uint_as_float(o.x << 16), __uint_as_float(o.x & 0xffff0000u));
  cf r = g - h;
  o.y = cvtpk(r.x, r.y);
  return o;
}

struct KParams { const u16 *Are, *Aim, *Bre, *Bim, *Cre, *Cim, *logdt; };

__device__ __forceinline__ void kgen_block(const KParams& P, float* __restrict__ Kc, u32* lds_all, int bid, int tid) {
  int wave = tid >> 6, lane = tid & 63;
  int d = bid * 2 + (wave >> 1);
  u32* lds = lds_all + wave * 32 * KREC;
  float dt = bfround(expf(bf2f(P.logdt[d])));
  int idx = d * NST + (wave & 1) * 64 + lane;
  float ar = dt * bf2f(P.Are[idx]), ai = dt * bf2f(P.Aim[idx]);
  cf dB = cmk(dt * bf2f(P.Bre[idx]), dt * bf2f(P.Bim[idx]));
  cf C = cmk(bf2f(P.Cre[idx]), bf2f(P.Cim[idx]));
  cf c0 = cmul(C, dB);
  cf e[12];
  {
    const float L2E = 1.44269504088896341f, I2PH = 0.15915494309189535f, I2PL = 6.4206383e-09f;
    float ar2 = ar * L2E;
    float aih = ai * I2PH;
    float ail = fmaf(ai, I2PL, fmaf(ai, I2PH, -aih));
    e[0] = cexp_scaled(ar2, aih, ail, 1.0f);
    e[4] = cexp_scaled(ar2, aih, ail, 16.0f);
    e[8] = cexp_scaled(ar2, aih, ail, 256.0f);
  }
  e[1] = csqr(e[0]); e[2] = csqr(e[1]); e[3] = csqr(e[2]);
  e[5] = csqr(e[4]); e[6] = csqr(e[5]); e[7] = csqr(e[6]);
  e[9] = csqr(e[8]); e[10] = csqr(e[9]); e[11] = csqr(e[10]);
  typedef volatile __attribute__((address_space(3))) u32x2* lp2;
  float s2048 = fmaf(c0.x, e[11].x, -(c0.y * e[11].y));
  {
    auto dpp = [](float x, auto ctl, auto rm) {
      return __int_as_float(__builtin_amdgcn_update_dpp(0, __float_as_int(x), decltype(ctl)::value, decltype(rm)::value, 0xf, true));
    };
    using std::integral_constant;
    s2048 += dpp(s2048, integral_constant<int, 0x111>{}, integral_constant<int, 0xf>{});
    s2048 += dpp(s2048, integral_constant<int, 0x112>{}, integral_constant<int, 0xf>{});
    s2048 += dpp(s2048, integral_constant<int, 0x114>{}, integral_constant<int, 0xf>{});
    s2048 += dpp(s2048, integral_constant<int, 0x118>{}, integral_constant<int, 0xf>{});
    s2048 += dpp(s2048, integral_constant<int, 0x142>{}, integral_constant<int, 0xa>{});
    s2048 += dpp(s2048, integral_constant<int, 0x143>{}, integral_constant<int, 0xc>{});
  }
  u32x2 Gw[20], Ew[32];
  const int rot = 2 * ((lane >> 2) & 3);
  u32* rec0 = lds + (lane & 31) * KREC;
  u32* rec = rec0 + rot;
  {
    cf G[20];
    G[0] = cmk(c0.x, -c0.y);
    G[1] = cmulc(G[0], e[0]);
    G[2] = cmulc(G[0], e[1]);
    G[3] = cmulc(G[1], e[1]);
    G[4] = cmulc(G[0], e[2]);
    G[5] = cmulc(G[1], e[2]);
    G[6] = cmulc(G[2], e[2]);
    G[7] = cmulc(G[3], e[2]);
    G[8] = cmulc(G[0], e[8]);
    G[9] = cmulc(G[2], e[8]);
    G[10] = cmulc(G[4], e[8]);
    G[11] = cmulc(G[6], e[8]);
    G[12] = cmulc(G[0], e[9]);
    G[13] = cmulc(G[4], e[9]);
    G[14] = cmulc(G[8], e[9]);
    G[15] = cmulc(G[10], e[9]);
    G[16] = cmulc(G[0], e[10]);
    G[17] = cmulc(G[8], e[10]);
    G[18] = cmulc(G[12], e[10]);
    G[19] = cmulc(G[14], e[10]);
#pragma unroll
    for (int i = 0; i < 20; ++i) Gw[i] = split2(G[i]);
  }
  if (lane < 32) {
#pragma unroll
    for (int i = 0; i < 20; ++i) *(lp2)(rec + 2 * i) = Gw[i];
  }
  {
    cf E[32];
    E[0] = cmk(1.f, 0.f);
    E[1] = e[3]; E[2] = e[4]; E[4] = e[5]; E[8] = e[6]; E[16] = e[7];
    static_for<3, 32>([&](auto ic) {
      constexpr int k = decltype(ic)::value;
      constexpr int top = (k >= 16) ? 16 : (k >= 8) ? 8 : (k >= 4) ? 4 : 2;
      if constexpr (k != top) E[k] = cmul(E[k - top], E[top]);
    });
    Ew[0].x = 0x3f80u;
    Ew[0].y = 0u;
#pragma unroll
    for (int i = 1; i < 32; ++i) Ew[i] = split2(E[i]);
  }
  f32x16 acc;
#pragma unroll
  for (int i = 0; i < 16; ++i) acc[i] = 0.f;
  const u32* base = lds + (2 * (lane >> 5)) * KREC;
  int offB[4];
#pragma unroll
  for (int q = 0; q < 4; ++q) {
    int w = 40 + 2 * (lane & 31) + 2 * q;
    offB[q] = (w >= KREC) ? w - KREC : w;
  }
#pragma unroll
  for (int ch = 0; ch < 2; ++ch) {
    if ((lane >> 5) == ch) {
      if (ch) {
#pragma unroll
        for (int i = 0; i < 20; ++i) *(lp2)(rec + 2 * i) = Gw[i];
      }
#pragma unroll
      for (int i = 0; i < 29; ++i) *(lp2)(rec + 40 + 2 * i) = Ew[i];
#pragma unroll
      for (int i = 29; i < 32; ++i) {
        int w = 40 + 2 * i + rot;
        *(lp2)(rec0 + ((w >= KREC) ? w - KREC : w)) = Ew[i];
      }
    }
#pragma unroll
    for (int m = 0; m < 8; ++m) {
      const u32* r0 = base + 4 * m * KREC;
      const u32* ra = r0 + 2 * (lane & 31) + 2 * (m & 3);
      const u32* rb = r0 + offB[m & 3];
      u32x2 a0 = *(lp2)(ra), a1 = *(lp2)(ra + KREC), b0 = *(lp2)(rb), b1 = *(lp2)(rb + KREC);
      u32x4 A = {a0.x, a0.y, a1.x, a1.y}, A2 = {a0.y, a0.x, a1.y, a1.x}, B = {b0.x, b0.y, b1.x, b1.y};
      acc = __builtin_amdgcn_mfma_f32_32x32x16_bf16(__builtin_bit_cast(bf16x8, A), __builtin_bit_cast(bf16x8, B), acc, 0, 0, 0);
      acc = __builtin_amdgcn_mfma_f32_32x32x16_bf16(__builtin_bit_cast(bf16x8, A2), __builtin_bit_cast(bf16x8, B), acc, 0, 0, 0);
    }
  }
  float* red = reinterpret_cast<float*>(lds_all + (wave | 1) * 32 * KREC);
  wave_sync();
  if (wave & 1) {
#pragma unroll
    for (int r = 0; r < 16; ++r) red[r * 64 + lane] = acc[r];
    if (lane == 63) red[1024] = s2048;
  }
  __syncthreads();
  if (!(wave & 1)) {
    int col = lane & 31, hh = lane >> 5;
    float* dst = Kc + (size_t)d * KC_PITCH;
#pragma unroll
    for (int r = 0; r < 16; ++r) {
      int row = (r & 3) + 8 * (r >> 2) + 4 * hh;
      int p = -1;
      if (row < 8) p = row + 8 * col;
      else if (row < 12) p = 256 + (row - 8) + 4 * col;
      else if (row < 16) p = 384 + ((row - 12) & 1) + 64 * ((row - 12) >> 1) + 2 * col;
      else if (row < 20) p = 512 + 32 * (row - 16) + col;
      if (p >= 0) dst[p] = acc[r] + red[r * 64 + lane];
    }
    if (lane == 63) dst[640] = s2048 + red[1024];
  }
}

#define RP 34
#define RRS (16 * RP)
#define ZP 517
#define TWOFF (16 * RRS)

__device__ __forceinline__ int pos2row(int p) { return 128 * (2 * (p >> 8) + (p & 1)) + ((p & 255) >> 1); }

typedef __amdgpu_buffer_rsrc_t rsrc_t;
__device__ __forceinline__ rsrc_t mkrsrc(const void* p) {
  return __builtin_amdgcn_make_buffer_rsrc(const_cast<void*>(p), 0, 512 * SEQ * 8, 0x00020000);
}
__device__ __forceinline__ void st_sc1(rsrc_t rs, int off, cf a, cf b) {
  u32x4 q = {__float_as_uint(a.x), __float_as_uint(a.y), __float_as_uint(b.x), __float_as_uint(b.y)};
  __builtin_amdgcn_raw_buffer_store_b128(q, rs, off, 0, 16);
}
__device__ __forceinline__ void ld_sc1(rsrc_t rs, int off, cf& a, cf& b) {
  u32x4 q = __builtin_amdgcn_raw_buffer_load_b128(rs, off, 0, 16);
  a = cmk(__uint_as_float(q.x), __uint_as_float(q.y));
  b = cmk(__uint_as_float(q.z), __uint_as_float(q.w));
}

typedef __attribute__((ext_vector_type(4))) float f32x4;
__device__ __forceinline__ void st_nt(cf* p, cf a, cf b) {
  f32x4 q = {a.x, a.y, b.x, b.y};
  __builtin_nontemporal_store(q, reinterpret_cast<f32x4*>(p));
}
__device__ __forceinline__ void brow_block(const u32* xw, cf* __restrict__ X1T, cf* sm, int bid, int tid) {
  cf* tw = sm + 8 * RRS;
  int s0 = bid * 8;
  {
    tw[tid] = cis_rev(-(float)tid / 1024.0f);
    if (tid == 0) tw[256] = cmk(0.f, -1.f);
  }
  int row = tid >> 5, t = tid & 31;
  cf v[16];
#pragma unroll
  for (int a = 0; a < 16; ++a) v[a] = cmk(bf2f(xw[a] & 0xffffu), bf2f(xw[a] >> 16));
  Tw16 tb = make_tw16(t, 9);
  fft_reg<16, -1>(v);
  apply_tw16<false>(v, tb);
  cf* rb = sm + row * RRS;
  {
    int pos = (t & 1) * 16 + (t >> 1);
#pragma unroll
    for (int k0 = 0; k0 < 16; ++k0) rb[k0 * RP + pos] = v[k0];
  }
  wave_sync();
  int k0 = t & 15, h = t >> 4;
  {
    const float4* p = reinterpret_cast<const float4*>(rb + k0 * RP + h * 16);
#pragma unroll
    for (int m = 0; m < 8; ++m) {
      float4 q = p[m];
      v[2 * m] = cmk(q.x, q.y);
      v[2 * m + 1] = cmk(q.z, q.w);
    }
  }
  fft_reg<16, -1>(v);
  bool hi = h != 0;
  static_for<0, 8>([&](auto ic) {
    constexpr int j = decltype(ic)::value;
    cf a = v[j], b = v[j + 8];
    swap16(a, b);
    cf br = cmk(hi ? b.y : b.x, hi ? -b.x : b.y);
    br = tw64<2 * j, -1>(br);
    v[j] = cadd(a, br);
    v[j + 8] = csub(a, br);
  });
  __syncthreads();
  cf* zs = sm + row * ZP;
#pragma unroll
  for (int r = 0; r < 16; ++r) zs[k0 + 16 * ((r & 7) + 8 * h + 16 * (r >> 3))] = v[r];
  __syncthreads();
  int rp = tid & 3, kq = tid >> 2;
#pragma unroll
  for (int i = 0; i < 5; ++i) {
    int k = kq + 64 * i;
    if (k <= 256) {
      cf lo[2], up[2];
#pragma unroll
      for (int hh = 0; hh < 2; ++hh) {
        const cf* zr = sm + (2 * rp + hh) * ZP;
        cf a = zr[k];
        cf b = zr[(512 - k) & 511];
        cf ev = cmk(0.5f * (a.x + b.x), 0.5f * (a.y - b.y));
        cf od = cmk(0.5f * (a.y + b.y), -0.5f * (a.x - b.x));
        cf wo = cmul(od, tw[k]);
        lo[hh] = (k == 0) ? cmk(a.x + a.y, a.x - a.y) : cadd(ev, wo);
        up[hh] = cmk(ev.x - wo.x, -(ev.y - wo.y));
      }
      st_nt(X1T + (size_t)k * SEQ + s0 + 2 * rp, lo[0], lo[1]);
      if (k != 0 && k != 256) st_nt(X1T + (size_t)(512 - k) * SEQ + s0 + 2 * rp, up[0], up[1]);
    }
  }
}

__device__ __forceinline__ void browinv_block(rsrc_t X2T, const Tw16& tb, const cf* wk, u16* __restrict__ out, cf* sm,
                                              int bid, int tid) {
  int s0 = bid * 8;
  {
    int rp = tid & 3, kq = tid >> 2;
    cf a[5][2], b[5][2];
#pragma unroll
    for (int i = 0; i < 5; ++i) {
      int k = kq + 64 * i;
      if (k <= 256) {
        ld_sc1(X2T, (k * SEQ + s0 + 2 * rp) * 8, a[i][0], a[i][1]);
        ld_sc1(X2T, (((512 - k) & 511) * SEQ + s0 + 2 * rp) * 8, b[i][0], b[i][1]);
      }
    }
#pragma unroll
    for (int i = 0; i < 5; ++i) {
      int k = kq + 64 * i;
      if (k <= 256) {
#pragma unroll
        for (int h = 0; h < 2; ++h) {
          cf* zr = sm + (2 * rp + h) * ZP;
          cf aa = a[i][h], bb = b[i][h];
          if (k == 0) {
            zr[0] = cmk(aa.x + aa.y, aa.x - aa.y);
          } else {
            cf w = wk[i];
            cf ev = cmk(aa.x + bb.x, aa.y - bb.y);
            cf df = cmk(aa.x - bb.x, aa.y + bb.y);
            cf od = cmulc(df, w);
            zr[k] = cmk(ev.x - od.y, ev.y + od.x);
            if (k != 256) zr[512 - k] = cmk(ev.x + od.y, -ev.y + od.x);
          }
        }
      }
    }
  }
  __syncthreads();
  int row = tid >> 5, t = tid & 31;
  int k0 = t & 15, h = t >> 4;
  cf v[16];
  {
    const cf* zs = sm + row * ZP;
#pragma unroll
    for (int r = 0; r < 16; ++r) v[r] = zs[k0 + 16 * ((r & 7) + 8 * h + 16 * (r >> 3))];
  }
  __syncthreads();
  bool hi = h != 0;
  static_for<0, 8>([&](auto ic) {
    constexpr int j = decltype(ic)::value;
    cf a = cadd(v[j], v[j + 8]);
    cf d = csub(v[j], v[j + 8]);
    cf dr = cmk(hi ? -d.y : d.x, hi ? d.x : d.y);
    dr = tw64<2 * j, 1>(dr);
    swap16(a, dr);
    v[j] = a;
    v[j + 8] = dr;
  });
  fft_reg<16, 1>(v);
  cf* rb = sm + row * RRS;
  {
    float4* p = reinterpret_cast<float4*>(rb + k0 * RP + h * 16);
#pragma unroll
    for (int m = 0; m < 8; ++m) p[m] = make_float4(v[2 * m].x, v[2 * m].y, v[2 * m + 1].x, v[2 * m + 1].y);
  }
  wave_sync();
  {
    int pos = (t & 1) * 16 + (t >> 1);
#pragma unroll
    for (int kk = 0; kk < 16; ++kk) v[kk] = rb[kk * RP + pos];
  }
  apply_tw16<true>(v, tb);
  fft_reg<16, 1>(v);
  u32* orow = reinterpret_cast<u32*>(out + (size_t)pos2row(s0 + row) * (2 * DCH) + DCH);
#pragma unroll
  for (int a = 0; a < 16; ++a) orow[32 * a + t] = cvtpk(v[a].x, v[a].y);
}

__device__ __forceinline__ cf csel(bool c, cf a, cf b) { return cmk(c ? a.x : b.x, c ? a.y : b.y); }
__device__ __forceinline__ cf unpk(u32 w, float sc) { return cmk(bf2f(w & 0xffffu) * sc, bf2f(w >> 16) * sc); }

template <bool SPECIAL>
__device__ __forceinline__ void bcol_block(const cf* __restrict__ X1T, rsrc_t X2T, const u32* __restrict__ WT,
                                           const cf* __restrict__ TW, cf* buf, int col, int t) {
  cf v[16];
  const cf* xc = X1T + (size_t)col * SEQ;
#pragma unroll
  for (int a = 0; a < 8; ++a) {
    float4 q = *reinterpret_cast<const float4*>(xc + 256 * a + 2 * t);
    v[2 * a] = cmk(q.x, q.y);
    v[2 * a + 1] = cmk(q.z, q.w);
  }
  FCtx f = make_fctx(TW, t);
  const float sc = 1.0f / (2048.0f * 1024.0f);
  const u32* wc = WT + (size_t)col * SEQ;
  u32 wq[16], wn[16];
#pragma unroll
  for (int j = 0; j < 8; ++j) {
    wq[j] = wc[f.kA + 256 * j];
    wq[8 + j] = wc[f.kB + 256 * j];
  }
  if (SPECIAL) {
    const u32* w1 = WT + (size_t)512 * SEQ;
#pragma unroll
    for (int j = 0; j < 8; ++j) {
      wn[j] = w1[f.kA + 256 * j];
      wn[8 + j] = w1[f.kB + 256 * j];
    }
  }
  fft_reg<16, -1>(v);
  fft2048_fwd_rest(v, buf, f);
  if (!SPECIAL || col != 0) {
#pragma unroll
    for (int r = 0; r < 16; ++r) v[r] = cmul(v[r], unpk(wq[r], sc));
  } else {
    const bool sp = (t == 0);
    cf oP[8], oQ[8];
    auto pair = [&](cf a, cf b, u32 pa, u32 pb, u32 qa, u32 qb, cf& hp, cf& hq) {
      cf A = cmk(0.5f * (a.x + b.x), 0.5f * (a.y - b.y));
      cf B = cmk(0.5f * (a.y + b.y), -0.5f * (a.x - b.x));
      cf p = unpk(pa, 1.f), q = unpk(pb, 1.f);
      cf wh0 = cmk(0.5f * sc * (p.x + q.x), 0.5f * sc * (p.y - q.y));
      p = unpk(qa, 1.f); q = unpk(qb, 1.f);
      cf wh1 = cmk(0.5f * sc * (p.x + q.x), 0.5f * sc * (p.y - q.y));
      cf h0 = cmul(A, wh0), h1 = cmul(B, wh1);
      hp = cmk(h0.x - h1.y, h0.y + h1.x);
      hq = cmk(h0.x + h1.y, -h0.y + h1.x);
    };
    static_for<0, 8>([&](auto ic) {
      constexpr int j = decltype(ic)::value;
      constexpr int ia = j, ib = 15 - j;
      constexpr int sa = (j < 4) ? j : 8 + j;
      constexpr int sb = (j == 0) ? 0 : (j < 4) ? 8 - j : 15 - j;
      pair(csel(sp, v[sa], v[ia]), csel(sp, v[sb], v[ib]), sp ? wq[sa] : wq[ia], sp ? wq[sb] : wq[ib],
           sp ? wn[sa] : wn[ia], sp ? wn[sb] : wn[ib], oP[j], oQ[j]);
    });
    cf s4, s4b;
    pair(v[4], v[4], wq[4], wq[4], wn[4], wn[4], s4, s4b);
    v[0] = oP[0];
    v[1] = oP[1];
    v[2] = oP[2];
    v[3] = oP[3];
    v[4] = csel(sp, s4, oP[4]);
    v[5] = csel(sp, oQ[3], oP[5]);
    v[6] = csel(sp, oQ[2], oP[6]);
    v[7] = csel(sp, oQ[1], oP[7]);
    v[8] = oQ[7];
    v[9] = oQ[6];
    v[10] = oQ[5];
    v[11] = oQ[4];
    v[12] = csel(sp, oP[4], oQ[3]);
    v[13] = csel(sp, oP[5], oQ[2]);
    v[14] = csel(sp, oP[6], oQ[1]);
    v[15] = csel(sp, oP[7], oQ[0]);
  }
  fft2048_inv_first(v, buf, f);
  fft_reg<16, 1>(v);
  const int off = (col * SEQ + 2 * t) * 8;
#pragma unroll
  for (int a = 0; a < 8; ++a) st_sc1(X2T, off + 2048 * a, v[2 * a], v[2 * a + 1]);
}

__device__ __forceinline__ int kpos(int l, int s) {
  if (s == 0) return l;
  int half = 1 << (s - 1);
  int q = (l + half - 1 + ((l >> s) & 1)) >> s;
  return 128 * (s + 1) + q - 128;
}

__device__ __forceinline__ void aconv_block(const u32* __restrict__ uT, const float* __restrict__ kl, const cf* __restrict__ TW,
                                            const u16* __restrict__ Dskip, u32* ost, cf* buf, int d, int t) {
  cf v[16], kz[16];
  u32 uw[8];
  {
    static_for<0, 8>([&](auto ic) {
      constexpr int a = decltype(ic)::value;
      constexpr int s = (a < 1) ? 0 : (a < 2) ? 1 : (a < 4) ? 2 : 3;
      int l = 256 * a + 2 * t;
      kz[a] = cmk(kl[kpos(l, s)], kl[kpos(l + 1, s)]);
    });
    const u32* ur = uT + (size_t)d * (SEQ / 2);
#pragma unroll
    for (int a = 0; a < 8; ++a) uw[a] = ur[128 * a + t];
  }
  FCtx f = make_fctx(TW, t);
  cf L = TW[f.kA];
  cf L2 = TW[t == 0 ? 128 : f.kA];
  fft16_halfzero<-1>(kz);
  fft2048_fwd_rest(kz, buf, f);
#pragma unroll
  for (int a = 0; a < 8; ++a) v[a] = cmk(bf2f(uw[a] & 0xffffu), bf2f(uw[a] >> 16)) * (0.5f / 2048.0f);
  fft16_halfzero<-1>(v);
  fft2048_fwd_rest(v, buf, f);
  {
    const bool sp = (t == 0);
    cf oP[8], oQ[8];
    static_for<0, 8>([&](auto ic) {
      constexpr int j = decltype(ic)::value;
      cf a, b, ka, kb;
      if constexpr (j == 0) {
        a = v[0]; b = v[15]; ka = kz[0]; kb = kz[15];
      } else if constexpr (j < 4) {
        a = v[j]; ka = kz[j];
        b = csel(sp, v[8 - j], v[15 - j]);
        kb = csel(sp, kz[8 - j], kz[15 - j]);
      } else {
        a = csel(sp, v[8 + j], v[j]);
        ka = csel(sp, kz[8 + j], kz[j]);
        b = v[15 - j]; kb = kz[15 - j];
      }
      cf eu = addc(a, b);
      cf ou = oddc(a, b) * 0.5f;
      cf ek = addc(ka, kb);
      cf ok = oddc(ka, kb);
      cf w = tw64<8 * j, -1>((j < 4) ? L : L2);
      cf wo = cmul(ok, w);
      cf T = add_pi(wo, ek);
      cf T2 = conj_add_pi(wo, ek);
      oP[j] = cmad(ou, T, cmul(eu, ka));
      oQ[j] = cmadc(T2, ou, cmulc(kb, eu));
    });
    cf s0, s4;
    {
      float eu = 2.0f * v[0].x, ou = 2.0f * v[0].y;
      s0 = cmk(fmaf(eu, kz[0].x, ou * kz[0].y), fmaf(eu, kz[0].y, ou * kz[0].x));
      eu = 2.0f * v[4].x; ou = 2.0f * v[4].y;
      s4 = cmk(fmaf(eu, kz[4].x, -(ou * kz[4].y)), fmaf(eu, kz[4].y, ou * kz[4].x));
    }
    v[0] = csel(sp, s0, oP[0]);
    v[1] = oP[1];
    v[2] = oP[2];
    v[3] = oP[3];
    v[4] = csel(sp, s4, oP[4]);
    v[5] = csel(sp, oQ[3], oP[5]);
    v[6] = csel(sp, oQ[2], oP[6]);
    v[7] = csel(sp, oQ[1], oP[7]);
    v[8] = oQ[7];
    v[9] = oQ[6];
    v[10] = oQ[5];
    v[11] = oQ[4];
    v[12] = csel(sp, oP[4], oQ[3]);
    v[13] = csel(sp, oP[5], oQ[2]);
    v[14] = csel(sp, oP[6], oQ[1]);
    v[15] = csel(sp, oP[7], oQ[0]);
  }
  fft2048_inv_first(v, buf, f);
  fft_reg<16, 1>(v);
  float D = bf2f(Dskip[d]);
#pragma unroll
  for (int a = 0; a < 8; ++a) {
    u32 yb = cvtpk(v[a].x, v[a].y);
    u32 sb = cvtpk(bf2f(uw[a] & 0xffffu) * D, bf2f(uw[a] >> 16) * D);
    ost[128 * a + t] = cvtpk(bf2f(yb & 0xffffu) + bf2f(sb & 0xffffu), bf2f(yb >> 16) + bf2f(sb >> 16));
  }
}

#define TS(k)
#define NSH 16
#define CSTR 64
typedef __attribute__((address_space(1))) unsigned gu32;

__device__ __forceinline__ u32 ld_u32_a2(const u16* p) {
  u32 w;
  __builtin_memcpy(&w, p, 4);
  return w;
}

__global__ __launch_bounds__(256, 3) void k1(KParams P, float* Kc, const u16* x, cf* X1T, const u16* wr,
                                            const u16* wi, u32* WT, cf* TW, const u16* u, u16* uT, u32* cnt) {
  __shared__ __attribute__((aligned(16))) u32 smw[4 * 32 * KREC];
  cf* sm = reinterpret_cast<cf*>(smw);
  int bb = blockIdx.x, tid = threadIdx.x;
  int b = bb / 3, r3 = bb % 3;
  if (r3 < 2) {
    kgen_block(P, Kc, smw, b * 2 + r3, tid);
  } else {
    u32 xw[16];
    {
      const u32* xr = reinterpret_cast<const u32*>(x + (size_t)pos2row(8 * b + (tid >> 5)) * DCH);
#pragma unroll
      for (int a = 0; a < 16; ++a) xw[a] = xr[32 * a + (tid & 31)];
    }
    uint4 uq[2][2];
    const int ur_ = tid >> 3, uc_ = tid & 7;
#pragma unroll
    for (int it = 0; it < 2; ++it) {
      int tl = b * 2 + it;
      int l0 = (tl >> 4) * 64, d0 = (tl & 15) * 64;
#pragma unroll
      for (int h = 0; h < 2; ++h)
        uq[it][h] = *reinterpret_cast<const uint4*>(u + (size_t)(l0 + ur_ + 32 * h) * DCH + d0 + 8 * uc_);
    }
    const int ws0 = (b >> 1) * 16, kb = (b & 1) * 257, nk = 257 - (b & 1);
    u32 wa[9], wb[9];
#pragma unroll
    for (int i = 0; i < 9; ++i) {
      int e = min(tid + 256 * i, 16 * 129 - 1);
      int r = e / 129, c = e - r * 129;
      size_t o = (size_t)(ws0 + r) * DF + kb + min(2 * c, nk - 2);
      wa[i] = ld_u32_a2(wr + o);
      wb[i] = ld_u32_a2(wi + o);
    }
    if (b < 8) {
      int j = b * 256 + tid;
      float s, c;
      sincospif(-(float)j / 1024.0f, &s, &c);
      TW[j] = cmk(c, s);
    }
    if (b == 8 && tid < NSH) cnt[tid * CSTR] = 0u;
    brow_block(xw, X1T, sm, b, tid);
    __syncthreads();
    u16* ut = reinterpret_cast<u16*>(smw);
    u32* wt = smw + 4608;
#pragma unroll
    for (int it = 0; it < 2; ++it) {
      u16* tl_ = ut + it * 64 * 72;
#pragma unroll
      for (int h = 0; h < 2; ++h) {
        u32 w[4] = {uq[it][h].x, uq[it][h].y, uq[it][h].z, uq[it][h].w};
#pragma unroll
        for (int j = 0; j < 4; ++j) {
          tl_[(8 * uc_ + 2 * j) * 72 + ur_ + 32 * h] = (u16)(w[j] & 0xffffu);
          tl_[(8 * uc_ + 2 * j + 1) * 72 + ur_ + 32 * h] = (u16)(w[j] >> 16);
        }
      }
    }
#pragma unroll
    for (int i = 0; i < 9; ++i) {
      int e = min(tid + 256 * i, 16 * 129 - 1);
      int r = e / 129, c = e - r * 129;
      int kk = min(2 * c, nk - 2);
      wt[kk * 17 + r] = (wa[i] & 0xffffu) | (wb[i] << 16);
      wt[(kk + 1) * 17 + r] = (wa[i] >> 16) | (wb[i] & 0xffff0000u);
    }
    __syncthreads();
#pragma unroll
    for (int it = 0; it < 2; ++it) {
      int tl = b * 2 + it;
      int l0 = (tl >> 4) * 64, d0 = (tl & 15) * 64;
      const u16* tl_ = ut + it * 64 * 72;
#pragma unroll
      for (int h = 0; h < 2; ++h) {
        uint4 o = *reinterpret_cast<const uint4*>(tl_ + (ur_ + 32 * h) * 72 + 8 * uc_);
        u32x4 oo = {o.x, o.y, o.z, o.w};
        __builtin_nontemporal_store(oo, reinterpret_cast<u32x4*>(uT + (size_t)(d0 + ur_ + 32 * h) * SEQ + l0 + 8 * uc_));
      }
    }
#pragma unroll
    for (int i = 0; i < 5; ++i) {
      int kk = (tid >> 2) + 64 * i, q = tid & 3;
      if (kk < nk) {
        const u32* p = wt + kk * 17 + 4 * q;
        u32x4 oo = {p[0], p[1], p[2], p[3]};
        __builtin_nontemporal_store(oo, reinterpret_cast<u32x4*>(WT + (size_t)(kb + kk) * SEQ + ws0 + 4 * q));
      }
    }
  }
}

__device__ __forceinline__ void handoff(gu32* gc, int b, int tid) {
  asm volatile("s_waitcnt vmcnt(0)" ::: "memory");
  __syncthreads();
  if (tid < 64) {
    if (tid == 0) __hip_atomic_fetch_add(gc + (b & (NSH - 1)) * CSTR, 1u, __ATOMIC_RELAXED, __HIP_MEMORY_SCOPE_AGENT);
    for (unsigned spins = 0; spins < (1u << 16); ++spins) {
      unsigned c = (tid < NSH) ? __hip_atomic_load(gc + tid * CSTR, __ATOMIC_RELAXED, __HIP_MEMORY_SCOPE_AGENT) : (256u / NSH);
      if (__all(c == 256u / NSH)) break;
      __builtin_amdgcn_s_sleep(1);
    }
    __builtin_amdgcn_fence(__ATOMIC_ACQUIRE, "wavefront");
  }
  __syncthreads();
}

__global__ __launch_bounds__(256, 3) void k2(const cf* X1T, cf* X2Tp, const u32* WT, const u32* uT, const float* Kc,
                                            const cf* TW, const u16* Dskip, u16* out, u32* cnt) {
  __shared__ __attribute__((aligned(16))) u32 smw[2 * 2 * XBUF + 2048];
  cf* sm = reinterpret_cast<cf*>(smw);
  int b = blockIdx.x, tid = threadIdx.x;
  int g = tid >> 7, t = tid & 127;
  TS(0);
  if (b < 256) {
    __builtin_amdgcn_s_setprio(3);
    rsrc_t X2T = mkrsrc(X2Tp);
    if (b == 0) bcol_block<true>(X1T, X2T, WT, TW, sm + g * XBUF, 2 * b + g, t);
    else bcol_block<false>(X1T, X2T, WT, TW, sm + g * XBUF, 2 * b + g, t);
    Tw16 tb = load_tw16(TW, 4 * (tid & 31));
    cf wk[5];
#pragma unroll
    for (int i = 0; i < 5; ++i) wk[i] = TW[(2 * ((tid >> 2) + 64 * i)) & 2047];
    TS(1);
    handoff((gu32*)cnt, b, tid);
    TS(2);
    browinv_block(X2T, tb, wk, out, sm, b, tid);
    TS(3);
    TS(4);
  } else {
    int i = b - 256;
    int p = (i & 7) * 64 + (i >> 3);
    u32* ost = smw + 4 * XBUF;
    aconv_block(uT, Kc + (size_t)(2 * p + g) * KC_PITCH, TW, Dskip, ost + g * 1024, sm + g * XBUF, 2 * p + g, t);
    __syncthreads();
    TS(2);
    u32* op = reinterpret_cast<u32*>(out + 2 * p);
#pragma unroll
    for (int j = 0; j < 4; ++j) {
      int q = tid + 256 * j;
      u32 w0 = ost[q], w1 = ost[1024 + q];
      op[(size_t)(2 * q) * DCH] = (w0 & 0xffffu) | (w1 << 16);
      op[(size_t)(2 * q + 1) * DCH] = (w0 >> 16) | (w1 & 0xffff0000u);
    }
    TS(3);
    TS(4);
  }
}

extern "C" void kernel_launch(void* const* d_in, const int* in_sizes, int n_in, void* d_out, int out_size,
                              void* d_ws, size_t ws_size, hipStream_t stream) {
  const u16* u = (const u16*)d_in[0];
  KParams P;
  P.Are = (const u16*)d_in[1];
  P.Aim = (const u16*)d_in[2];
  P.Bre = (const u16*)d_in[3];
  P.Bim = (const u16*)d_in[4];
  P.Cre = (const u16*)d_in[5];
  P.Cim = (const u16*)d_in[6];
  P.logdt = (const u16*)d_in[7];
  const u16* Dskip = (const u16*)d_in[8];
  const u16* x = (const u16*)d_in[9];
  const u16* wr = (const u16*)d_in[10];
  const u16* wi = (const u16*)d_in[11];
  u16* out = (u16*)d_out;
  char* ws = (char*)d_ws;
  cf* X1T = (cf*)ws;
  u32* WT = (u32*)(ws + (size_t)8 * 1024 * 1024);
  cf* X2T = (cf*)(ws + (size_t)16 * 1024 * 1024);
  u16* uT = (u16*)(ws + (size_t)24 * 1024 * 1024);
  cf* TW = (cf*)(ws + (size_t)28 * 1024 * 1024);
  u32* cnt = (u32*)(ws + (size_t)29 * 1024 * 1024);
  float* Kc = (float*)(ws + (size_t)30 * 1024 * 1024);

  k1<<<768, 256, 0, stream>>>(P, Kc, x, X1T, wr, wi, WT, TW, u, uT, cnt);
  k2<<<768, 256, 0, stream>>>(X1T, X2T, WT, (const u32*)uT, Kc, TW, Dskip, out, cnt);
}
```

```cpp
#include <hip/hip_runtime.h>
#include <hip/hip_bf16.h>
#include <type_traits>

#define SEQ 2048
#define DCH 1024
#define NST 128
#define DF 513

typedef __attribute__((ext_vector_type(2))) float cf;
typedef unsigned int u32;
typedef unsigned short u16;
using f32x16 = __attribute__((ext_vector_type(16))) float;

template <int I, int N, class F>
__device__ __forceinline__ void static_for(F&& f) {
  if constexpr (I < N) {
    f(std::integral_constant<int, I>{});
    static_for<I + 1, N>(f);
  }
}

__device__ __forceinline__ cf cmk(float x, float y) { cf r; r.x = x; r.y = y; return r; }
__device__ __forceinline__ cf cadd(cf a, cf b) { return a + b; }
__device__ __forceinline__ cf csub(cf a, cf b) { return a - b; }
__device__ __forceinline__ cf add_pi(cf a, cf b) {
  cf r;
  asm("v_pk_add_f32 %0, %1, %2 op_sel:[0,1] op_sel_hi:[1,0] neg_lo:[0,1]" : "=v"(r) : "v"(a), "v"(b));
  return r;
}
__device__ __forceinline__ cf add_mi(cf a, cf b) {
  cf r;
  asm("v_pk_add_f32 %0, %1, %2 op_sel:[0,1] op_sel_hi:[1,0] neg_hi:[0,1]" : "=v"(r) : "v"(a), "v"(b));
  return r;
}
__device__ __forceinline__ cf cmul(cf a, cf b) {
  cf t, r;
  asm("v_pk_mul_f32 %0, %1, %2 op_sel:[0,0] op_sel_hi:[0,1]" : "=v"(t) : "v"(a), "v"(b));
  asm("v_pk_fma_f32 %0, %1, %2, %3 op_sel:[1,1,0] op_sel_hi:[1,0,1] neg_lo:[0,1,0]" : "=v"(r) : "v"(a), "v"(b), "v"(t));
  return r;
}
__device__ __forceinline__ cf cmad(cf a, cf b, cf c) {
  cf t, r;
  asm("v_pk_fma_f32 %0, %1, %2, %3 op_sel:[0,0,0] op_sel_hi:[0,1,1]" : "=v"(t) : "v"(a), "v"(b), "v"(c));
  asm("v_pk_fma_f32 %0, %1, %2, %3 op_sel:[1,1,0] op_sel_hi:[1,0,1] neg_lo:[0,1,0]" : "=v"(r) : "v"(a), "v"(b), "v"(t));
  return r;
}
__device__ __forceinline__ cf cmulc(cf a, cf b) {
  cf t, r;
  asm("v_pk_mul_f32 %0, %1, %2 op_sel:[0,0] op_sel_hi:[1,0]" : "=v"(t) : "v"(a), "v"(b));
  asm("v_pk_fma_f32 %0, %1, %2, %3 op_sel:[1,1,0] op_sel_hi:[0,1,1] neg_hi:[1,0,0]" : "=v"(r) : "v"(a), "v"(b), "v"(t));
  return r;
}
__device__ __forceinline__ cf cmadc(cf a, cf b, cf c) {
  cf t, r;
  asm("v_pk_fma_f32 %0, %1, %2, %3 op_sel:[0,0,0] op_sel_hi:[1,0,1]" : "=v"(t) : "v"(a), "v"(b), "v"(c));
  asm("v_pk_fma_f32 %0, %1, %2, %3 op_sel:[1,1,0] op_sel_hi:[0,1,1] neg_hi:[1,0,0]" : "=v"(r) : "v"(a), "v"(b), "v"(t));
  return r;
}
__device__ __forceinline__ cf addc(cf a, cf b) {
  cf r;
  asm("v_pk_add_f32 %0, %1, %2 neg_hi:[0,1]" : "=v"(r) : "v"(a), "v"(b));
  return r;
}
__device__ __forceinline__ cf oddc(cf a, cf b) {
  cf r;
  asm("v_pk_add_f32 %0, %1, %2 op_sel:[1,1] op_sel_hi:[0,0] neg_hi:[1,0]" : "=v"(r) : "v"(a), "v"(b));
  return r;
}
__device__ __forceinline__ cf conj_add_pi(cf a, cf b) {
  cf r;
  asm("v_pk_add_f32 %0, %1, %2 op_sel:[0,1] op_sel_hi:[1,0] neg_hi:[1,0]" : "=v"(r) : "v"(a), "v"(b));
  return r;
}
__device__ __forceinline__ cf cmulk(cf a, cf cs) {
  cf t, r;
  asm("v_pk_mul_f32 %0, %1, %2 op_sel:[0,0] op_sel_hi:[1,0]" : "=v"(t) : "v"(a), "s"(cs));
  asm("v_pk_fma_f32 %0, %1, %2, %3 op_sel:[1,1,0] op_sel_hi:[0,1,1] neg_lo:[1,0,0]" : "=v"(r) : "v"(a), "s"(cs), "v"(t));
  return r;
}
__device__ __forceinline__ cf csqr(cf a) { return cmk(fmaf(a.x, a.x, -(a.y * a.y)), 2.0f * a.x * a.y); }

__device__ __forceinline__ float bf2f(u32 h) { return __uint_as_float(h << 16); }
__device__ __forceinline__ u32 f2bf(float f) {
  __hip_bfloat16 b = __float2bfloat16(f);
  return (u32)(*reinterpret_cast<u16*>(&b));
}
__device__ __forceinline__ float bfround(float f) { return bf2f(f2bf(f)); }

__device__ __forceinline__ void wave_sync() {
  __builtin_amdgcn_fence(__ATOMIC_SEQ_CST, "wavefront");
  __builtin_amdgcn_wave_barrier();
  __builtin_amdgcn_fence(__ATOMIC_SEQ_CST, "wavefront");
}

constexpr double COS64_TAB[17] = {
    1.0, 0.99518472667219688624, 0.98078528040323044913, 0.95694033573220886494,
    0.92387953251128675613, 0.88192126434835502971, 0.83146961230254523708,
    0.77301045336273696081, 0.70710678118654752440, 0.63439328416364549822,
    0.55557023301960222474, 0.47139673682599764856, 0.38268343236508977173,
    0.29028467725446236764, 0.19509032201612826785, 0.09801714032956060199, 0.0};
constexpr double cos64(int j) {
  j = ((j % 64) + 64) % 64;
  if (j > 32) j = 64 - j;
  return (j <= 16) ? COS64_TAB[j] : -COS64_TAB[32 - j];
}
constexpr double sin64(int j) { return cos64(j - 16); }

template <int J, int DIR>
__device__ __forceinline__ cf tw64(cf a) {
  constexpr int j = ((J % 64) + 64) % 64;
  if constexpr (j == 0) {
    return a;
  } else if constexpr (j == 32) {
    return -a;
  } else if constexpr (j == 16) {
    return (DIR > 0) ? cmk(-a.y, a.x) : cmk(a.y, -a.x);
  } else if constexpr (j == 48) {
    return (DIR > 0) ? cmk(a.y, -a.x) : cmk(-a.y, a.x);
  } else {
    constexpr float c = (float)cos64(j);
    constexpr float s = (float)(DIR * sin64(j));
    return cmulk(a, cmk(c, s));
  }
}

template <int N, int DIR, int S>
__device__ __forceinline__ void fft_rec(const cf* in, cf* out) {
  if constexpr (N == 1) {
    out[0] = in[0];
  } else if constexpr (N == 2) {
    cf a = in[0], b = in[S];
    out[0] = cadd(a, b);
    out[1] = csub(a, b);
  } else {
    constexpr int Q = N / 4;
    cf t0[Q], t1[Q], t2[Q], t3[Q];
    fft_rec<Q, DIR, 4 * S>(in, t0);
    fft_rec<Q, DIR, 4 * S>(in + S, t1);
    fft_rec<Q, DIR, 4 * S>(in + 2 * S, t2);
    fft_rec<Q, DIR, 4 * S>(in + 3 * S, t3);
    static_for<0, Q>([&](auto ic) {
      constexpr int k = decltype(ic)::value;
      constexpr int M = 64 / N;
      cf a = t0[k];
      cf b = tw64<k * M, DIR>(t1[k]);
      cf d = tw64<3 * k * M, DIR>(t3[k]);
      cf s0, s1;
      if constexpr (2 * k * M == 16) {
        s0 = (DIR > 0) ? add_pi(a, t2[k]) : add_mi(a, t2[k]);
        s1 = (DIR > 0) ? add_mi(a, t2[k]) : add_pi(a, t2[k]);
      } else {
        cf c = tw64<2 * k * M, DIR>(t2[k]);
        s0 = cadd(a, c);
        s1 = csub(a, c);
      }
      cf s2 = cadd(b, d), s3 = csub(b, d);
      out[k] = cadd(s0, s2);
      out[k + Q] = (DIR > 0) ? add_pi(s1, s3) : add_mi(s1, s3);
      out[k + 2 * Q] = csub(s0, s2);
      out[k + 3 * Q] = (DIR > 0) ? add_mi(s1, s3) : add_pi(s1, s3);
    });
  }
}

template <int N, int DIR>
__device__ __forceinline__ void fft_reg(cf* v) {
  cf o[N];
  fft_rec<N, DIR, 1>(v, o);
#pragma unroll
  for (int i = 0; i < N; ++i) v[i] = o[i];
}

template <int DIR>
__device__ __forceinline__ void fft16_halfzero(cf* v) {
  cf a[8], b[8], oa[8], ob[8];
  static_for<0, 8>([&](auto ic) {
    constexpr int n = decltype(ic)::value;
    a[n] = v[n];
    b[n] = tw64<4 * n, DIR>(v[n]);
  });
  fft_rec<8, DIR, 1>(a, oa);
  fft_rec<8, DIR, 1>(b, ob);
#pragma unroll
  for (int k = 0; k < 8; ++k) {
    v[2 * k] = oa[k];
    v[2 * k + 1] = ob[k];
  }
}

struct Tw16 { cf w[4]; };

__device__ __forceinline__ cf cis_rev(float rev) {
  float r = rev - __builtin_rintf(rev);
  return cmk(__builtin_amdgcn_cosf(r), __builtin_amdgcn_sinf(r));
}

__device__ __forceinline__ Tw16 make_tw16(int n, int logN) {
  Tw16 b;
#pragma unroll
  for (int t = 0; t < 4; ++t) {
    int e = (n << t) & ((1 << logN) - 1);
    b.w[t] = cis_rev(-(float)e / (float)(1 << logN));
  }
  return b;
}

__device__ __forceinline__ Tw16 load_tw16(const cf* __restrict__ TW, int n) {
  Tw16 b;
#pragma unroll
  for (int t = 0; t < 4; ++t) b.w[t] = TW[(n << t) & 2047];
  return b;
}

template <bool CONJ>
__device__ __forceinline__ void apply_tw16(cf* v, const Tw16& b) {
  cf p[16];
  p[1] = b.w[0];
  p[2] = b.w[1];
  p[4] = b.w[2];
  p[8] = b.w[3];
  asm volatile("" : "+v"(p[1]), "+v"(p[2]), "+v"(p[4]), "+v"(p[8]));
  static_for<3, 16>([&](auto ic) {
    constexpr int k = decltype(ic)::value;
    constexpr int top = (k >= 8) ? 8 : (k >= 4) ? 4 : 2;
    if constexpr (k != top) p[k] = cmul(p[k - top], p[top]);
  });
#pragma unroll
  for (int k = 1; k < 16; ++k) v[k] = CONJ ? cmulc(v[k], p[k]) : cmul(v[k], p[k]);
}

__device__ __forceinline__ void swap16(cf& a, cf& b) {
  asm volatile("s_nop 1" : "+v"(a), "+v"(b));
  auto r0 = __builtin_amdgcn_permlane16_swap(__float_as_uint(a.x), __float_as_uint(b.x), false, false);
  auto r1 = __builtin_amdgcn_permlane16_swap(__float_as_uint(a.y), __float_as_uint(b.y), false, false);
  a.x = __uint_as_float(r0[0]);
  b.x = __uint_as_float(r0[1]);
  a.y = __uint_as_float(r1[0]);
  b.y = __uint_as_float(r1[1]);
}

#define XP 152
#define XBUF (16 * XP)
typedef volatile __attribute__((address_space(3))) cf* ldsp;
__device__ __forceinline__ cf lds_ld(const cf* p) { return *(ldsp)(p); }
__device__ __forceinline__ void lds_st(cf* p, cf a) { *(ldsp)(p) = a; }

struct FCtx {
  Tw16 ta, tb;
  int t, offA, offB, kA, kB;
};
__device__ __forceinline__ FCtx make_fctx(const cf* __restrict__ TW, int t) {
  FCtx f;
  int k0 = t >> 3, c = t & 7;
  f.t = t;
  f.ta = load_tw16(TW, t);
  f.tb = load_tw16(TW, 16 * c);
  int k0m = (16 - k0) & 15;
  int k1m = (k0 != 0) ? (15 - c) : ((c != 0) ? (16 - c) : 8);
  f.offA = k0 * XP + 9 * c;
  f.offB = k0m * XP + 9 * k1m;
  f.kA = k0 + 16 * c;
  f.kB = k0m + 16 * k1m;
  return f;
}

__device__ __forceinline__ void fft2048_fwd_rest(cf* v, cf* buf, const FCtx& f) {
  apply_tw16<false>(v, f.ta);
  __syncthreads();
#pragma unroll
  for (int k0 = 0; k0 < 16; ++k0) lds_st(buf + k0 * XP + f.t, v[k0]);
  __syncthreads();
  cf* rb = buf + (f.t >> 3) * XP + (f.t & 7);
#pragma unroll
  for (int b = 0; b < 16; ++b) v[b] = lds_ld(rb + 8 * b);
  fft_reg<16, -1>(v);
  apply_tw16<false>(v, f.tb);
#pragma unroll
  for (int k1 = 0; k1 < 16; ++k1) lds_st(rb + 9 * k1, v[k1]);
  __syncthreads();
#pragma unroll
  for (int j = 0; j < 8; ++j) v[j] = lds_ld(buf + f.offA + j);
#pragma unroll
  for (int j = 0; j < 8; ++j) v[8 + j] = lds_ld(buf + f.offB + j);
  fft_reg<8, -1>(v);
  fft_reg<8, -1>(v + 8);
}

__device__ __forceinline__ void fft2048_inv_first(cf* v, cf* buf, const FCtx& f) {
  fft_reg<8, 1>(v);
  fft_reg<8, 1>(v + 8);
  __syncthreads();
#pragma unroll
  for (int j = 0; j < 8; ++j) lds_st(buf + f.offA + j, v[j]);
#pragma unroll
  for (int j = 0; j < 8; ++j) lds_st(buf + f.offB + j, v[8 + j]);
  __syncthreads();
  cf* rb = buf + (f.t >> 3) * XP + (f.t & 7);
#pragma unroll
  for (int k1 = 0; k1 < 16; ++k1) v[k1] = lds_ld(rb + 9 * k1);
  apply_tw16<true>(v, f.tb);
  fft_reg<16, 1>(v);
#pragma unroll
  for (int b = 0; b < 16; ++b) lds_st(rb + 8 * b, v[b]);
  __syncthreads();
#pragma unroll
  for (int kk = 0; kk < 16; ++kk) v[kk] = lds_ld(buf + kk * XP + f.t);
  apply_tw16<true>(v, f.ta);
}

#define KC_PITCH 648
#define KREC 104
using u32x4 = __attribute__((ext_vector_type(4))) unsigned;
using u32x2 = __attribute__((ext_vector_type(2))) unsigned;
using bf16x8 = __attribute__((ext_vector_type(8))) __bf16;

__device__ __forceinline__ cf cexp_scaled(float ar2, float aih, float ail, float sc) {
  float e = __builtin_amdgcn_exp2f(ar2 * sc);
  float h = aih * sc;
  float r = (h - __builtin_rintf(h)) + ail * sc;
  cf w = cis_rev(r);
  return cmk(e * w.x, e * w.y);
}
__device__ __forceinline__ u32 cvtpk(float a, float b) {
  u32 r;
  asm("v_cvt_pk_bf16_f32 %0, %1, %2" : "=v"(r) : "v"(a), "v"(b));
  return r;
}
__device__ __forceinline__ u32x2 split2(cf g) {
  u32x2 o;
  o.x = cvtpk(g.x, g.y);
  cf h = cmk(__uint_as_float(o.x << 16), __uint_as_float(o.x & 0xffff0000u));
  cf r = g - h;
  o.y = cvtpk(r.x, r.y);
  return o;
}

struct KParams { const u16 *Are, *Aim, *Bre, *Bim, *Cre, *Cim, *logdt; };

__device__ __forceinline__ void kgen_block(const KParams& P, float* Kst, u32* lds_all, int bid, int tid) {
  int wave = tid >> 6, lane = tid & 63;
  int d = bid * 2 + (wave >> 1);
  u32* lds = lds_all + wave * 32 * KREC;
  float dt = bfround(expf(bf2f(P.logdt[d])));
  int idx = d * NST + (wave & 1) * 64 + lane;
  float ar = dt * bf2f(P.Are[idx]), ai = dt * bf2f(P.Aim[idx]);
  cf dB = cmk(dt * bf2f(P.Bre[idx]), dt * bf2f(P.Bim[idx]));
  cf C = cmk(bf2f(P.Cre[idx]), bf2f(P.Cim[idx]));
  cf c0 = cmul(C, dB);
  cf e[12];
  {
    const float L2E = 1.44269504088896341f, I2PH = 0.15915494309189535f, I2PL = 6.4206383e-09f;
    float ar2 = ar * L2E;
    float aih = ai * I2PH;
    float ail = fmaf(ai, I2PL, fmaf(ai, I2PH, -aih));
    e[0] = cexp_scaled(ar2, aih, ail, 1.0f);
    e[4] = cexp_scaled(ar2, aih, ail, 16.0f);
    e[8] = cexp_scaled(ar2, aih, ail, 256.0f);
  }
  e[1] = csqr(e[0]); e[2] = csqr(e[1]); e[3] = csqr(e[2]);
  e[5] = csqr(e[4]); e[6] = csqr(e[5]); e[7] = csqr(e[6]);
  e[9] = csqr(e[8]); e[10] = csqr(e[9]); e[11] = csqr(e[10]);
  typedef volatile __attribute__((address_space(3))) u32x2* lp2;
  float s2048 = fmaf(c0.x, e[11].x, -(c0.y * e[11].y));
  {
    auto dpp = [](float x, auto ctl, auto rm) {
      return __int_as_float(__builtin_amdgcn_update_dpp(0, __float_as_int(x), decltype(ctl)::value, decltype(rm)::value, 0xf, true));
    };
    using std::integral_constant;
    s2048 += dpp(s2048, integral_constant<int, 0x111>{}, integral_constant<int, 0xf>{});
    s2048 += dpp(s2048, integral_constant<int, 0x112>{}, integral_constant<int, 0xf>{});
    s2048 += dpp(s2048, integral_constant<int, 0x114>{}, integral_constant<int, 0xf>{});
    s2048 += dpp(s2048, integral_constant<int, 0x118>{}, integral_constant<int, 0xf>{});
    s2048 += dpp(s2048, integral_constant<int, 0x142>{}, integral_constant<int, 0xa>{});
    s2048 += dpp(s2048, integral_constant<int, 0x143>{}, integral_constant<int, 0xc>{});
  }
  u32x2 Gw[20], Ew[32];
  const int rot = 2 * ((lane >> 2) & 3);
  u32* rec0 = lds + (lane & 31) * KREC;
  u32* rec = rec0 + rot;
  {
    cf G[20];
    G[0] = cmk(c0.x, -c0.y);
    G[1] = cmulc(G[0], e[0]);
    G[2] = cmulc(G[0], e[1]);
    G[3] = cmulc(G[1], e[1]);
    G[4] = cmulc(G[0], e[2]);
    G[5] = cmulc(G[1], e[2]);
    G[6] = cmulc(G[2], e[2]);
    G[7] = cmulc(G[3], e[2]);
    G[8] = cmulc(G[0], e[8]);
    G[9] = cmulc(G[2], e[8]);
    G[10] = cmulc(G[4], e[8]);
    G[11] = cmulc(G[6], e[8]);
    G[12] = cmulc(G[0], e[9]);
    G[13] = cmulc(G[4], e[9]);
    G[14] = cmulc(G[8], e[9]);
    G[15] = cmulc(G[10], e[9]);
    G[16] = cmulc(G[0], e[10]);
    G[17] = cmulc(G[8], e[10]);
    G[18] = cmulc(G[12], e[10]);
    G[19] = cmulc(G[14], e[10]);
#pragma unroll
    for (int i = 0; i < 20; ++i) Gw[i] = split2(G[i]);
  }
  if (lane < 32) {
#pragma unroll
    for (int i = 0; i < 20; ++i) *(lp2)(rec + 2 * i) = Gw[i];
  }
  {
    cf E[32];
    E[0] = cmk(1.f, 0.f);
    E[1] = e[3]; E[2] = e[4]; E[4] = e[5]; E[8] = e[6]; E[16] = e[7];
    static_for<3, 32>([&](auto ic) {
      constexpr int k = decltype(ic)::value;
      constexpr int top = (k >= 16) ? 16 : (k >= 8) ? 8 : (k >= 4) ? 4 : 2;
      if constexpr (k != top) E[k] = cmul(E[k - top], E[top]);
    });
    Ew[0].x = 0x3f80u;
    Ew[0].y = 0u;
#pragma unroll
    for (int i = 1; i < 32; ++i) Ew[i] = split2(E[i]);
  }
  f32x16 acc;
#pragma unroll
  for (int i = 0; i < 16; ++i) acc[i] = 0.f;
  const u32* base = lds + (2 * (lane >> 5)) * KREC;
  int offB[4];
#pragma unroll
  for (int q = 0; q < 4; ++q) {
    int w = 40 + 2 * (lane & 31) + 2 * q;
    offB[q] = (w >= KREC) ? w - KREC : w;
  }
#pragma unroll
  for (int ch = 0; ch < 2; ++ch) {
    if ((lane >> 5) == ch) {
      if (ch) {
#pragma unroll
        for (int i = 0; i < 20; ++i) *(lp2)(rec + 2 * i) = Gw[i];
      }
#pragma unroll
      for (int i = 0; i < 29; ++i) *(lp2)(rec + 40 + 2 * i) = Ew[i];
#pragma unroll
      for (int i = 29; i < 32; ++i) {
        int w = 40 + 2 * i + rot;
        *(lp2)(rec0 + ((w >= KREC) ? w - KREC : w)) = Ew[i];
      }
    }
#pragma unroll
    for (int m = 0; m < 8; ++m) {
      const u32* r0 = base + 4 * m * KREC;
      const u32* ra = r0 + 2 * (lane & 31) + 2 * (m & 3);
      const u32* rb = r0 + offB[m & 3];
      u32x2 a0 = *(lp2)(ra), a1 = *(lp2)(ra + KREC), b0 = *(lp2)(rb), b1 = *(lp2)(rb + KREC);
      u32x4 A = {a0.x, a0.y, a1.x, a1.y}, A2 = {a0.y, a0.x, a1.y, a1.x}, B = {b0.x, b0.y, b1.x, b1.y};
      acc = __builtin_amdgcn_mfma_f32_32x32x16_bf16(__builtin_bit_cast(bf16x8, A), __builtin_bit_cast(bf16x8, B), acc, 0, 0, 0);
      acc = __builtin_amdgcn_mfma_f32_32x32x16_bf16(__builtin_bit_cast(bf16x8, A2), __builtin_bit_cast(bf16x8, B), acc, 0, 0, 0);
    }
  }
  float* red = reinterpret_cast<float*>(lds_all + (wave | 1) * 32 * KREC);
  wave_sync();
  if (wave & 1) {
#pragma unroll
    for (int r = 0; r < 16; ++r) red[r * 64 + lane] = acc[r];
    if (lane == 63) red[1024] = s2048;
  }
  __syncthreads();
  if (!(wave & 1)) {
    int col = lane & 31, hh = lane >> 5;
    float* dst = Kst + (wave >> 1) * KC_PITCH;
#pragma unroll
    for (int r = 0; r < 16; ++r) {
      int row = (r & 3) + 8 * (r >> 2) + 4 * hh;
      int p = -1;
      if (row < 8) p = row + 8 * col;
      else if (row < 12) p = 256 + (row - 8) + 4 * col;
      else if (row < 16) p = 384 + ((row - 12) & 1) + 64 * ((row - 12) >> 1) + 2 * col;
      else if (row < 20) p = 512 + 32 * (row - 16) + col;
      if (p >= 0) dst[p] = acc[r] + red[r * 64 + lane];
    }
    if (lane == 63) dst[640] = s2048 + red[1024];
  }
}

#define RP 34
#define RRS (16 * RP)
#define ZP 517
#define TWOFF (16 * RRS)

__device__ __forceinline__ int pos2row(int p) { return 128 * (2 * (p >> 8) + (p & 1)) + ((p & 255) >> 1); }

typedef __amdgpu_buffer_rsrc_t rsrc_t;
__device__ __forceinline__ rsrc_t mkrsrc(const void* p) {
  return __builtin_amdgcn_make_buffer_rsrc(const_cast<void*>(p), 0, 512 * SEQ * 8, 0x00020000);
}
__device__ __forceinline__ void st_sc1(rsrc_t rs, int off, cf a, cf b) {
  u32x4 q = {__float_as_uint(a.x), __float_as_uint(a.y), __float_as_uint(b.x), __float_as_uint(b.y)};
  __builtin_amdgcn_raw_buffer_store_b128(q, rs, off, 0, 16);
}
__device__ __forceinline__ void ld_sc1(rsrc_t rs, int off, cf& a, cf& b) {
  u32x4 q = __builtin_amdgcn_raw_buffer_load_b128(rs, off, 0, 16);
  a = cmk(__uint_as_float(q.x), __uint_as_float(q.y));
  b = cmk(__uint_as_float(q.z), __uint_as_float(q.w));
}

typedef __attribute__((ext_vector_type(4))) float f32x4;
__device__ __forceinline__ void st_nt(cf* p, cf a, cf b) {
  f32x4 q = {a.x, a.y, b.x, b.y};
  __builtin_nontemporal_store(q, reinterpret_cast<f32x4*>(p));
}
__device__ __forceinline__ void brow_block(const u32* xw, cf* __restrict__ X1T, cf* sm, int bid, int tid) {
  cf* tw = sm + 8 * RRS;
  int s0 = bid * 8;
  {
    tw[tid] = cis_rev(-(float)tid / 1024.0f);
    if (tid == 0) tw[256] = cmk(0.f, -1.f);
  }
  int row = tid >> 5, t = tid & 31;
  cf v[16];
#pragma unroll
  for (int a = 0; a < 16; ++a) v[a] = cmk(bf2f(xw[a] & 0xffffu), bf2f(xw[a] >> 16));
  Tw16 tb = make_tw16(t, 9);
  fft_reg<16, -1>(v);
  apply_tw16<false>(v, tb);
  cf* rb = sm + row * RRS;
  {
    int pos = (t & 1) * 16 + (t >> 1);
#pragma unroll
    for (int k0 = 0; k0 < 16; ++k0) rb[k0 * RP + pos] = v[k0];
  }
  wave_sync();
  int k0 = t & 15, h = t >> 4;
  {
    const float4* p = reinterpret_cast<const float4*>(rb + k0 * RP + h * 16);
#pragma unroll
    for (int m = 0; m < 8; ++m) {
      float4 q = p[m];
      v[2 * m] = cmk(q.x, q.y);
      v[2 * m + 1] = cmk(q.z, q.w);
    }
  }
  fft_reg<16, -1>(v);
  bool hi = h != 0;
  static_for<0, 8>([&](auto ic) {
    constexpr int j = decltype(ic)::value;
    cf a = v[j], b = v[j + 8];
    swap16(a, b);
    cf br = cmk(hi ? b.y : b.x, hi ? -b.x : b.y);
    br = tw64<2 * j, -1>(br);
    v[j] = cadd(a, br);
    v[j + 8] = csub(a, br);
  });
  __syncthreads();
  cf* zs = sm + row * ZP;
#pragma unroll
  for (int r = 0; r < 16; ++r) zs[k0 + 16 * ((r & 7) + 8 * h + 16 * (r >> 3))] = v[r];
  __syncthreads();
  int rp = tid & 3, kq = tid >> 2;
#pragma unroll
  for (int i = 0; i < 5; ++i) {
    int k = kq + 64 * i;
    if (k <= 256) {
      cf lo[2], up[2];
#pragma unroll
      for (int hh = 0; hh < 2; ++hh) {
        const cf* zr = sm + (2 * rp + hh) * ZP;
        cf a = zr[k];
        cf b = zr[(512 - k) & 511];
        cf ev = cmk(0.5f * (a.x + b.x), 0.5f * (a.y - b.y));
        cf od = cmk(0.5f * (a.y + b.y), -0.5f * (a.x - b.x));
        cf wo = cmul(od, tw[k]);
        lo[hh] = (k == 0) ? cmk(a.x + a.y, a.x - a.y) : cadd(ev, wo);
        up[hh] = cmk(ev.x - wo.x, -(ev.y - wo.y));
      }
      st_nt(X1T + (size_t)k * SEQ + s0 + 2 * rp, lo[0], lo[1]);
      if (k != 0 && k != 256) st_nt(X1T + (size_t)(512 - k) * SEQ + s0 + 2 * rp, up[0], up[1]);
    }
  }
}

__device__ __forceinline__ void browinv_block(rsrc_t X2T, const Tw16& tb, const cf* wk, u16* __restrict__ out, cf* sm,
                                              int bid, int tid) {
  int s0 = bid * 8;
  {
    int rp = tid & 3, kq = tid >> 2;
    cf a[5][2], b[5][2];
#pragma unroll
    for (int i = 0; i < 5; ++i) {
      int k = kq + 64 * i;
      if (k <= 256) {
        ld_sc1(X2T, (k * SEQ + s0 + 2 * rp) * 8, a[i][0], a[i][1]);
        ld_sc1(X2T, (((512 - k) & 511) * SEQ + s0 + 2 * rp) * 8, b[i][0], b[i][1]);
      }
    }
#pragma unroll
    for (int i = 0; i < 5; ++i) {
      int k = kq + 64 * i;
      if (k <= 256) {
#pragma unroll
        for (int h = 0; h < 2; ++h) {
          cf* zr = sm + (2 * rp + h) * ZP;
          cf aa = a[i][h], bb = b[i][h];
          if (k == 0) {
            zr[0] = cmk(aa.x + aa.y, aa.x - aa.y);
          } else {
            cf w = wk[i];
            cf ev = cmk(aa.x + bb.x, aa.y - bb.y);
            cf df = cmk(aa.x - bb.x, aa.y + bb.y);
            cf od = cmulc(df, w);
            zr[k] = cmk(ev.x - od.y, ev.y + od.x);
            if (k != 256) zr[512 - k] = cmk(ev.x + od.y, -ev.y + od.x);
          }
        }
      }
    }
  }
  __syncthreads();
  int row = tid >> 5, t = tid & 31;
  int k0 = t & 15, h = t >> 4;
  cf v[16];
  {
    const cf* zs = sm + row * ZP;
#pragma unroll
    for (int r = 0; r < 16; ++r) v[r] = zs[k0 + 16 * ((r & 7) + 8 * h + 16 * (r >> 3))];
  }
  __syncthreads();
  bool hi = h != 0;
  static_for<0, 8>([&](auto ic) {
    constexpr int j = decltype(ic)::value;
    cf a = cadd(v[j], v[j + 8]);
    cf d = csub(v[j], v[j + 8]);
    cf dr = cmk(hi ? -d.y : d.x, hi ? d.x : d.y);
    dr = tw64<2 * j, 1>(dr);
    swap16(a, dr);
    v[j] = a;
    v[j + 8] = dr;
  });
  fft_reg<16, 1>(v);
  cf* rb = sm + row * RRS;
  {
    float4* p = reinterpret_cast<float4*>(rb + k0 * RP + h * 16);
#pragma unroll
    for (int m = 0; m < 8; ++m) p[m] = make_float4(v[2 * m].x, v[2 * m].y, v[2 * m + 1].x, v[2 * m + 1].y);
  }
  wave_sync();
  {
    int pos = (t & 1) * 16 + (t >> 1);
#pragma unroll
    for (int kk = 0; kk < 16; ++kk) v[kk] = rb[kk * RP + pos];
  }
  apply_tw16<true>(v, tb);
  fft_reg<16, 1>(v);
  u32* orow = reinterpret_cast<u32*>(out + (size_t)pos2row(s0 + row) * (2 * DCH) + DCH);
#pragma unroll
  for (int a = 0; a < 16; ++a) orow[32 * a + t] = cvtpk(v[a].x, v[a].y);
}

__device__ __forceinline__ cf csel(bool c, cf a, cf b) { return cmk(c ? a.x : b.x, c ? a.y : b.y); }
__device__ __forceinline__ cf unpk(u32 w, float sc) { return cmk(bf2f(w & 0xffffu) * sc, bf2f(w >> 16) * sc); }

template <bool SPECIAL>
__device__ __forceinline__ void bcol_block(const cf* __restrict__ X1T, rsrc_t X2T, const u32* __restrict__ WT,
                                           const cf* __restrict__ TW, cf* buf, int col, int t) {
  cf v[16];
  const cf* xc = X1T + (size_t)col * SEQ;
#pragma unroll
  for (int a = 0; a < 8; ++a) {
    float4 q = *reinterpret_cast<const float4*>(xc + 256 * a + 2 * t);
    v[2 * a] = cmk(q.x, q.y);
    v[2 * a + 1] = cmk(q.z, q.w);
  }
  FCtx f = make_fctx(TW, t);
  const float sc = 1.0f / (2048.0f * 1024.0f);
  const u32* wc = WT + (size_t)col * SEQ;
  u32 wq[16], wn[16];
#pragma unroll
  for (int j = 0; j < 8; ++j) {
    wq[j] = wc[f.kA + 256 * j];
    wq[8 + j] = wc[f.kB + 256 * j];
  }
  if (SPECIAL) {
    const u32* w1 = WT + (size_t)512 * SEQ;
#pragma unroll
    for (int j = 0; j < 8; ++j) {
      wn[j] = w1[f.kA + 256 * j];
      wn[8 + j] = w1[f.kB + 256 * j];
    }
  }
  fft_reg<16, -1>(v);
  fft2048_fwd_rest(v, buf, f);
  if (!SPECIAL || col != 0) {
#pragma unroll
    for (int r = 0; r < 16; ++r) v[r] = cmul(v[r], unpk(wq[r], sc));
  } else {
    const bool sp = (t == 0);
    cf oP[8], oQ[8];
    auto pair = [&](cf a, cf b, u32 pa, u32 pb, u32 qa, u32 qb, cf& hp, cf& hq) {
      cf A = cmk(0.5f * (a.x + b.x), 0.5f * (a.y - b.y));
      cf B = cmk(0.5f * (a.y + b.y), -0.5f * (a.x - b.x));
      cf p = unpk(pa, 1.f), q = unpk(pb, 1.f);
      cf wh0 = cmk(0.5f * sc * (p.x + q.x), 0.5f * sc * (p.y - q.y));
      p = unpk(qa, 1.f); q = unpk(qb, 1.f);
      cf wh1 = cmk(0.5f * sc * (p.x + q.x), 0.5f * sc * (p.y - q.y));
      cf h0 = cmul(A, wh0), h1 = cmul(B, wh1);
      hp = cmk(h0.x - h1.y, h0.y + h1.x);
      hq = cmk(h0.x + h1.y, -h0.y + h1.x);
    };
    static_for<0, 8>([&](auto ic) {
      constexpr int j = decltype(ic)::value;
      constexpr int ia = j, ib = 15 - j;
      constexpr int sa = (j < 4) ? j : 8 + j;
      constexpr int sb = (j == 0) ? 0 : (j < 4) ? 8 - j : 15 - j;
      pair(csel(sp, v[sa], v[ia]), csel(sp, v[sb], v[ib]), sp ? wq[sa] : wq[ia], sp ? wq[sb] : wq[ib],
           sp ? wn[sa] : wn[ia], sp ? wn[sb] : wn[ib], oP[j], oQ[j]);
    });
    cf s4, s4b;
    pair(v[4], v[4], wq[4], wq[4], wn[4], wn[4], s4, s4b);
    v[0] = oP[0];
    v[1] = oP[1];
    v[2] = oP[2];
    v[3] = oP[3];
    v[4] = csel(sp, s4, oP[4]);
    v[5] = csel(sp, oQ[3], oP[5]);
    v[6] = csel(sp, oQ[2], oP[6]);
    v[7] = csel(sp, oQ[1], oP[7]);
    v[8] = oQ[7];
    v[9] = oQ[6];
    v[10] = oQ[5];
    v[11] = oQ[4];
    v[12] = csel(sp, oP[4], oQ[3]);
    v[13] = csel(sp, oP[5], oQ[2]);
    v[14] = csel(sp, oP[6], oQ[1]);
    v[15] = csel(sp, oP[7], oQ[0]);
  }
  fft2048_inv_first(v, buf, f);
  fft_reg<16, 1>(v);
  const int off = (col * SEQ + 2 * t) * 8;
#pragma unroll
  for (int a = 0; a < 8; ++a) st_sc1(X2T, off + 2048 * a, v[2 * a], v[2 * a + 1]);
}

__device__ __forceinline__ int kpos(int l, int s) {
  if (s == 0) return l;
  int half = 1 << (s - 1);
  int q = (l + half - 1 + ((l >> s) & 1)) >> s;
  return 128 * (s + 1) + q - 128;
}

__device__ __forceinline__ void aconv_block(const u32* __restrict__ uT, const float* kl, const cf* __restrict__ TW,
                                            const u16* __restrict__ Dskip, u32* ost, cf* buf, int d, int t) {
  cf v[16], kz[16];
  u32 uw[8];
  {
    static_for<0, 8>([&](auto ic) {
      constexpr int a = decltype(ic)::value;
      constexpr int s = (a < 1) ? 0 : (a < 2) ? 1 : (a < 4) ? 2 : 3;
      int l = 256 * a + 2 * t;
      kz[a] = cmk(kl[kpos(l, s)], kl[kpos(l + 1, s)]);
    });
    const u32* ur = uT + (size_t)d * (SEQ / 2);
#pragma unroll
    for (int a = 0; a < 8; ++a) uw[a] = ur[128 * a + t];
  }
  FCtx f = make_fctx(TW, t);
  cf L = TW[f.kA];
  cf L2 = TW[t == 0 ? 128 : f.kA];
  fft16_halfzero<-1>(kz);
  fft2048_fwd_rest(kz, buf, f);
#pragma unroll
  for (int a = 0; a < 8; ++a) v[a] = cmk(bf2f(uw[a] & 0xffffu), bf2f(uw[a] >> 16)) * (0.5f / 2048.0f);
  fft16_halfzero<-1>(v);
  fft2048_fwd_rest(v, buf, f);
  {
    const bool sp = (t == 0);
    cf oP[8], oQ[8];
    static_for<0, 8>([&](auto ic) {
      constexpr int j = decltype(ic)::value;
      cf a, b, ka, kb;
      if constexpr (j == 0) {
        a = v[0]; b = v[15]; ka = kz[0]; kb = kz[15];
      } else if constexpr (j < 4) {
        a = v[j]; ka = kz[j];
        b = csel(sp, v[8 - j], v[15 - j]);
        kb = csel(sp, kz[8 - j], kz[15 - j]);
      } else {
        a = csel(sp, v[8 + j], v[j]);
        ka = csel(sp, kz[8 + j], kz[j]);
        b = v[15 - j]; kb = kz[15 - j];
      }
      cf eu = addc(a, b);
      cf ou = oddc(a, b) * 0.5f;
      cf ek = addc(ka, kb);
      cf ok = oddc(ka, kb);
      cf w = tw64<8 * j, -1>((j < 4) ? L : L2);
      cf wo = cmul(ok, w);
      cf T = add_pi(wo, ek);
      cf T2 = conj_add_pi(wo, ek);
      oP[j] = cmad(ou, T, cmul(eu, ka));
      oQ[j] = cmadc(T2, ou, cmulc(kb, eu));
    });
    cf s0, s4;
    {
      float eu = 2.0f * v[0].x, ou = 2.0f * v[0].y;
      s0 = cmk(fmaf(eu, kz[0].x, ou * kz[0].y), fmaf(eu, kz[0].y, ou * kz[0].x));
      eu = 2.0f * v[4].x; ou = 2.0f * v[4].y;
      s4 = cmk(fmaf(eu, kz[4].x, -(ou * kz[4].y)), fmaf(eu, kz[4].y, ou * kz[4].x));
    }
    v[0] = csel(sp, s0, oP[0]);
    v[1] = oP[1];
    v[2] = oP[2];
    v[3] = oP[3];
    v[4] = csel(sp, s4, oP[4]);
    v[5] = csel(sp, oQ[3], oP[5]);
    v[6] = csel(sp, oQ[2], oP[6]);
    v[7] = csel(sp, oQ[1], oP[7]);
    v[8] = oQ[7];
    v[9] = oQ[6];
    v[10] = oQ[5];
    v[11] = oQ[4];
    v[12] = csel(sp, oP[4], oQ[3]);
    v[13] = csel(sp, oP[5], oQ[2]);
    v[14] = csel(sp, oP[6], oQ[1]);
    v[15] = csel(sp, oP[7], oQ[0]);
  }
  fft2048_inv_first(v, buf, f);
  fft_reg<16, 1>(v);
  float D = bf2f(Dskip[d]);
#pragma unroll
  for (int a = 0; a < 8; ++a) {
    u32 yb = cvtpk(v[a].x, v[a].y);
    u32 sb = cvtpk(bf2f(uw[a] & 0xffffu) * D, bf2f(uw[a] >> 16) * D);
    ost[128 * a + t] = cvtpk(bf2f(yb & 0xffffu) + bf2f(sb & 0xffffu), bf2f(yb >> 16) + bf2f(sb >> 16));
  }
}

#define TS(k)
#define NSH 16
#define CSTR 64
typedef __attribute__((address_space(1))) unsigned gu32;

__device__ __forceinline__ u32 ld_u32_a2(const u16* p) {
  u32 w;
  __builtin_memcpy(&w, p, 4);
  return w;
}

__global__ __launch_bounds__(256, 3) void k1(const u16* x, cf* X1T, const u16* wr,
                                            const u16* wi, u32* WT, cf* TW, const u16* u, u16* uT, u32* cnt) {
  __shared__ __attribute__((aligned(16))) u32 smw[4 * 32 * KREC];
  cf* sm = reinterpret_cast<cf*>(smw);
  int b = blockIdx.x, tid = threadIdx.x;
  {
    u32 xw[16];
    {
      const u32* xr = reinterpret_cast<const u32*>(x + (size_t)pos2row(8 * b + (tid >> 5)) * DCH);
#pragma unroll
      for (int a = 0; a < 16; ++a) xw[a] = xr[32 * a + (tid & 31)];
    }
    uint4 uq[2][2];
    const int ur_ = tid >> 3, uc_ = tid & 7;
#pragma unroll
    for (int it = 0; it < 2; ++it) {
      int tl = b * 2 + it;
      int l0 = (tl >> 4) * 64, d0 = (tl & 15) * 64;
#pragma unroll
      for (int h = 0; h < 2; ++h)
        uq[it][h] = *reinterpret_cast<const uint4*>(u + (size_t)(l0 + ur_ + 32 * h) * DCH + d0 + 8 * uc_);
    }
    const int ws0 = (b >> 1) * 16, kb = (b & 1) * 257, nk = 257 - (b & 1);
    u32 wa[9], wb[9];
#pragma unroll
    for (int i = 0; i < 9; ++i) {
      int e = min(tid + 256 * i, 16 * 129 - 1);
      int r = e / 129, c = e - r * 129;
      size_t o = (size_t)(ws0 + r) * DF + kb + min(2 * c, nk - 2);
      wa[i] = ld_u32_a2(wr + o);
      wb[i] = ld_u32_a2(wi + o);
    }
    if (b < 8) {
      int j = b * 256 + tid;
      float s, c;
      sincospif(-(float)j / 1024.0f, &s, &c);
      TW[j] = cmk(c, s);
    }
    if (b == 8 && tid < NSH) cnt[tid * CSTR] = 0u;
    brow_block(xw, X1T, sm, b, tid);
    __syncthreads();
    u16* ut = reinterpret_cast<u16*>(smw);
    u32* wt = smw + 4608;
#pragma unroll
    for (int it = 0; it < 2; ++it) {
      u16* tl_ = ut + it * 64 * 72;
#pragma unroll
      for (int h = 0; h < 2; ++h) {
        u32 w[4] = {uq[it][h].x, uq[it][h].y, uq[it][h].z, uq[it][h].w};
#pragma unroll
        for (int j = 0; j < 4; ++j) {
          tl_[(8 * uc_ + 2 * j) * 72 + ur_ + 32 * h] = (u16)(w[j] & 0xffffu);
          tl_[(8 * uc_ + 2 * j + 1) * 72 + ur_ + 32 * h] = (u16)(w[j] >> 16);
        }
      }
    }
#pragma unroll
    for (int i = 0; i < 9; ++i) {
      int e = min(tid + 256 * i, 16 * 129 - 1);
      int r = e / 129, c = e - r * 129;
      int kk = min(2 * c, nk - 2);
      wt[kk * 17 + r] = (wa[i] & 0xffffu) | (wb[i] << 16);
      wt[(kk + 1) * 17 + r] = (wa[i] >> 16) | (wb[i] & 0xffff0000u);
    }
    __syncthreads();
#pragma unroll
    for (int it = 0; it < 2; ++it) {
      int tl = b * 2 + it;
      int l0 = (tl >> 4) * 64, d0 = (tl & 15) * 64;
      const u16* tl_ = ut + it * 64 * 72;
#pragma unroll
      for (int h = 0; h < 2; ++h) {
        uint4 o = *reinterpret_cast<const uint4*>(tl_ + (ur_ + 32 * h) * 72 + 8 * uc_);
        u32x4 oo = {o.x, o.y, o.z, o.w};
        __builtin_nontemporal_store(oo, reinterpret_cast<u32x4*>(uT + (size_t)(d0 + ur_ + 32 * h) * SEQ + l0 + 8 * uc_));
      }
    }
#pragma unroll
    for (int i = 0; i < 5; ++i) {
      int kk = (tid >> 2) + 64 * i, q = tid & 3;
      if (kk < nk) {
        const u32* p = wt + kk * 17 + 4 * q;
        u32x4 oo = {p[0], p[1], p[2], p[3]};
        __builtin_nontemporal_store(oo, reinterpret_cast<u32x4*>(WT + (size_t)(kb + kk) * SEQ + ws0 + 4 * q));
      }
    }
  }
}

__device__ __forceinline__ void handoff(gu32* gc, int b, int tid) {
  asm volatile("s_waitcnt vmcnt(0)" ::: "memory");
  __syncthreads();
  if (tid < 64) {
    if (tid == 0) __hip_atomic_fetch_add(gc + (b & (NSH - 1)) * CSTR, 1u, __ATOMIC_RELAXED, __HIP_MEMORY_SCOPE_AGENT);
    for (unsigned spins = 0; spins < (1u << 16); ++spins) {
      unsigned c = (tid < NSH) ? __hip_atomic_load(gc + tid * CSTR, __ATOMIC_RELAXED, __HIP_MEMORY_SCOPE_AGENT) : (256u / NSH);
      if (__all(c == 256u / NSH)) break;
      __builtin_amdgcn_s_sleep(1);
    }
    __builtin_amdgcn_fence(__ATOMIC_ACQUIRE, "wavefront");
  }
  __syncthreads();
}

__global__ __launch_bounds__(256, 3) void k2(KParams P, const cf* X1T, cf* X2Tp, const u32* WT, const u32* uT,
                                            const cf* TW, const u16* Dskip, u16* out, u32* cnt) {
  __shared__ __attribute__((aligned(16))) u32 smw[4 * 32 * KREC];
  cf* sm = reinterpret_cast<cf*>(smw);
  int b = blockIdx.x, tid = threadIdx.x;
  int g = tid >> 7, t = tid & 127;
  TS(0);
  if (b < 256) {
    __builtin_amdgcn_s_setprio(3);
    rsrc_t X2T = mkrsrc(X2Tp);
    if (b == 0) bcol_block<true>(X1T, X2T, WT, TW, sm + g * XBUF, 2 * b + g, t);
    else bcol_block<false>(X1T, X2T, WT, TW, sm + g * XBUF, 2 * b + g, t);
    Tw16 tb = load_tw16(TW, 4 * (tid & 31));
    cf wk[5];
#pragma unroll
    for (int i = 0; i < 5; ++i) wk[i] = TW[(2 * ((tid >> 2) + 64 * i)) & 2047];
    TS(1);
    handoff((gu32*)cnt, b, tid);
    TS(2);
    browinv_block(X2T, tb, wk, out, sm, b, tid);
    TS(3);
    TS(4);
  } else {
    int i = b - 256;
    int p = (i & 7) * 64 + (i >> 3);
    u32* ost = smw + 4 * XBUF;
    float* Kst = reinterpret_cast<float*>(smw + 11136);
    kgen_block(P, Kst, smw, p, tid);
    __syncthreads();
    TS(1);
    aconv_block(uT, Kst + g * KC_PITCH, TW, Dskip, ost + g * 1024, sm + g * XBUF, 2 * p + g, t);
    __syncthreads();
    TS(2);
    u32* op = reinterpret_cast<u32*>(out + 2 * p);
#pragma unroll
    for (int j = 0; j < 4; ++j) {
      int q = tid + 256 * j;
      u32 w0 = ost[q], w1 = ost[1024 + q];
      op[(size_t)(2 * q) * DCH] = (w0 & 0xffffu) | (w1 << 16);
      op[(size_t)(2 * q + 1) * DCH] = (w0 >> 16) | (w1 & 0xffff0000u);
    }
    TS(3);
    TS(4);
  }
}

extern "C" void kernel_launch(void* const* d_in, const int* in_sizes, int n_in, void* d_out, int out_size,
                              void* d_ws, size_t ws_size, hipStream_t stream) {
  const u16* u = (const u16*)d_in[0];
  KParams P;
  P.Are = (const u16*)d_in[1];
  P.Aim = (const u16*)d_in[2];
  P.Bre = (const u16*)d_in[3];
  P.Bim = (const u16*)d_in[4];
  P.Cre = (const u16*)d_in[5];
  P.Cim = (const u16*)d_in[6];
  P.logdt = (const u16*)d_in[7];
  const u16* Dskip = (const u16*)d_in[8];
  const u16* x = (const u16*)d_in[9];
  const u16* wr = (const u16*)d_in[10];
  const u16* wi = (const u16*)d_in[11];
  u16* out = (u16*)d_out;
  char* ws = (char*)d_ws;
  cf* X1T = (cf*)ws;
  u32* WT = (u32*)(ws + (size_t)8 * 1024 * 1024);
  cf* X2T = (cf*)(ws + (size_t)16 * 1024 * 1024);
  u16* uT = (u16*)(ws + (size_t)24 * 1024 * 1024);
  cf* TW = (cf*)(ws + (size_t)28 * 1024 * 1024);
  u32* cnt = (u32*)(ws + (size_t)29 * 1024 * 1024);
  float* Kc = (float*)(ws + (size_t)30 * 1024 * 1024);

  k1<<<256, 256, 0, stream>>>(x, X1T, wr, wi, WT, TW, u, uT, cnt);
  k2<<<768, 256, 0, stream>>>(P, X1T, X2T, WT, (const u32*)uT, TW, Dskip, out, cnt);
}
```

```cpp
#include <hip/hip_runtime.h>
#include <hip/hip_bf16.h>
#include <type_traits>

#define SEQ 2048
#define DCH 1024
#define NST 128
#define DF 513

typedef __attribute__((ext_vector_type(2))) float cf;
typedef unsigned int u32;
typedef unsigned short u16;
using f32x16 = __attribute__((ext_vector_type(16))) float;

template <int I, int N, class F>
__device__ __forceinline__ void static_for(F&& f) {
  if constexpr (I < N) {
    f(std::integral_constant<int, I>{});
    static_for<I + 1, N>(f);
  }
}

__device__ __forceinline__ cf cmk(float x, float y) { cf r; r.x = x; r.y = y; return r; }
__device__ __forceinline__ cf cadd(cf a, cf b) { return a + b; }
__device__ __forceinline__ cf csub(cf a, cf b) { return a - b; }
__device__ __forceinline__ cf add_pi(cf a, cf b) {
  cf r;
  asm("v_pk_add_f32 %0, %2, %1 op_sel:[1,0] op_sel_hi:[0,1] neg_lo:[1,0]" : "=v"(r) : "v"(a), "v"(b));
  return r;
}
__device__ __forceinline__ cf add_mi(cf a, cf b) {
  cf r;
  asm("v_pk_add_f32 %0, %2, %1 op_sel:[1,0] op_sel_hi:[0,1] neg_hi:[1,0]" : "=v"(r) : "v"(a), "v"(b));
  return r;
}
__device__ __forceinline__ cf cmul(cf a, cf b) {
  cf t, r;
  asm("v_pk_mul_f32 %0, %1, %2 op_sel:[0,0] op_sel_hi:[0,1]" : "=v"(t) : "v"(a), "v"(b));
  asm("v_pk_fma_f32 %0, %1, %2, %3 op_sel:[1,1,0] op_sel_hi:[1,0,1] neg_lo:[0,1,0]" : "=v"(r) : "v"(a), "v"(b), "v"(t));
  return r;
}
__device__ __forceinline__ cf cmad(cf a, cf b, cf c) {
  cf t, r;
  asm("v_pk_fma_f32 %0, %1, %2, %3 op_sel:[0,0,0] op_sel_hi:[0,1,1]" : "=v"(t) : "v"(a), "v"(b), "v"(c));
  asm("v_pk_fma_f32 %0, %1, %2, %3 op_sel:[1,1,0] op_sel_hi:[1,0,1] neg_lo:[0,1,0]" : "=v"(r) : "v"(a), "v"(b), "v"(t));
  return r;
}
__device__ __forceinline__ cf cmulc(cf a, cf b) {
  cf t, r;
  asm("v_pk_mul_f32 %0, %1, %2 op_sel:[0,0] op_sel_hi:[1,0]" : "=v"(t) : "v"(a), "v"(b));
  asm("v_pk_fma_f32 %0, %1, %2, %3 op_sel:[1,1,0] op_sel_hi:[0,1,1] neg_hi:[1,0,0]" : "=v"(r) : "v"(a), "v"(b), "v"(t));
  return r;
}
__device__ __forceinline__ cf cmadc(cf a, cf b, cf c) {
  cf t, r;
  asm("v_pk_fma_f32 %0, %1, %2, %3 op_sel:[0,0,0] op_sel_hi:[1,0,1]" : "=v"(t) : "v"(a), "v"(b), "v"(c));
  asm("v_pk_fma_f32 %0, %1, %2, %3 op_sel:[1,1,0] op_sel_hi:[0,1,1] neg_hi:[1,0,0]" : "=v"(r) : "v"(a), "v"(b), "v"(t));
  return r;
}
__device__ __forceinline__ cf addc(cf a, cf b) {
  cf r;
  asm("v_pk_add_f32 %0, %1, %2 neg_hi:[0,1]" : "=v"(r) : "v"(a), "v"(b));
  return r;
}
__device__ __forceinline__ cf subc(cf a, cf b) {
  cf r;
  asm("v_pk_add_f32 %0, %1, %2 neg_lo:[0,1]" : "=v"(r) : "v"(a), "v"(b));
  return r;
}
__device__ __forceinline__ cf oddc(cf a, cf b) {
  cf r;
  asm("v_pk_add_f32 %0, %1, %2 op_sel:[1,1] op_sel_hi:[0,0] neg_hi:[1,0]" : "=v"(r) : "v"(a), "v"(b));
  return r;
}
__device__ __forceinline__ cf conj_add_pi(cf a, cf b) {
  cf r;
  asm("v_pk_add_f32 %0, %2, %1 op_sel:[1,0] op_sel_hi:[0,1] neg_hi:[0,1]" : "=v"(r) : "v"(a), "v"(b));
  return r;
}
__device__ __forceinline__ cf cmulk(cf a, cf cs) {
  cf t, r;
  asm("v_pk_mul_f32 %0, %1, %2 op_sel:[0,0] op_sel_hi:[1,0]" : "=v"(t) : "v"(a), "s"(cs));
  asm("v_pk_fma_f32 %0, %1, %2, %3 op_sel:[1,1,0] op_sel_hi:[0,1,1] neg_lo:[1,0,0]" : "=v"(r) : "v"(a), "s"(cs), "v"(t));
  return r;
}
__device__ __forceinline__ cf csqr(cf a) { return cmul(a, a); }

__device__ __forceinline__ float bf2f(u32 h) { return __uint_as_float(h << 16); }
__device__ __forceinline__ u32 f2bf(float f) {
  __hip_bfloat16 b = __float2bfloat16(f);
  return (u32)(*reinterpret_cast<u16*>(&b));
}
__device__ __forceinline__ float bfround(float f) { return bf2f(f2bf(f)); }

__device__ __forceinline__ void wave_sync() {
  __builtin_amdgcn_fence(__ATOMIC_SEQ_CST, "wavefront");
  __builtin_amdgcn_wave_barrier();
  __builtin_amdgcn_fence(__ATOMIC_SEQ_CST, "wavefront");
}

constexpr double COS64_TAB[17] = {
    1.0, 0.99518472667219688624, 0.98078528040323044913, 0.95694033573220886494,
    0.92387953251128675613, 0.88192126434835502971, 0.83146961230254523708,
    0.77301045336273696081, 0.70710678118654752440, 0.63439328416364549822,
    0.55557023301960222474, 0.47139673682599764856, 0.38268343236508977173,
    0.29028467725446236764, 0.19509032201612826785, 0.09801714032956060199, 0.0};
constexpr double cos64(int j) {
  j = ((j % 64) + 64) % 64;
  if (j > 32) j = 64 - j;
  return (j <= 16) ? COS64_TAB[j] : -COS64_TAB[32 - j];
}
constexpr double sin64(int j) { return cos64(j - 16); }

template <int J, int DIR>
__device__ __forceinline__ cf tw64(cf a) {
  constexpr int j = ((J % 64) + 64) % 64;
  if constexpr (j == 0) {
    return a;
  } else if constexpr (j == 32) {
    return -a;
  } else if constexpr (j == 16) {
    return (DIR > 0) ? cmk(-a.y, a.x) : cmk(a.y, -a.x);
  } else if constexpr (j == 48) {
    return (DIR > 0) ? cmk(a.y, -a.x) : cmk(-a.y, a.x);
  } else {
    constexpr float c = (float)cos64(j);
    constexpr float s = (float)(DIR * sin64(j));
    return cmulk(a, cmk(c, s));
  }
}

template <int N, int DIR, int S>
__device__ __forceinline__ void fft_rec(const cf* in, cf* out) {
  if constexpr (N == 1) {
    out[0] = in[0];
  } else if constexpr (N == 2) {
    cf a = in[0], b = in[S];
    out[0] = cadd(a, b);
    out[1] = csub(a, b);
  } else {
    constexpr int Q = N / 4;
    cf t0[Q], t1[Q], t2[Q], t3[Q];
    fft_rec<Q, DIR, 4 * S>(in, t0);
    fft_rec<Q, DIR, 4 * S>(in + S, t1);
    fft_rec<Q, DIR, 4 * S>(in + 2 * S, t2);
    fft_rec<Q, DIR, 4 * S>(in + 3 * S, t3);
    static_for<0, Q>([&](auto ic) {
      constexpr int k = decltype(ic)::value;
      constexpr int M = 64 / N;
      cf a = t0[k];
      cf b = tw64<k * M, DIR>(t1[k]);
      cf d = tw64<3 * k * M, DIR>(t3[k]);
      cf s0, s1;
      if constexpr (2 * k * M == 16) {
        s0 = (DIR > 0) ? add_pi(a, t2[k]) : add_mi(a, t2[k]);
        s1 = (DIR > 0) ? add_mi(a, t2[k]) : add_pi(a, t2[k]);
      } else {
        cf c = tw64<2 * k * M, DIR>(t2[k]);
        s0 = cadd(a, c);
        s1 = csub(a, c);
      }
      cf s2 = cadd(b, d), s3 = csub(b, d);
      out[k] = cadd(s0, s2);
      out[k + Q] = (DIR > 0) ? add_pi(s1, s3) : add_mi(s1, s3);
      out[k + 2 * Q] = csub(s0, s2);
      out[k + 3 * Q] = (DIR > 0) ? add_mi(s1, s3) : add_pi(s1, s3);
    });
  }
}

template <int N, int DIR>
__device__ __forceinline__ void fft_reg(cf* v) {
  cf o[N];
  fft_rec<N, DIR, 1>(v, o);
#pragma unroll
  for (int i = 0; i < N; ++i) v[i] = o[i];
}

template <int DIR>
__device__ __forceinline__ void fft16_halfzero(cf* v) {
  cf a[8], b[8], oa[8], ob[8];
  static_for<0, 8>([&](auto ic) {
    constexpr int n = decltype(ic)::value;
    a[n] = v[n];
    b[n] = tw64<4 * n, DIR>(v[n]);
  });
  fft_rec<8, DIR, 1>(a, oa);
  fft_rec<8, DIR, 1>(b, ob);
#pragma unroll
  for (int k = 0; k < 8; ++k) {
    v[2 * k] = oa[k];
    v[2 * k + 1] = ob[k];
  }
}

struct Tw16 { cf w[4]; };

__device__ __forceinline__ cf cis_rev(float rev) {
  float r = rev - __builtin_rintf(rev);
  return cmk(__builtin_amdgcn_cosf(r), __builtin_amdgcn_sinf(r));
}

__device__ __forceinline__ Tw16 make_tw16(int n, int logN) {
  Tw16 b;
#pragma unroll
  for (int t = 0; t < 4; ++t) {
    int e = (n << t) & ((1 << logN) - 1);
    b.w[t] = cis_rev(-(float)e / (float)(1 << logN));
  }
  return b;
}

__device__ __forceinline__ Tw16 load_tw16(const cf* __restrict__ TW, int n) {
  Tw16 b;
#pragma unroll
  for (int t = 0; t < 4; ++t) b.w[t] = TW[(n << t) & 2047];
  return b;
}

template <bool CONJ>
__device__ __forceinline__ void apply_tw16(cf* v, const Tw16& b) {
  cf p[16];
  p[1] = b.w[0];
  p[2] = b.w[1];
  p[4] = b.w[2];
  p[8] = b.w[3];
  asm volatile("" : "+v"(p[1]), "+v"(p[2]), "+v"(p[4]), "+v"(p[8]));
  static_for<3, 16>([&](auto ic) {
    constexpr int k = decltype(ic)::value;
    constexpr int top = (k >= 8) ? 8 : (k >= 4) ? 4 : 2;
    if constexpr (k != top) p[k] = cmul(p[k - top], p[top]);
  });
#pragma unroll
  for (int k = 1; k < 16; ++k) v[k] = CONJ ? cmulc(v[k], p[k]) : cmul(v[k], p[k]);
}

__device__ __forceinline__ void swap16(cf& a, cf& b) {
  asm volatile("s_nop 1" : "+v"(a), "+v"(b));
  auto r0 = __builtin_amdgcn_permlane16_swap(__float_as_uint(a.x), __float_as_uint(b.x), false, false);
  auto r1 = __builtin_amdgcn_permlane16_swap(__float_as_uint(a.y), __float_as_uint(b.y), false, false);
  a.x = __uint_as_float(r0[0]);
  b.x = __uint_as_float(r0[1]);
  a.y = __uint_as_float(r1[0]);
  b.y = __uint_as_float(r1[1]);
}

#define XP 152
#define XBUF (16 * XP)
typedef volatile __attribute__((address_space(3))) cf* ldsp;
__device__ __forceinline__ cf lds_ld(const cf* p) { return *(ldsp)(p); }
__device__ __forceinline__ void lds_st(cf* p, cf a) { *(ldsp)(p) = a; }

struct FCtx {
  Tw16 ta, tb;
  int t, offA, offB, kA, kB;
};
__device__ __forceinline__ FCtx make_fctx(const cf* __restrict__ TW, int t) {
  FCtx f;
  int k0 = t >> 3, c = t & 7;
  f.t = t;
  f.ta = load_tw16(TW, t);
  f.tb = load_tw16(TW, 16 * c);
  int k0m = (16 - k0) & 15;
  int k1m = (k0 != 0) ? (15 - c) : ((c != 0) ? (16 - c) : 8);
  f.offA = k0 * XP + 9 * c;
  f.offB = k0m * XP + 9 * k1m;
  f.kA = k0 + 16 * c;
  f.kB = k0m + 16 * k1m;
  return f;
}

__device__ __forceinline__ void fft2048_fwd_rest(cf* v, cf* buf, const FCtx& f) {
  apply_tw16<false>(v, f.ta);
  __syncthreads();
#pragma unroll
  for (int k0 = 0; k0 < 16; ++k0) lds_st(buf + k0 * XP + f.t, v[k0]);
  __syncthreads();
  cf* rb = buf + (f.t >> 3) * XP + (f.t & 7);
#pragma unroll
  for (int b = 0; b < 16; ++b) v[b] = lds_ld(rb + 8 * b);
  fft_reg<16, -1>(v);
  apply_tw16<false>(v, f.tb);
#pragma unroll
  for (int k1 = 0; k1 < 16; ++k1) lds_st(rb + 9 * k1, v[k1]);
  __syncthreads();
#pragma unroll
  for (int j = 0; j < 8; ++j) v[j] = lds_ld(buf + f.offA + j);
#pragma unroll
  for (int j = 0; j < 8; ++j) v[8 + j] = lds_ld(buf + f.offB + j);
  fft_reg<8, -1>(v);
  fft_reg<8, -1>(v + 8);
}

__device__ __forceinline__ void fft2048_inv_first(cf* v, cf* buf, const FCtx& f) {
  fft_reg<8, 1>(v);
  fft_reg<8, 1>(v + 8);
  __syncthreads();
#pragma unroll
  for (int j = 0; j < 8; ++j) lds_st(buf + f.offA + j, v[j]);
#pragma unroll
  for (int j = 0; j < 8; ++j) lds_st(buf + f.offB + j, v[8 + j]);
  __syncthreads();
  cf* rb = buf + (f.t >> 3) * XP + (f.t & 7);
#pragma unroll
  for (int k1 = 0; k1 < 16; ++k1) v[k1] = lds_ld(rb + 9 * k1);
  apply_tw16<true>(v, f.tb);
  fft_reg<16, 1>(v);
#pragma unroll
  for (int b = 0; b < 16; ++b) lds_st(rb + 8 * b, v[b]);
  __syncthreads();
#pragma unroll
  for (int kk = 0; kk < 16; ++kk) v[kk] = lds_ld(buf + kk * XP + f.t);
  apply_tw16<true>(v, f.ta);
}

#define DLEN 1056
#define DA_CH (4 * DLEN)
#define U_OFF (2 * DA_CH)
#define U_LEN 1152
#define P_OFF (U_OFF + 2 * U_LEN)
#define P_LEN 648
#define OST_OFF 4224
#define KREC 104
using u32x4 = __attribute__((ext_vector_type(4))) unsigned;
using u32x2 = __attribute__((ext_vector_type(2))) unsigned;
using bf16x8 = __attribute__((ext_vector_type(8))) __bf16;

__device__ __forceinline__ cf cexp_scaled(float ar2, float aih, float ail, float sc) {
  float e = __builtin_amdgcn_exp2f(ar2 * sc);
  float h = aih * sc;
  float r = (h - __builtin_rintf(h)) + ail * sc;
  cf w = cis_rev(r);
  return cmk(e * w.x, e * w.y);
}
__device__ __forceinline__ u32 cvtpk(float a, float b) {
  u32 r;
  asm("v_cvt_pk_bf16_f32 %0, %1, %2" : "=v"(r) : "v"(a), "v"(b));
  return r;
}
__device__ __forceinline__ u32x2 split2(cf g) {
  u32x2 o;
  o.x = cvtpk(g.x, g.y);
  cf h = cmk(__uint_as_float(o.x << 16), __uint_as_float(o.x & 0xffff0000u));
  cf r = g - h;
  o.y = cvtpk(r.x, r.y);
  return o;
}

struct KParams { const u16 *Are, *Aim, *Bre, *Bim, *Cre, *Cim, *logdt; };

__device__ __forceinline__ void kgen_block(const KParams& P, u32* lds_all, int bid, int tid) {
  int wave = tid >> 6, lane = tid & 63;
  int d = bid * 2 + (wave >> 1);
  u32* lds = lds_all + wave * 32 * KREC;
  float dt = bfround(expf(bf2f(P.logdt[d])));
  int idx = d * NST + (wave & 1) * 64 + lane;
  float ar = dt * bf2f(P.Are[idx]), ai = dt * bf2f(P.Aim[idx]);
  cf dB = cmk(dt * bf2f(P.Bre[idx]), dt * bf2f(P.Bim[idx]));
  cf C = cmk(bf2f(P.Cre[idx]), bf2f(P.Cim[idx]));
  cf c0 = cmul(C, dB);
  cf e[12];
  {
    const float L2E = 1.44269504088896341f, I2PH = 0.15915494309189535f, I2PL = 6.4206383e-09f;
    float ar2 = ar * L2E;
    float aih = ai * I2PH;
    float ail = fmaf(ai, I2PL, fmaf(ai, I2PH, -aih));
    e[0] = cexp_scaled(ar2, aih, ail, 1.0f);
    e[4] = cexp_scaled(ar2, aih, ail, 16.0f);
    e[8] = cexp_scaled(ar2, aih, ail, 256.0f);
  }
  e[1] = csqr(e[0]); e[2] = csqr(e[1]); e[3] = csqr(e[2]);
  e[5] = csqr(e[4]); e[6] = csqr(e[5]); e[7] = csqr(e[6]);
  e[9] = csqr(e[8]); e[10] = csqr(e[9]); e[11] = csqr(e[10]);
  typedef volatile __attribute__((address_space(3))) u32x2* lp2;
  float s2048 = fmaf(c0.x, e[11].x, -(c0.y * e[11].y));
  {
    auto dpp = [](float x, auto ctl, auto rm) {
      return __int_as_float(__builtin_amdgcn_update_dpp(0, __float_as_int(x), decltype(ctl)::value, decltype(rm)::value, 0xf, true));
    };
    using std::integral_constant;
    s2048 += dpp(s2048, integral_constant<int, 0x111>{}, integral_constant<int, 0xf>{});
    s2048 += dpp(s2048, integral_constant<int, 0x112>{}, integral_constant<int, 0xf>{});
    s2048 += dpp(s2048, integral_constant<int, 0x114>{}, integral_constant<int, 0xf>{});
    s2048 += dpp(s2048, integral_constant<int, 0x118>{}, integral_constant<int, 0xf>{});
    s2048 += dpp(s2048, integral_constant<int, 0x142>{}, integral_constant<int, 0xa>{});
    s2048 += dpp(s2048, integral_constant<int, 0x143>{}, integral_constant<int, 0xc>{});
  }
  u32x2 Gw[20], Ew[32];
  const int rot = 2 * ((lane >> 2) & 3);
  u32* rec0 = lds + (lane & 31) * KREC;
  u32* rec = rec0 + rot;
  {
    cf G[20];
    G[0] = cmk(c0.x, -c0.y);
    G[1] = cmulc(G[0], e[0]);
    G[2] = cmulc(G[0], e[1]);
    G[3] = cmulc(G[1], e[1]);
    G[4] = cmulc(G[0], e[2]);
    G[5] = cmulc(G[1], e[2]);
    G[6] = cmulc(G[2], e[2]);
    G[7] = cmulc(G[3], e[2]);
    G[8] = cmulc(G[0], e[8]);
    G[9] = cmulc(G[2], e[8]);
    G[10] = cmulc(G[4], e[8]);
    G[11] = cmulc(G[6], e[8]);
    G[12] = cmulc(G[0], e[9]);
    G[13] = cmulc(G[4], e[9]);
    G[14] = cmulc(G[8], e[9]);
    G[15] = cmulc(G[10], e[9]);
    G[16] = cmulc(G[0], e[10]);
    G[17] = cmulc(G[8], e[10]);
    G[18] = cmulc(G[12], e[10]);
    G[19] = cmulc(G[14], e[10]);
#pragma unroll
    for (int i = 0; i < 20; ++i) Gw[i] = split2(G[i]);
  }
  if (lane < 32) {
#pragma unroll
    for (int i = 0; i < 20; ++i) *(lp2)(rec + 2 * i) = Gw[i];
  }
  {
    cf E[32];
    E[0] = cmk(1.f, 0.f);
    E[1] = e[3]; E[2] = e[4]; E[4] = e[5]; E[8] = e[6]; E[16] = e[7];
    static_for<3, 32>([&](auto ic) {
      constexpr int k = decltype(ic)::value;
      constexpr int top = (k >= 16) ? 16 : (k >= 8) ? 8 : (k >= 4) ? 4 : 2;
      if constexpr (k != top) E[k] = cmul(E[k - top], E[top]);
    });
    Ew[0].x = 0x3f80u;
    Ew[0].y = 0u;
#pragma unroll
    for (int i = 1; i < 32; ++i) Ew[i] = split2(E[i]);
  }
  f32x16 acc;
#pragma unroll
  for (int i = 0; i < 16; ++i) acc[i] = 0.f;
  const u32* base = lds + (2 * (lane >> 5)) * KREC;
  int offB[4];
#pragma unroll
  for (int q = 0; q < 4; ++q) {
    int w = 40 + 2 * (lane & 31) + 2 * q;
    offB[q] = (w >= KREC) ? w - KREC : w;
  }
#pragma unroll
  for (int ch = 0; ch < 2; ++ch) {
    if ((lane >> 5) == ch) {
      if (ch) {
#pragma unroll
        for (int i = 0; i < 20; ++i) *(lp2)(rec + 2 * i) = Gw[i];
      }
#pragma unroll
      for (int i = 0; i < 29; ++i) *(lp2)(rec + 40 + 2 * i) = Ew[i];
#pragma unroll
      for (int i = 29; i < 32; ++i) {
        int w = 40 + 2 * i + rot;
        *(lp2)(rec0 + ((w >= KREC) ? w - KREC : w)) = Ew[i];
      }
    }
#pragma unroll
    for (int m = 0; m < 8; ++m) {
      const u32* r0 = base + 4 * m * KREC;
      const u32* ra = r0 + 2 * (lane & 31) + 2 * (m & 3);
      const u32* rb = r0 + offB[m & 3];
      u32x2 a0 = *(lp2)(ra), a1 = *(lp2)(ra + KREC), b0 = *(lp2)(rb), b1 = *(lp2)(rb + KREC);
      u32x4 A = {a0.x, a0.y, a1.x, a1.y}, A2 = {a0.y, a0.x, a1.y, a1.x}, B = {b0.x, b0.y, b1.x, b1.y};
      acc = __builtin_amdgcn_mfma_f32_32x32x16_bf16(__builtin_bit_cast(bf16x8, A), __builtin_bit_cast(bf16x8, B), acc, 0, 0, 0);
      acc = __builtin_amdgcn_mfma_f32_32x32x16_bf16(__builtin_bit_cast(bf16x8, A2), __builtin_bit_cast(bf16x8, B), acc, 0, 0, 0);
    }
  }
  float* red = reinterpret_cast<float*>(lds_all + wave * 32 * KREC);
  const float* redp = reinterpret_cast<const float*>(lds_all + (wave ^ 1) * 32 * KREC);
  u32* Pt = lds_all + P_OFF + (wave >> 1) * P_LEN;
  wave_sync();
  const int col = lane & 31, hh = lane >> 5;
  if (wave & 1) {
#pragma unroll
    for (int r = 0; r < 4; ++r) red[r * 64 + lane] = acc[r];
    red[4 * 64 + lane] = acc[8];
    red[5 * 64 + lane] = acc[9];
    if (lane == 63) red[384] = s2048;
  } else {
#pragma unroll
    for (int r = 0; r < 4; ++r) red[r * 64 + lane] = acc[4 + r];
    red[4 * 64 + lane] = acc[10];
    red[5 * 64 + lane] = acc[11];
  }
  __syncthreads();
  {
    float kv[6];
    if (wave & 1) {
#pragma unroll
      for (int r = 0; r < 4; ++r) kv[r] = acc[4 + r];
      kv[4] = acc[10];
      kv[5] = acc[11];
    } else {
#pragma unroll
      for (int r = 0; r < 4; ++r) kv[r] = acc[r];
      kv[4] = acc[8];
      kv[5] = acc[9];
    }
    u32 pk[6];
#pragma unroll
    for (int r = 0; r < 6; r += 2) {
      cf gsum = cmk(kv[r] + redp[r * 64 + lane], kv[r + 1] + redp[(r + 1) * 64 + lane]);
      u32x2 hl = split2(gsum);
      pk[r] = __builtin_amdgcn_perm(hl.y, hl.x, 0x05040100u);
      pk[r + 1] = __builtin_amdgcn_perm(hl.y, hl.x, 0x07060302u);
    }
    if (wave & 1) {
      int p0 = hh ? 384 + 2 * col : 256 + 4 * col;
      int p2 = hh ? 448 + 2 * col : 258 + 4 * col;
      u32x2 q0 = {pk[0], pk[1]}, q1 = {pk[2], pk[3]};
      *(lp2)(Pt + p0) = q0;
      *(lp2)(Pt + p2) = q1;
      if (!hh) {
        Pt[576 + col] = pk[4];
        Pt[608 + col] = pk[5];
      }
      if (lane == 63) Pt[644] = 0u;
    } else {
      u32x2 q0 = {pk[0], pk[1]}, q1 = {pk[2], pk[3]};
      *(lp2)(Pt + 4 * hh + 8 * col) = q0;
      *(lp2)(Pt + 4 * hh + 8 * col + 2) = q1;
      if (!hh) {
        Pt[512 + col] = pk[4];
        Pt[544 + col] = pk[5];
      }
      if (lane == 63) {
        u32x2 hl = split2(cmk(s2048 + redp[384], 0.f));
        Pt[640] = __builtin_amdgcn_perm(hl.y, hl.x, 0x05040100u);
      }
    }
  }
}

#define RP 34
#define RRS (16 * RP)
#define ZP 517
#define TWOFF (16 * RRS)

__device__ __forceinline__ int pos2row(int p) { return 128 * (2 * (p >> 8) + (p & 1)) + ((p & 255) >> 1); }

typedef __amdgpu_buffer_rsrc_t rsrc_t;
__device__ __forceinline__ rsrc_t mkrsrc(const void* p) {
  return __builtin_amdgcn_make_buffer_rsrc(const_cast<void*>(p), 0, 512 * SEQ * 8, 0x00020000);
}
__device__ __forceinline__ void st_sc1(rsrc_t rs, int off, cf a, cf b) {
  u32x4 q = {__float_as_uint(a.x), __float_as_uint(a.y), __float_as_uint(b.x), __float_as_uint(b.y)};
  __builtin_amdgcn_raw_buffer_store_b128(q, rs, off, 0, 16);
}
__device__ __forceinline__ void ld_sc1(rsrc_t rs, int off, cf& a, cf& b) {
  u32x4 q = __builtin_amdgcn_raw_buffer_load_b128(rs, off, 0, 16);
  a = cmk(__uint_as_float(q.x), __uint_as_float(q.y));
  b = cmk(__uint_as_float(q.z), __uint_as_float(q.w));
}

typedef __attribute__((ext_vector_type(4))) float f32x4;
__device__ __forceinline__ void st_nt(cf* p, cf a, cf b) {
  f32x4 q = {a.x, a.y, b.x, b.y};
  __builtin_nontemporal_store(q, reinterpret_cast<f32x4*>(p));
}
__device__ __forceinline__ void brow_block(const u32* xw, cf* __restrict__ X1T, cf* sm, int bid, int tid) {
  cf* tw = sm + 8 * RRS;
  int s0 = bid * 8;
  {
    tw[tid] = cis_rev(-(float)tid / 1024.0f);
    if (tid == 0) tw[256] = cmk(0.f, -1.f);
  }
  int row = tid >> 5, t = tid & 31;
  cf v[16];
#pragma unroll
  for (int a = 0; a < 16; ++a) v[a] = cmk(bf2f(xw[a] & 0xffffu), bf2f(xw[a] >> 16));
  Tw16 tb = make_tw16(t, 9);
  fft_reg<16, -1>(v);
  apply_tw16<false>(v, tb);
  cf* rb = sm + row * RRS;
  {
    int pos = (t & 1) * 16 + (t >> 1);
#pragma unroll
    for (int k0 = 0; k0 < 16; ++k0) rb[k0 * RP + pos] = v[k0];
  }
  wave_sync();
  int k0 = t & 15, h = t >> 4;
  {
    const float4* p = reinterpret_cast<const float4*>(rb + k0 * RP + h * 16);
#pragma unroll
    for (int m = 0; m < 8; ++m) {
      float4 q = p[m];
      v[2 * m] = cmk(q.x, q.y);
      v[2 * m + 1] = cmk(q.z, q.w);
    }
  }
  fft_reg<16, -1>(v);
  bool hi = h != 0;
  static_for<0, 8>([&](auto ic) {
    constexpr int j = decltype(ic)::value;
    cf a = v[j], b = v[j + 8];
    swap16(a, b);
    cf br = cmk(hi ? b.y : b.x, hi ? -b.x : b.y);
    br = tw64<2 * j, -1>(br);
    v[j] = cadd(a, br);
    v[j + 8] = csub(a, br);
  });
  __syncthreads();
  cf* zs = sm + row * ZP;
#pragma unroll
  for (int r = 0; r < 16; ++r) zs[k0 + 16 * ((r & 7) + 8 * h + 16 * (r >> 3))] = v[r];
  __syncthreads();
  int rp = tid & 3, kq = tid >> 2;
#pragma unroll
  for (int i = 0; i < 5; ++i) {
    int k = kq + 64 * i;
    if (k <= 256) {
      cf lo[2], up[2];
#pragma unroll
      for (int hh = 0; hh < 2; ++hh) {
        const cf* zr = sm + (2 * rp + hh) * ZP;
        cf a = zr[k];
        cf b = zr[(512 - k) & 511];
        cf ev = cmk(0.5f * (a.x + b.x), 0.5f * (a.y - b.y));
        cf od = cmk(0.5f * (a.y + b.y), -0.5f * (a.x - b.x));
        cf wo = cmul(od, tw[k]);
        lo[hh] = (k == 0) ? conj_add_pi(a, a) : cadd(ev, wo);
        up[hh] = cmk(ev.x - wo.x, -(ev.y - wo.y));
      }
      st_nt(X1T + (size_t)k * SEQ + s0 + 2 * rp, lo[0], lo[1]);
      if (k != 0 && k != 256) st_nt(X1T + (size_t)(512 - k) * SEQ + s0 + 2 * rp, up[0], up[1]);
    }
  }
}

__device__ __forceinline__ void browinv_block(rsrc_t X2T, const Tw16& tb, const cf* wk, u16* __restrict__ out, cf* sm,
                                              int bid, int tid) {
  int s0 = bid * 8;
  {
    int rp = tid & 3, kq = tid >> 2;
    cf a[5][2], b[5][2];
#pragma unroll
    for (int i = 0; i < 5; ++i) {
      int k = kq + 64 * i;
      if (k <= 256) {
        ld_sc1(X2T, (k * SEQ + s0 + 2 * rp) * 8, a[i][0], a[i][1]);
        ld_sc1(X2T, (((512 - k) & 511) * SEQ + s0 + 2 * rp) * 8, b[i][0], b[i][1]);
      }
    }
#pragma unroll
    for (int i = 0; i < 5; ++i) {
      int k = kq + 64 * i;
      if (k <= 256) {
#pragma unroll
        for (int h = 0; h < 2; ++h) {
          cf* zr = sm + (2 * rp + h) * ZP;
          cf aa = a[i][h], bb = b[i][h];
          if (k == 0) {
            zr[0] = conj_add_pi(aa, aa);
          } else {
            cf ev = addc(aa, bb);
            cf od = cmulc(subc(aa, bb), wk[i]);
            zr[k] = add_pi(ev, od);
            if (k != 256) zr[512 - k] = conj_add_pi(ev, od);
          }
        }
      }
    }
  }
  __syncthreads();
  int row = tid >> 5, t = tid & 31;
  int k0 = t & 15, h = t >> 4;
  cf v[16];
  {
    const cf* zs = sm + row * ZP;
#pragma unroll
    for (int r = 0; r < 16; ++r) v[r] = zs[k0 + 16 * ((r & 7) + 8 * h + 16 * (r >> 3))];
  }
  __syncthreads();
  bool hi = h != 0;
  static_for<0, 8>([&](auto ic) {
    constexpr int j = decltype(ic)::value;
    cf a = cadd(v[j], v[j + 8]);
    cf d = csub(v[j], v[j + 8]);
    cf dr = cmk(hi ? -d.y : d.x, hi ? d.x : d.y);
    dr = tw64<2 * j, 1>(dr);
    swap16(a, dr);
    v[j] = a;
    v[j + 8] = dr;
  });
  fft_reg<16, 1>(v);
  cf* rb = sm + row * RRS;
  {
    float4* p = reinterpret_cast<float4*>(rb + k0 * RP + h * 16);
#pragma unroll
    for (int m = 0; m < 8; ++m) p[m] = make_float4(v[2 * m].x, v[2 * m].y, v[2 * m + 1].x, v[2 * m + 1].y);
  }
  wave_sync();
  {
    int pos = (t & 1) * 16 + (t >> 1);
#pragma unroll
    for (int kk = 0; kk < 16; ++kk) v[kk] = rb[kk * RP + pos];
  }
  apply_tw16<true>(v, tb);
  fft_reg<16, 1>(v);
  u32* orow = reinterpret_cast<u32*>(out + (size_t)pos2row(s0 + row) * (2 * DCH) + DCH);
#pragma unroll
  for (int a = 0; a < 16; ++a) orow[32 * a + t] = cvtpk(v[a].x, v[a].y);
}

__device__ __forceinline__ cf csel(bool c, cf a, cf b) { return cmk(c ? a.x : b.x, c ? a.y : b.y); }
__device__ __forceinline__ cf unpk(u32 w, float sc) { return cmk(bf2f(w & 0xffffu) * sc, bf2f(w >> 16) * sc); }

template <bool SPECIAL>
__device__ __forceinline__ void bcol_block(const cf* __restrict__ X1T, rsrc_t X2T, const u32* __restrict__ WT,
                                           const cf* __restrict__ TW, cf* buf, int col, int t) {
  cf v[16];
  const cf* xc = X1T + (size_t)col * SEQ;
#pragma unroll
  for (int a = 0; a < 8; ++a) {
    float4 q = *reinterpret_cast<const float4*>(xc + 256 * a + 2 * t);
    v[2 * a] = cmk(q.x, q.y);
    v[2 * a + 1] = cmk(q.z, q.w);
  }
  FCtx f = make_fctx(TW, t);
  const float sc = 1.0f / (2048.0f * 1024.0f);
  const u32* wc = WT + (size_t)col * SEQ;
  u32 wq[16], wn[16];
#pragma unroll
  for (int j = 0; j < 8; ++j) {
    wq[j] = wc[f.kA + 256 * j];
    wq[8 + j] = wc[f.kB + 256 * j];
  }
  if (SPECIAL) {
    const u32* w1 = WT + (size_t)512 * SEQ;
#pragma unroll
    for (int j = 0; j < 8; ++j) {
      wn[j] = w1[f.kA + 256 * j];
      wn[8 + j] = w1[f.kB + 256 * j];
    }
  }
  fft_reg<16, -1>(v);
  fft2048_fwd_rest(v, buf, f);
  if (!SPECIAL || col != 0) {
#pragma unroll
    for (int r = 0; r < 16; ++r) v[r] = cmul(v[r], unpk(wq[r], sc));
  } else {
    const bool sp = (t == 0);
    cf oP[8], oQ[8];
    auto pair = [&](cf a, cf b, u32 pa, u32 pb, u32 qa, u32 qb, cf& hp, cf& hq) {
      cf A = cmk(0.5f * (a.x + b.x), 0.5f * (a.y - b.y));
      cf B = cmk(0.5f * (a.y + b.y), -0.5f * (a.x - b.x));
      cf p = unpk(pa, 1.f), q = unpk(pb, 1.f);
      cf wh0 = cmk(0.5f * sc * (p.x + q.x), 0.5f * sc * (p.y - q.y));
      p = unpk(qa, 1.f); q = unpk(qb, 1.f);
      cf wh1 = cmk(0.5f * sc * (p.x + q.x), 0.5f * sc * (p.y - q.y));
      cf h0 = cmul(A, wh0), h1 = cmul(B, wh1);
      hp = cmk(h0.x - h1.y, h0.y + h1.x);
      hq = cmk(h0.x + h1.y, -h0.y + h1.x);
    };
    static_for<0, 8>([&](auto ic) {
      constexpr int j = decltype(ic)::value;
      constexpr int ia = j, ib = 15 - j;
      constexpr int sa = (j < 4) ? j : 8 + j;
      constexpr int sb = (j == 0) ? 0 : (j < 4) ? 8 - j : 15 - j;
      pair(csel(sp, v[sa], v[ia]), csel(sp, v[sb], v[ib]), sp ? wq[sa] : wq[ia], sp ? wq[sb] : wq[ib],
           sp ? wn[sa] : wn[ia], sp ? wn[sb] : wn[ib], oP[j], oQ[j]);
    });
    cf s4, s4b;
    pair(v[4], v[4], wq[4], wq[4], wn[4], wn[4], s4, s4b);
    v[0] = oP[0];
    v[1] = oP[1];
    v[2] = oP[2];
    v[3] = oP[3];
    v[4] = csel(sp, s4, oP[4]);
    v[5] = csel(sp, oQ[3], oP[5]);
    v[6] = csel(sp, oQ[2], oP[6]);
    v[7] = csel(sp, oQ[1], oP[7]);
    v[8] = oQ[7];
    v[9] = oQ[6];
    v[10] = oQ[5];
    v[11] = oQ[4];
    v[12] = csel(sp, oP[4], oQ[3]);
    v[13] = csel(sp, oP[5], oQ[2]);
    v[14] = csel(sp, oP[6], oQ[1]);
    v[15] = csel(sp, oP[7], oQ[0]);
  }
  fft2048_inv_first(v, buf, f);
  fft_reg<16, 1>(v);
  const int off = (col * SEQ + 2 * t) * 8;
#pragma unroll
  for (int a = 0; a < 8; ++a) st_sc1(X2T, off + 2048 * a, v[2 * a], v[2 * a + 1]);
}

using f32x4v = __attribute__((ext_vector_type(4))) float;
typedef u32x4 u32x4a4 __attribute__((aligned(4)));

constexpr int rne_loc(int j, int s) { return s == 0 ? j : (j + (1 << (s - 1)) - 1 + ((j >> s) & 1)) >> s; }

__device__ __forceinline__ void kexpand(u32* DA, const u32* Pt, int t) {
  const int m = 127 - t;
  const int s = (m < 16) ? 0 : (m < 32) ? 1 : (m < 64) ? 2 : 3;
  const int base = 128 * s + ((16 * m) >> s);
  const int s8 = 8 * s;
  u32 pv[17];
  static_for<0, 16>([&](auto ic) {
    constexpr int j = decltype(ic)::value;
    constexpr u32 tab = rne_loc(j, 0) | (rne_loc(j, 1) << 8) | (rne_loc(j, 2) << 16) | (rne_loc(j, 3) << 24);
    pv[j + 1] = Pt[base + ((tab >> s8) & 0xffu)];
  });
  {
    const int m1 = m - 1;
    const int s1 = (m1 < 16) ? 0 : (m1 < 32) ? 1 : (m1 < 64) ? 2 : 3;
    constexpr u32 tab = rne_loc(15, 0) | (rne_loc(15, 1) << 8) | (rne_loc(15, 2) << 16) | (rne_loc(15, 3) << 24);
    int ix = 128 * s1 + ((16 * m1) >> s1) + ((tab >> (8 * s1)) & 0xffu);
    pv[0] = Pt[m == 0 ? 644 : ix];
  }
  u32 he[8], ho[8], le[8], lo[8];
#pragma unroll
  for (int ii = 0; ii < 8; ++ii) {
    u32 a = pv[16 - 2 * ii], b = pv[15 - 2 * ii], c = pv[14 - 2 * ii];
    ho[ii] = __builtin_amdgcn_perm(b, a, 0x05040100u);
    lo[ii] = __builtin_amdgcn_perm(b, a, 0x07060302u);
    he[ii] = __builtin_amdgcn_perm(c, b, 0x05040100u);
    le[ii] = __builtin_amdgcn_perm(c, b, 0x07060302u);
  }
  u32* q = DA + 8 * t;
#pragma unroll
  for (int h = 0; h < 2; ++h) {
    *reinterpret_cast<u32x4*>(q + 4 * h) = u32x4{he[4 * h], he[4 * h + 1], he[4 * h + 2], he[4 * h + 3]};
    *reinterpret_cast<u32x4*>(q + DLEN + 4 * h) = u32x4{ho[4 * h], ho[4 * h + 1], ho[4 * h + 2], ho[4 * h + 3]};
    *reinterpret_cast<u32x4*>(q + 2 * DLEN + 4 * h) = u32x4{le[4 * h], le[4 * h + 1], le[4 * h + 2], le[4 * h + 3]};
    *reinterpret_cast<u32x4*>(q + 3 * DLEN + 4 * h) = u32x4{lo[4 * h], lo[4 * h + 1], lo[4 * h + 2], lo[4 * h + 3]};
  }
  if (t < 8) *reinterpret_cast<u32x4*>(DA + (t >> 1) * DLEN + 1024 + 4 * (t & 1)) = u32x4{0u, 0u, 0u, 0u};
}

__device__ __forceinline__ void conv_mfma(const u32* DA, const u32* U, int w2, int lane, f32x4v* acc) {
  const int e = lane & 15, g = lane >> 4;
  const u32* da = DA + (e & 1) * DLEN + 4 * g - (e >> 1) + 7 - 64 * w2;
  const u32* ub = U + 120 + 8 * e + 4 * g - 64 * w2 - 48;
#pragma unroll
  for (int i = 0; i < 8; ++i) acc[i] = f32x4v{0.f, 0.f, 0.f, 0.f};
#pragma unroll
  for (int rr = 0; rr < 4; ++rr) {
    u32x4 B[8];
#pragma unroll
    for (int k = 0; k < 8; ++k) B[k] = *reinterpret_cast<const u32x4*>(ub + 128 * k + 48 - 16 * rr);
#pragma unroll
    for (int sp = 0; sp < 8; ++sp) {
      u32x4 Ah = *reinterpret_cast<const u32x4a4*>(da + 1008 - 128 * sp - 16 * rr);
      u32x4 Al = *reinterpret_cast<const u32x4a4*>(da + 2 * DLEN + 1008 - 128 * sp - 16 * rr);
#pragma unroll
      for (int i = sp; i < 8; ++i)
        acc[i] = __builtin_amdgcn_mfma_f32_16x16x32_bf16(__builtin_bit_cast(bf16x8, Ah), __builtin_bit_cast(bf16x8, B[i - sp]), acc[i], 0, 0, 0);
#pragma unroll
      for (int i = sp; i < 8; ++i)
        acc[i] = __builtin_amdgcn_mfma_f32_16x16x32_bf16(__builtin_bit_cast(bf16x8, Al), __builtin_bit_cast(bf16x8, B[i - sp]), acc[i], 0, 0, 0);
    }
  }
}

#define TS(k)
#define NSH 16
#define CSTR 64
typedef __attribute__((address_space(1))) unsigned gu32;

__device__ __forceinline__ u32 ld_u32_a2(const u16* p) {
  u32 w;
  __builtin_memcpy(&w, p, 4);
  return w;
}

__global__ __launch_bounds__(256, 3) void k1(const u16* x, cf* X1T, const u16* wr,
                                            const u16* wi, u32* WT, cf* TW, const u16* u, u16* uT, u32* cnt) {
  __shared__ __attribute__((aligned(16))) u32 smw[4 * 32 * KREC];
  cf* sm = reinterpret_cast<cf*>(smw);
  int b = blockIdx.x, tid = threadIdx.x;
  {
    u32 xw[16];
    {
      const u32* xr = reinterpret_cast<const u32*>(x + (size_t)pos2row(8 * b + (tid >> 5)) * DCH);
#pragma unroll
      for (int a = 0; a < 16; ++a) xw[a] = xr[32 * a + (tid & 31)];
    }
    uint4 uq[2][2];
    const int ur_ = tid >> 3, uc_ = tid & 7;
#pragma unroll
    for (int it = 0; it < 2; ++it) {
      int tl = b * 2 + it;
      int l0 = (tl >> 4) * 64, d0 = (tl & 15) * 64;
#pragma unroll
      for (int h = 0; h < 2; ++h)
        uq[it][h] = *reinterpret_cast<const uint4*>(u + (size_t)(l0 + ur_ + 32 * h) * DCH + d0 + 8 * uc_);
    }
    const int ws0 = (b >> 1) * 16, kb = (b & 1) * 257, nk = 257 - (b & 1);
    u32 wa[9], wb[9];
#pragma unroll
    for (int i = 0; i < 9; ++i) {
      int e = min(tid + 256 * i, 16 * 129 - 1);
      int r = e / 129, c = e - r * 129;
      size_t o = (size_t)(ws0 + r) * DF + kb + min(2 * c, nk - 2);
      wa[i] = ld_u32_a2(wr + o);
      wb[i] = ld_u32_a2(wi + o);
    }
    if (b < 8) {
      int j = b * 256 + tid;
      float s, c;
      sincospif(-(float)j / 1024.0f, &s, &c);
      TW[j] = cmk(c, s);
    }
    if (b == 8 && tid < NSH) cnt[tid * CSTR] = 0u;
    brow_block(xw, X1T, sm, b, tid);
    __syncthreads();
    u16* ut = reinterpret_cast<u16*>(smw);
    u32* wt = smw + 4608;
#pragma unroll
    for (int it = 0; it < 2; ++it) {
      u16* tl_ = ut + it * 64 * 72;
#pragma unroll
      for (int h = 0; h < 2; ++h) {
        u32 w[4] = {uq[it][h].x, uq[it][h].y, uq[it][h].z, uq[it][h].w};
#pragma unroll
        for (int j = 0; j < 4; ++j) {
          tl_[(8 * uc_ + 2 * j) * 72 + ur_ + 32 * h] = (u16)(w[j] & 0xffffu);
          tl_[(8 * uc_ + 2 * j + 1) * 72 + ur_ + 32 * h] = (u16)(w[j] >> 16);
        }
      }
    }
#pragma unroll
    for (int i = 0; i < 9; ++i) {
      int e = min(tid + 256 * i, 16 * 129 - 1);
      int r = e / 129, c = e - r * 129;
      int kk = min(2 * c, nk - 2);
      wt[kk * 17 + r] = (wa[i] & 0xffffu) | (wb[i] << 16);
      wt[(kk + 1) * 17 + r] = (wa[i] >> 16) | (wb[i] & 0xffff0000u);
    }
    __syncthreads();
#pragma unroll
    for (int it = 0; it < 2; ++it) {
      int tl = b * 2 + it;
      int l0 = (tl >> 4) * 64, d0 = (tl & 15) * 64;
      const u16* tl_ = ut + it * 64 * 72;
#pragma unroll
      for (int h = 0; h < 2; ++h) {
        uint4 o = *reinterpret_cast<const uint4*>(tl_ + (ur_ + 32 * h) * 72 + 8 * uc_);
        u32x4 oo = {o.x, o.y, o.z, o.w};
        __builtin_nontemporal_store(oo, reinterpret_cast<u32x4*>(uT + (size_t)(d0 + ur_ + 32 * h) * SEQ + l0 + 8 * uc_));
      }
    }
#pragma unroll
    for (int i = 0; i < 5; ++i) {
      int kk = (tid >> 2) + 64 * i, q = tid & 3;
      if (kk < nk) {
        const u32* p = wt + kk * 17 + 4 * q;
        u32x4 oo = {p[0], p[1], p[2], p[3]};
        __builtin_nontemporal_store(oo, reinterpret_cast<u32x4*>(WT + (size_t)(kb + kk) * SEQ + ws0 + 4 * q));
      }
    }
  }
}

__device__ __forceinline__ void handoff(gu32* gc, int b, int tid) {
  asm volatile("s_waitcnt vmcnt(0)" ::: "memory");
  __syncthreads();
  if (tid < 64) {
    if (tid == 0) __hip_atomic_fetch_add(gc + (b & (NSH - 1)) * CSTR, 1u, __ATOMIC_RELAXED, __HIP_MEMORY_SCOPE_AGENT);
    for (unsigned spins = 0; spins < (1u << 16); ++spins) {
      unsigned c = (tid < NSH) ? __hip_atomic_load(gc + tid * CSTR, __ATOMIC_RELAXED, __HIP_MEMORY_SCOPE_AGENT) : (256u / NSH);
      if (__all(c == 256u / NSH)) break;
      __builtin_amdgcn_s_sleep(1);
    }
    __builtin_amdgcn_fence(__ATOMIC_ACQUIRE, "wavefront");
  }
  __syncthreads();
}

__global__ __launch_bounds__(256, 3) void k2(KParams P, const cf* X1T, cf* X2Tp, const u32* WT, const u32* uT,
                                            const cf* TW, const u16* Dskip, u16* out, u32* cnt) {
  __shared__ __attribute__((aligned(16))) u32 smw[4 * 32 * KREC];
  cf* sm = reinterpret_cast<cf*>(smw);
  int b = blockIdx.x, tid = threadIdx.x;
  int g = tid >> 7, t = tid & 127;
  TS(0);
  if (b < 256) {
    __builtin_amdgcn_s_setprio(3);
    rsrc_t X2T = mkrsrc(X2Tp);
    if (b == 0) bcol_block<true>(X1T, X2T, WT, TW, sm + g * XBUF, 2 * b + g, t);
    else bcol_block<false>(X1T, X2T, WT, TW, sm + g * XBUF, 2 * b + g, t);
    Tw16 tb = load_tw16(TW, 4 * (tid & 31));
    cf wk[5];
#pragma unroll
    for (int i = 0; i < 5; ++i) wk[i] = TW[(2 * ((tid >> 2) + 64 * i)) & 2047];
    TS(1);
    handoff((gu32*)cnt, b, tid);
    TS(2);
    browinv_block(X2T, tb, wk, out, sm, b, tid);
    TS(3);
    TS(4);
  } else {
    int i = b - 256;
    int p = (i & 7) * 64 + (i >> 3);
    const int d = 2 * p + g, wave = tid >> 6, lane = tid & 63, w2 = wave & 1;
    u32x4 uq[2];
    {
      const u32x4* ur = reinterpret_cast<const u32x4*>(uT + (size_t)d * (SEQ / 2) + 8 * t);
      uq[0] = ur[0];
      uq[1] = ur[1];
    }
    const float D = bf2f(Dskip[d]);
    kgen_block(P, smw, p, tid);
    __syncthreads();
    TS(1);
    u32* DA = smw + g * DA_CH;
    u32* U = smw + U_OFF + g * U_LEN;
    kexpand(DA, smw + P_OFF + g * P_LEN, t);
    *reinterpret_cast<u32x4*>(U + 128 + 8 * t) = uq[0];
    *reinterpret_cast<u32x4*>(U + 128 + 8 * t + 4) = uq[1];
    if (t < 32) *reinterpret_cast<u32x4*>(U + 4 * t) = u32x4{0u, 0u, 0u, 0u};
    __syncthreads();
    f32x4v acc[8];
    conv_mfma(DA, U, w2, lane, acc);
    __syncthreads();
    {
      f32x4v* ex = reinterpret_cast<f32x4v*>(smw) + wave * 256;
      const f32x4v* exp_ = reinterpret_cast<const f32x4v*>(smw) + (wave ^ 1) * 256;
      f32x4v kp[4];
#pragma unroll
      for (int i = 0; i < 4; ++i) {
        f32x4v a0 = acc[i], a1 = acc[4 + i], gv;
#pragma unroll
        for (int j = 0; j < 4; ++j) {
          gv[j] = w2 ? a0[j] : a1[j];
          kp[i][j] = w2 ? a1[j] : a0[j];
        }
        ex[i * 64 + lane] = gv;
      }
      __syncthreads();
      const int q0 = 512 * w2 + 8 * (lane & 15) + 2 * (lane >> 4);
      u32* osc = smw + OST_OFF + g * 1024;
#pragma unroll
      for (int i = 0; i < 4; ++i) {
        f32x4v y = kp[i] + exp_[i * 64 + lane];
        u32x2 uw = *reinterpret_cast<const u32x2*>(U + 128 + q0 + 128 * i);
        u32x2 o;
        {
          u32 yb = cvtpk(y[0], y[1]);
          u32 sb = cvtpk(bf2f(uw.x & 0xffffu) * D, bf2f(uw.x >> 16) * D);
          o.x = cvtpk(bf2f(yb & 0xffffu) + bf2f(sb & 0xffffu), bf2f(yb >> 16) + bf2f(sb >> 16));
          yb = cvtpk(y[2], y[3]);
          sb = cvtpk(bf2f(uw.y & 0xffffu) * D, bf2f(uw.y >> 16) * D);
          o.y = cvtpk(bf2f(yb & 0xffffu) + bf2f(sb & 0xffffu), bf2f(yb >> 16) + bf2f(sb >> 16));
        }
        *reinterpret_cast<u32x2*>(osc + q0 + 128 * i) = o;
      }
    }
    __syncthreads();
    TS(2);
    const u32* ost = smw + OST_OFF;
    u32* op = reinterpret_cast<u32*>(out + 2 * p);
#pragma unroll
    for (int j = 0; j < 4; ++j) {
      int q = tid + 256 * j;
      u32 w0 = ost[q], w1 = ost[1024 + q];
      op[(size_t)(2 * q) * DCH] = (w0 & 0xffffu) | (w1 << 16);
      op[(size_t)(2 * q + 1) * DCH] = (w0 >> 16) | (w1 & 0xffff0000u);
    }
    TS(3);
    TS(4);
  }
}

extern "C" void kernel_launch(void* const* d_in, const int* in_sizes, int n_in, void* d_out, int out_size,
                              void* d_ws, size_t ws_size, hipStream_t stream) {
  const u16* u = (const u16*)d_in[0];
  KParams P;
  P.Are = (const u16*)d_in[1];
  P.Aim = (const u16*)d_in[2];
  P.Bre = (const u16*)d_in[3];
  P.Bim = (const u16*)d_in[4];
  P.Cre = (const u16*)d_in[5];
  P.Cim = (const u16*)d_in[6];
  P.logdt = (const u16*)d_in[7];
  const u16* Dskip = (const u16*)d_in[8];
  const u16* x = (const u16*)d_in[9];
  const u16* wr = (const u16*)d_in[10];
  const u16* wi = (const u16*)d_in[11];
  u16* out = (u16*)d_out;
  char* ws = (char*)d_ws;
  cf* X1T = (cf*)ws;
  u32* WT = (u32*)(ws + (size_t)8 * 1024 * 1024);
  cf* X2T = (cf*)(ws + (size_t)16 * 1024 * 1024);
  u16* uT = (u16*)(ws + (size_t)24 * 1024 * 1024);
  cf* TW = (cf*)(ws + (size_t)28 * 1024 * 1024);
  u32* cnt = (u32*)(ws + (size_t)29 * 1024 * 1024);
  float* Kc = (float*)(ws + (size_t)30 * 1024 * 1024);

  k1<<<256, 256, 0, stream>>>(x, X1T, wr, wi, WT, TW, u, uT, cnt);
  k2<<<768, 256, 0, stream>>>(P, X1T, X2T, WT, (const u32*)uT, TW, Dskip, out, cnt);
}
```

```cpp
#define GATEPOS 2
#include <hip/hip_runtime.h>
#include <hip/hip_bf16.h>
#include <type_traits>

#define SEQ 2048
#define DCH 1024
#define NST 128
#define DF 513

typedef __attribute__((ext_vector_type(2))) float cf;
typedef unsigned int u32;
typedef unsigned short u16;
using f32x16 = __attribute__((ext_vector_type(16))) float;

template <int I, int N, class F>
__device__ __forceinline__ void static_for(F&& f) {
  if constexpr (I < N) {
    f(std::integral_constant<int, I>{});
    static_for<I + 1, N>(f);
  }
}

__device__ __forceinline__ cf cmk(float x, float y) { cf r; r.x = x; r.y = y; return r; }
__device__ __forceinline__ cf cadd(cf a, cf b) { return a + b; }
__device__ __forceinline__ cf csub(cf a, cf b) { return a - b; }
__device__ __forceinline__ cf add_pi(cf a, cf b) {
  cf r;
  asm("v_pk_add_f32 %0, %2, %1 op_sel:[1,0] op_sel_hi:[0,1] neg_lo:[1,0]" : "=v"(r) : "v"(a), "v"(b));
  return r;
}
__device__ __forceinline__ cf add_mi(cf a, cf b) {
  cf r;
  asm("v_pk_add_f32 %0, %2, %1 op_sel:[1,0] op_sel_hi:[0,1] neg_hi:[1,0]" : "=v"(r) : "v"(a), "v"(b));
  return r;
}
__device__ __forceinline__ cf cmul(cf a, cf b) {
  cf t, r;
  asm("v_pk_mul_f32 %0, %1, %2 op_sel:[0,0] op_sel_hi:[0,1]" : "=v"(t) : "v"(a), "v"(b));
  asm("v_pk_fma_f32 %0, %1, %2, %3 op_sel:[1,1,0] op_sel_hi:[1,0,1] neg_lo:[0,1,0]" : "=v"(r) : "v"(a), "v"(b), "v"(t));
  return r;
}
__device__ __forceinline__ cf cmad(cf a, cf b, cf c) {
  cf t, r;
  asm("v_pk_fma_f32 %0, %1, %2, %3 op_sel:[0,0,0] op_sel_hi:[0,1,1]" : "=v"(t) : "v"(a), "v"(b), "v"(c));
  asm("v_pk_fma_f32 %0, %1, %2, %3 op_sel:[1,1,0] op_sel_hi:[1,0,1] neg_lo:[0,1,0]" : "=v"(r) : "v"(a), "v"(b), "v"(t));
  return r;
}
__device__ __forceinline__ cf cmulc(cf a, cf b) {
  cf t, r;
  asm("v_pk_mul_f32 %0, %1, %2 op_sel:[0,0] op_sel_hi:[1,0]" : "=v"(t) : "v"(a), "v"(b));
  asm("v_pk_fma_f32 %0, %1, %2, %3 op_sel:[1,1,0] op_sel_hi:[0,1,1] neg_hi:[1,0,0]" : "=v"(r) : "v"(a), "v"(b), "v"(t));
  return r;
}
__device__ __forceinline__ cf cmadc(cf a, cf b, cf c) {
  cf t, r;
  asm("v_pk_fma_f32 %0, %1, %2, %3 op_sel:[0,0,0] op_sel_hi:[1,0,1]" : "=v"(t) : "v"(a), "v"(b), "v"(c));
  asm("v_pk_fma_f32 %0, %1, %2, %3 op_sel:[1,1,0] op_sel_hi:[0,1,1] neg_hi:[1,0,0]" : "=v"(r) : "v"(a), "v"(b), "v"(t));
  return r;
}
__device__ __forceinline__ cf addc(cf a, cf b) {
  cf r;
  asm("v_pk_add_f32 %0, %1, %2 neg_hi:[0,1]" : "=v"(r) : "v"(a), "v"(b));
  return r;
}
__device__ __forceinline__ cf subc(cf a, cf b) {
  cf r;
  asm("v_pk_add_f32 %0, %1, %2 neg_lo:[0,1]" : "=v"(r) : "v"(a), "v"(b));
  return r;
}
__device__ __forceinline__ cf oddc(cf a, cf b) {
  cf r;
  asm("v_pk_add_f32 %0, %1, %2 op_sel:[1,1] op_sel_hi:[0,0] neg_hi:[1,0]" : "=v"(r) : "v"(a), "v"(b));
  return r;
}
__device__ __forceinline__ cf conj_add_pi(cf a, cf b) {
  cf r;
  asm("v_pk_add_f32 %0, %2, %1 op_sel:[1,0] op_sel_hi:[0,1] neg_hi:[0,1]" : "=v"(r) : "v"(a), "v"(b));
  return r;
}
__device__ __forceinline__ cf cmulk(cf a, cf cs) {
  cf t, r;
  asm("v_pk_mul_f32 %0, %1, %2 op_sel:[0,0] op_sel_hi:[1,0]" : "=v"(t) : "v"(a), "s"(cs));
  asm("v_pk_fma_f32 %0, %1, %2, %3 op_sel:[1,1,0] op_sel_hi:[0,1,1] neg_lo:[1,0,0]" : "=v"(r) : "v"(a), "s"(cs), "v"(t));
  return r;
}
__device__ __forceinline__ cf csqr(cf a) { return cmul(a, a); }

__device__ __forceinline__ float bf2f(u32 h) { return __uint_as_float(h << 16); }
__device__ __forceinline__ u32 f2bf(float f) {
  __hip_bfloat16 b = __float2bfloat16(f);
  return (u32)(*reinterpret_cast<u16*>(&b));
}
__device__ __forceinline__ float bfround(float f) { return bf2f(f2bf(f)); }

__device__ __forceinline__ void wave_sync() {
  __builtin_amdgcn_fence(__ATOMIC_SEQ_CST, "wavefront");
  __builtin_amdgcn_wave_barrier();
  __builtin_amdgcn_fence(__ATOMIC_SEQ_CST, "wavefront");
}

constexpr double COS64_TAB[17] = {
    1.0, 0.99518472667219688624, 0.98078528040323044913, 0.95694033573220886494,
    0.92387953251128675613, 0.88192126434835502971, 0.83146961230254523708,
    0.77301045336273696081, 0.70710678118654752440, 0.63439328416364549822,
    0.55557023301960222474, 0.47139673682599764856, 0.38268343236508977173,
    0.29028467725446236764, 0.19509032201612826785, 0.09801714032956060199, 0.0};
constexpr double cos64(int j) {
  j = ((j % 64) + 64) % 64;
  if (j > 32) j = 64 - j;
  return (j <= 16) ? COS64_TAB[j] : -COS64_TAB[32 - j];
}
constexpr double sin64(int j) { return cos64(j - 16); }

template <int J, int DIR>
__device__ __forceinline__ cf tw64(cf a) {
  constexpr int j = ((J % 64) + 64) % 64;
  if constexpr (j == 0) {
    return a;
  } else if constexpr (j == 32) {
    return -a;
  } else if constexpr (j == 16) {
    return (DIR > 0) ? cmk(-a.y, a.x) : cmk(a.y, -a.x);
  } else if constexpr (j == 48) {
    return (DIR > 0) ? cmk(a.y, -a.x) : cmk(-a.y, a.x);
  } else {
    constexpr float c = (float)cos64(j);
    constexpr float s = (float)(DIR * sin64(j));
    return cmulk(a, cmk(c, s));
  }
}

template <int N, int DIR, int S>
__device__ __forceinline__ void fft_rec(const cf* in, cf* out) {
  if constexpr (N == 1) {
    out[0] = in[0];
  } else if constexpr (N == 2) {
    cf a = in[0], b = in[S];
    out[0] = cadd(a, b);
    out[1] = csub(a, b);
  } else {
    constexpr int Q = N / 4;
    cf t0[Q], t1[Q], t2[Q], t3[Q];
    fft_rec<Q, DIR, 4 * S>(in, t0);
    fft_rec<Q, DIR, 4 * S>(in + S, t1);
    fft_rec<Q, DIR, 4 * S>(in + 2 * S, t2);
    fft_rec<Q, DIR, 4 * S>(in + 3 * S, t3);
    static_for<0, Q>([&](auto ic) {
      constexpr int k = decltype(ic)::value;
      constexpr int M = 64 / N;
      cf a = t0[k];
      cf b = tw64<k * M, DIR>(t1[k]);
      cf d = tw64<3 * k * M, DIR>(t3[k]);
      cf s0, s1;
      if constexpr (2 * k * M == 16) {
        s0 = (DIR > 0) ? add_pi(a, t2[k]) : add_mi(a, t2[k]);
        s1 = (DIR > 0) ? add_mi(a, t2[k]) : add_pi(a, t2[k]);
      } else {
        cf c = tw64<2 * k * M, DIR>(t2[k]);
        s0 = cadd(a, c);
        s1 = csub(a, c);
      }
      cf s2 = cadd(b, d), s3 = csub(b, d);
      out[k] = cadd(s0, s2);
      out[k + Q] = (DIR > 0) ? add_pi(s1, s3) : add_mi(s1, s3);
      out[k + 2 * Q] = csub(s0, s2);
      out[k + 3 * Q] = (DIR > 0) ? add_mi(s1, s3) : add_pi(s1, s3);
    });
  }
}

template <int N, int DIR>
__device__ __forceinline__ void fft_reg(cf* v) {
  cf o[N];
  fft_rec<N, DIR, 1>(v, o);
#pragma unroll
  for (int i = 0; i < N; ++i) v[i] = o[i];
}

template <int DIR>
__device__ __forceinline__ void fft16_halfzero(cf* v) {
  cf a[8], b[8], oa[8], ob[8];
  static_for<0, 8>([&](auto ic) {
    constexpr int n = decltype(ic)::value;
    a[n] = v[n];
    b[n] = tw64<4 * n, DIR>(v[n]);
  });
  fft_rec<8, DIR, 1>(a, oa);
  fft_rec<8, DIR, 1>(b, ob);
#pragma unroll
  for (int k = 0; k < 8; ++k) {
    v[2 * k] = oa[k];
    v[2 * k + 1] = ob[k];
  }
}

struct Tw16 { cf w[4]; };

__device__ __forceinline__ cf cis_rev(float rev) {
  float r = rev - __builtin_rintf(rev);
  return cmk(__builtin_amdgcn_cosf(r), __builtin_amdgcn_sinf(r));
}

__device__ __forceinline__ Tw16 make_tw16(int n, int logN) {
  Tw16 b;
#pragma unroll
  for (int t = 0; t < 4; ++t) {
    int e = (n << t) & ((1 << logN) - 1);
    b.w[t] = cis_rev(-(float)e / (float)(1 << logN));
  }
  return b;
}

__device__ __forceinline__ Tw16 load_tw16(const cf* __restrict__ TW, int n) {
  Tw16 b;
#pragma unroll
  for (int t = 0; t < 4; ++t) b.w[t] = TW[(n << t) & 2047];
  return b;
}

template <bool CONJ>
__device__ __forceinline__ void apply_tw16(cf* v, const Tw16& b) {
  cf p[16];
  p[1] = b.w[0];
  p[2] = b.w[1];
  p[4] = b.w[2];
  p[8] = b.w[3];
  asm volatile("" : "+v"(p[1]), "+v"(p[2]), "+v"(p[4]), "+v"(p[8]));
  static_for<3, 16>([&](auto ic) {
    constexpr int k = decltype(ic)::value;
    constexpr int top = (k >= 8) ? 8 : (k >= 4) ? 4 : 2;
    if constexpr (k != top) p[k] = cmul(p[k - top], p[top]);
  });
#pragma unroll
  for (int k = 1; k < 16; ++k) v[k] = CONJ ? cmulc(v[k], p[k]) : cmul(v[k], p[k]);
}

__device__ __forceinline__ void swap16(cf& a, cf& b) {
  asm volatile("s_nop 1" : "+v"(a), "+v"(b));
  auto r0 = __builtin_amdgcn_permlane16_swap(__float_as_uint(a.x), __float_as_uint(b.x), false, false);
  auto r1 = __builtin_amdgcn_permlane16_swap(__float_as_uint(a.y), __float_as_uint(b.y), false, false);
  a.x = __uint_as_float(r0[0]);
  b.x = __uint_as_float(r0[1]);
  a.y = __uint_as_float(r1[0]);
  b.y = __uint_as_float(r1[1]);
}

#define XP 152
#define XBUF (16 * XP)
typedef volatile __attribute__((address_space(3))) cf* ldsp;
__device__ __forceinline__ cf lds_ld(const cf* p) { return *(ldsp)(p); }
__device__ __forceinline__ void lds_st(cf* p, cf a) { *(ldsp)(p) = a; }

struct FCtx {
  Tw16 ta, tb;
  int t, offA, offB, kA, kB;
};
__device__ __forceinline__ FCtx make_fctx(const cf* __restrict__ TW, int t) {
  FCtx f;
  int k0 = t >> 3, c = t & 7;
  f.t = t;
  f.ta = load_tw16(TW, t);
  f.tb = load_tw16(TW, 16 * c);
  int k0m = (16 - k0) & 15;
  int k1m = (k0 != 0) ? (15 - c) : ((c != 0) ? (16 - c) : 8);
  f.offA = k0 * XP + 9 * c;
  f.offB = k0m * XP + 9 * k1m;
  f.kA = k0 + 16 * c;
  f.kB = k0m + 16 * k1m;
  return f;
}

__device__ __forceinline__ void fft2048_fwd_rest(cf* v, cf* buf, const FCtx& f) {
  apply_tw16<false>(v, f.ta);
  __syncthreads();
#pragma unroll
  for (int k0 = 0; k0 < 16; ++k0) lds_st(buf + k0 * XP + f.t, v[k0]);
  __syncthreads();
  cf* rb = buf + (f.t >> 3) * XP + (f.t & 7);
#pragma unroll
  for (int b = 0; b < 16; ++b) v[b] = lds_ld(rb + 8 * b);
  fft_reg<16, -1>(v);
  apply_tw16<false>(v, f.tb);
#pragma unroll
  for (int k1 = 0; k1 < 16; ++k1) lds_st(rb + 9 * k1, v[k1]);
  __syncthreads();
#pragma unroll
  for (int j = 0; j < 8; ++j) v[j] = lds_ld(buf + f.offA + j);
#pragma unroll
  for (int j = 0; j < 8; ++j) v[8 + j] = lds_ld(buf + f.offB + j);
  fft_reg<8, -1>(v);
  fft_reg<8, -1>(v + 8);
}

__device__ __forceinline__ void fft2048_inv_first(cf* v, cf* buf, const FCtx& f) {
  fft_reg<8, 1>(v);
  fft_reg<8, 1>(v + 8);
  __syncthreads();
#pragma unroll
  for (int j = 0; j < 8; ++j) lds_st(buf + f.offA + j, v[j]);
#pragma unroll
  for (int j = 0; j < 8; ++j) lds_st(buf + f.offB + j, v[8 + j]);
  __syncthreads();
  cf* rb = buf + (f.t >> 3) * XP + (f.t & 7);
#pragma unroll
  for (int k1 = 0; k1 < 16; ++k1) v[k1] = lds_ld(rb + 9 * k1);
  apply_tw16<true>(v, f.tb);
  fft_reg<16, 1>(v);
#pragma unroll
  for (int b = 0; b < 16; ++b) lds_st(rb + 8 * b, v[b]);
  __syncthreads();
#pragma unroll
  for (int kk = 0; kk < 16; ++kk) v[kk] = lds_ld(buf + kk * XP + f.t);
  apply_tw16<true>(v, f.ta);
}

#define DLEN 1040
#define DA_CH (4 * DLEN)
#define U_OFF (2 * DA_CH)
#define U_LEN 1152
#define P_OFF (U_OFF + 2 * U_LEN)
#define P_LEN 648
#define OST_OFF 4160
#define KREC 104
using u32x4 = __attribute__((ext_vector_type(4))) unsigned;
using u32x2 = __attribute__((ext_vector_type(2))) unsigned;
using bf16x8 = __attribute__((ext_vector_type(8))) __bf16;

__device__ __forceinline__ cf cexp_scaled(float ar2, float aih, float ail, float sc) {
  float e = __builtin_amdgcn_exp2f(ar2 * sc);
  float h = aih * sc;
  float r = (h - __builtin_rintf(h)) + ail * sc;
  cf w = cis_rev(r);
  return cmk(e * w.x, e * w.y);
}
__device__ __forceinline__ u32 cvtpk(float a, float b) {
  u32 r;
  asm("v_cvt_pk_bf16_f32 %0, %1, %2" : "=v"(r) : "v"(a), "v"(b));
  return r;
}
__device__ __forceinline__ u32x2 split2(cf g) {
  u32x2 o;
  o.x = cvtpk(g.x, g.y);
  cf h = cmk(__uint_as_float(o.x << 16), __uint_as_float(o.x & 0xffff0000u));
  cf r = g - h;
  o.y = cvtpk(r.x, r.y);
  return o;
}

struct KParams { const u16 *Are, *Aim, *Bre, *Bim, *Cre, *Cim, *logdt; };

__device__ __forceinline__ void kgen_block(const KParams& P, u32* __restrict__ Pg, u32* lds_all, int bid, int tid) {
  int wave = tid >> 6, lane = tid & 63;
  int d = bid * 2 + (wave >> 1);
  u32* lds = lds_all + wave * 32 * KREC;
  float dt = bfround(expf(bf2f(P.logdt[d])));
  int idx = d * NST + (wave & 1) * 64 + lane;
  float ar = dt * bf2f(P.Are[idx]), ai = dt * bf2f(P.Aim[idx]);
  cf dB = cmk(dt * bf2f(P.Bre[idx]), dt * bf2f(P.Bim[idx]));
  cf C = cmk(bf2f(P.Cre[idx]), bf2f(P.Cim[idx]));
  cf c0 = cmul(C, dB);
  cf e[12];
  {
    const float L2E = 1.44269504088896341f, I2PH = 0.15915494309189535f, I2PL = 6.4206383e-09f;
    float ar2 = ar * L2E;
    float aih = ai * I2PH;
    float ail = fmaf(ai, I2PL, fmaf(ai, I2PH, -aih));
    e[0] = cexp_scaled(ar2, aih, ail, 1.0f);
    e[4] = cexp_scaled(ar2, aih, ail, 16.0f);
    e[8] = cexp_scaled(ar2, aih, ail, 256.0f);
  }
  e[1] = csqr(e[0]); e[2] = csqr(e[1]); e[3] = csqr(e[2]);
  e[5] = csqr(e[4]); e[6] = csqr(e[5]); e[7] = csqr(e[6]);
  e[9] = csqr(e[8]); e[10] = csqr(e[9]); e[11] = csqr(e[10]);
  typedef volatile __attribute__((address_space(3))) u32x2* lp2;
  float s2048 = fmaf(c0.x, e[11].x, -(c0.y * e[11].y));
  {
    auto dpp = [](float x, auto ctl, auto rm) {
      return __int_as_float(__builtin_amdgcn_update_dpp(0, __float_as_int(x), decltype(ctl)::value, decltype(rm)::value, 0xf, true));
    };
    using std::integral_constant;
    s2048 += dpp(s2048, integral_constant<int, 0x111>{}, integral_constant<int, 0xf>{});
    s2048 += dpp(s2048, integral_constant<int, 0x112>{}, integral_constant<int, 0xf>{});
    s2048 += dpp(s2048, integral_constant<int, 0x114>{}, integral_constant<int, 0xf>{});
    s2048 += dpp(s2048, integral_constant<int, 0x118>{}, integral_constant<int, 0xf>{});
    s2048 += dpp(s2048, integral_constant<int, 0x142>{}, integral_constant<int, 0xa>{});
    s2048 += dpp(s2048, integral_constant<int, 0x143>{}, integral_constant<int, 0xc>{});
  }
  u32x2 Gw[20], Ew[32];
  const int rot = 2 * ((lane >> 2) & 3) + 52 * (lane >> 5);
  u32* rec0 = lds + (lane & 31) * KREC;
  u32* rec = rec0 + rot;
  {
    cf G[20];
    G[0] = cmk(c0.x, -c0.y);
    G[1] = cmulc(G[0], e[0]);
    G[2] = cmulc(G[0], e[1]);
    G[3] = cmulc(G[1], e[1]);
    G[4] = cmulc(G[0], e[2]);
    G[5] = cmulc(G[1], e[2]);
    G[6] = cmulc(G[2], e[2]);
    G[7] = cmulc(G[3], e[2]);
    G[8] = cmulc(G[0], e[8]);
    G[9] = cmulc(G[2], e[8]);
    G[10] = cmulc(G[4], e[8]);
    G[11] = cmulc(G[6], e[8]);
    G[12] = cmulc(G[0], e[9]);
    G[13] = cmulc(G[4], e[9]);
    G[14] = cmulc(G[8], e[9]);
    G[15] = cmulc(G[10], e[9]);
    G[16] = cmulc(G[0], e[10]);
    G[17] = cmulc(G[8], e[10]);
    G[18] = cmulc(G[12], e[10]);
    G[19] = cmulc(G[14], e[10]);
#pragma unroll
    for (int i = 0; i < 20; ++i) Gw[i] = split2(G[i]);
  }
  {
    cf E[32];
    E[0] = cmk(1.f, 0.f);
    E[1] = e[3]; E[2] = e[4]; E[4] = e[5]; E[8] = e[6]; E[16] = e[7];
    static_for<3, 32>([&](auto ic) {
      constexpr int k = decltype(ic)::value;
      constexpr int top = (k >= 16) ? 16 : (k >= 8) ? 8 : (k >= 4) ? 4 : 2;
      if constexpr (k != top) E[k] = cmul(E[k - top], E[top]);
    });
    Ew[0].x = 0x3f80u;
    Ew[0].y = 0u;
#pragma unroll
    for (int i = 1; i < 32; ++i) Ew[i] = split2(E[i]);
  }
  u32x2 Xa[26], Xb[26];
#pragma unroll
  for (int i = 0; i < 26; ++i) {
    Xa[i] = (i < 20) ? Gw[i] : Ew[i - 20];
    Xb[i] = Ew[i + 6];
  }
#pragma unroll
  for (int i = 0; i < 26; i += 2) {
    asm volatile("s_nop 1" : "+v"(Xa[i]), "+v"(Xb[i]), "+v"(Xa[i + 1]), "+v"(Xb[i + 1]));
#pragma unroll
    for (int j = i; j < i + 2; ++j) {
      auto r0 = __builtin_amdgcn_permlane32_swap(Xa[j].x, Xb[j].x, false, false);
      auto r1 = __builtin_amdgcn_permlane32_swap(Xa[j].y, Xb[j].y, false, false);
      Xa[j].x = r0[0]; Xb[j].x = r0[1];
      Xa[j].y = r1[0]; Xb[j].y = r1[1];
    }
  }
  f32x16 acc;
#pragma unroll
  for (int i = 0; i < 16; ++i) acc[i] = 0.f;
  const u32* base = lds + (2 * (lane >> 5)) * KREC;
  int offB[4];
#pragma unroll
  for (int q = 0; q < 4; ++q) {
    int w = 40 + 2 * (lane & 31) + 2 * q;
    offB[q] = (w >= KREC) ? w - KREC : w;
  }
#pragma unroll
  for (int ch = 0; ch < 2; ++ch) {
#pragma unroll
    for (int i = 0; i < 23; ++i) *(lp2)(rec + 2 * i) = ch ? Xb[i] : Xa[i];
#pragma unroll
    for (int i = 23; i < 26; ++i) {
      int w = 2 * i + rot;
      *(lp2)(rec0 + ((w >= KREC) ? w - KREC : w)) = ch ? Xb[i] : Xa[i];
    }
#pragma unroll
    for (int m = 0; m < 8; ++m) {
      const u32* r0 = base + 4 * m * KREC;
      const u32* ra = r0 + 2 * (lane & 31) + 2 * (m & 3);
      const u32* rb = r0 + offB[m & 3];
      u32x2 a0 = *(lp2)(ra), a1 = *(lp2)(ra + KREC), b0 = *(lp2)(rb), b1 = *(lp2)(rb + KREC);
      u32x4 A = {a0.x, a0.y, a1.x, a1.y}, A2 = {a0.y, a0.x, a1.y, a1.x}, B = {b0.x, b0.y, b1.x, b1.y};
      acc = __builtin_amdgcn_mfma_f32_32x32x16_bf16(__builtin_bit_cast(bf16x8, A), __builtin_bit_cast(bf16x8, B), acc, 0, 0, 0);
      acc = __builtin_amdgcn_mfma_f32_32x32x16_bf16(__builtin_bit_cast(bf16x8, A2), __builtin_bit_cast(bf16x8, B), acc, 0, 0, 0);
    }
  }
  float* red = reinterpret_cast<float*>(lds_all + wave * 32 * KREC);
  const float* redp = reinterpret_cast<const float*>(lds_all + (wave ^ 1) * 32 * KREC);
  u32* Pt = Pg + (size_t)d * P_LEN;
  wave_sync();
  const int col = lane & 31, hh = lane >> 5;
  const bool odd = (wave & 1) != 0;
  float kv[6];
  {
    float sa[6] = {acc[0], acc[1], acc[2], acc[3], acc[8], acc[9]};
    float sb[6] = {acc[4], acc[5], acc[6], acc[7], acc[10], acc[11]};
#pragma unroll
    for (int r = 0; r < 6; ++r) {
      asm volatile("" : "+v"(sa[r]), "+v"(sb[r]));
      red[r * 64 + lane] = odd ? sa[r] : sb[r];
      kv[r] = odd ? sb[r] : sa[r];
    }
    if (lane == 63) red[384] = s2048;
  }
  __syncthreads();
  {
    u32 pk[6];
#pragma unroll
    for (int r = 0; r < 6; r += 2) {
      cf gsum = cmk(kv[r] + redp[r * 64 + lane], kv[r + 1] + redp[(r + 1) * 64 + lane]);
      u32x2 hl = split2(gsum);
      pk[r] = __builtin_amdgcn_perm(hl.y, hl.x, 0x05040100u);
      pk[r + 1] = __builtin_amdgcn_perm(hl.y, hl.x, 0x07060302u);
    }
    const int p0 = odd ? (hh ? 384 + 2 * col : 256 + 4 * col) : 4 * hh + 8 * col;
    const int p2 = (odd && hh) ? p0 + 64 : p0 + 2;
    *reinterpret_cast<u32x2*>(Pt + p0) = u32x2{pk[0], pk[1]};
    *reinterpret_cast<u32x2*>(Pt + p2) = u32x2{pk[2], pk[3]};
    if (!hh) {
      const int p4 = (odd ? 576 : 512) + col;
      Pt[p4] = pk[4];
      Pt[p4 + 32] = pk[5];
    }
    if (lane == 63) {
      u32x2 hl = split2(cmk(s2048 + redp[384], 0.f));
      Pt[odd ? 644 : 640] = odd ? 0u : __builtin_amdgcn_perm(hl.y, hl.x, 0x05040100u);
    }
  }
}

#define RP 34
#define RRS (16 * RP)
#define ZP 517
#define TWOFF (16 * RRS)

__device__ __forceinline__ int pos2row(int p) { return 128 * (2 * (p >> 8) + (p & 1)) + ((p & 255) >> 1); }

typedef __attribute__((ext_vector_type(4))) float f32x4;
typedef __amdgpu_buffer_rsrc_t rsrc_t;
__device__ __forceinline__ rsrc_t mkrsrc(const void* p) {
  return __builtin_amdgcn_make_buffer_rsrc(const_cast<void*>(p), 0, 32 << 20, 0x00020000);
}
__device__ __forceinline__ void st_wt(rsrc_t rs, int off, u32x4 q) { __builtin_amdgcn_raw_buffer_store_b128(q, rs, off, 0, 16); }
__device__ __forceinline__ void st_x1(rsrc_t rs, int idx, cf a, cf b) {
  st_wt(rs, idx * 8, u32x4{__float_as_uint(a.x) & ~1u, __float_as_uint(a.y), __float_as_uint(b.x) & ~1u, __float_as_uint(b.y)});
}
typedef unsigned long long u64;
typedef __attribute__((address_space(1))) u64 gu64;
__device__ __forceinline__ void st_tag(cf* p, cf a) {
  u64 w = (u64)(__float_as_uint(a.x) | 1u) | ((u64)__float_as_uint(a.y) << 32);
  __hip_atomic_store((gu64*)p, w, __ATOMIC_RELAXED, __HIP_MEMORY_SCOPE_AGENT);
}
__device__ __forceinline__ cf ld_tag(const cf* p) {
  u64 w = __hip_atomic_load((gu64*)p, __ATOMIC_RELAXED, __HIP_MEMORY_SCOPE_AGENT);
  return cmk(__uint_as_float((u32)w), __uint_as_float((u32)(w >> 32)));
}
#define TSR(k)
__device__ __forceinline__ void brow_block(const u32* xw, rsrc_t X1T, cf* sm, int bid, int tid) {
  cf* tw = sm + 8 * RRS;
  int s0 = bid * 8;
  {
    tw[tid] = cis_rev(-(float)tid / 1024.0f);
    if (tid == 0) tw[256] = cmk(0.f, -1.f);
  }
  int row = tid >> 5, t = tid & 31;
  cf v[16];
#pragma unroll
  for (int a = 0; a < 16; ++a) v[a] = cmk(bf2f(xw[a] & 0xffffu), bf2f(xw[a] >> 16));
  Tw16 tb = make_tw16(t, 9);
  fft_reg<16, -1>(v);
  apply_tw16<false>(v, tb);
  cf* rb = sm + row * RRS;
  {
    int pos = (t & 1) * 16 + (t >> 1);
#pragma unroll
    for (int k0 = 0; k0 < 16; ++k0) rb[k0 * RP + pos] = v[k0];
  }
  wave_sync();
  int k0 = t & 15, h = t >> 4;
  {
    const float4* p = reinterpret_cast<const float4*>(rb + k0 * RP + h * 16);
#pragma unroll
    for (int m = 0; m < 8; ++m) {
      float4 q = p[m];
      v[2 * m] = cmk(q.x, q.y);
      v[2 * m + 1] = cmk(q.z, q.w);
    }
  }
  fft_reg<16, -1>(v);
  bool hi = h != 0;
  static_for<0, 8>([&](auto ic) {
    constexpr int j = decltype(ic)::value;
    cf a = v[j], b = v[j + 8];
    swap16(a, b);
    cf br = cmk(hi ? b.y : b.x, hi ? -b.x : b.y);
    br = tw64<2 * j, -1>(br);
    v[j] = cadd(a, br);
    v[j + 8] = csub(a, br);
  });
  __syncthreads();
  cf* zs = sm + row * ZP;
#pragma unroll
  for (int r = 0; r < 16; ++r) zs[k0 + 16 * ((r & 7) + 8 * h + 16 * (r >> 3))] = v[r];
  __syncthreads();
  int rp = tid & 3, kq = tid >> 2;
#pragma unroll
  for (int i = 0; i < 5; ++i) {
    int k = kq + 64 * i;
    if (k <= 256) {
      cf lo[2], up[2];
#pragma unroll
      for (int hh = 0; hh < 2; ++hh) {
        const cf* zr = sm + (2 * rp + hh) * ZP;
        cf a = zr[k];
        cf b = zr[(512 - k) & 511];
        cf ev = cmk(0.5f * (a.x + b.x), 0.5f * (a.y - b.y));
        cf od = cmk(0.5f * (a.y + b.y), -0.5f * (a.x - b.x));
        cf wo = cmul(od, tw[k]);
        lo[hh] = (k == 0) ? conj_add_pi(a, a) : cadd(ev, wo);
        up[hh] = cmk(ev.x - wo.x, -(ev.y - wo.y));
      }
      st_x1(X1T, k * SEQ + s0 + 2 * rp, lo[0], lo[1]);
      if (k != 0 && k != 256) st_x1(X1T, (512 - k) * SEQ + s0 + 2 * rp, up[0], up[1]);
    }
  }
}

__device__ __forceinline__ void browinv_block(const cf* X2, const Tw16& tb, const cf* wk, u16* __restrict__ out, cf* sm,
                                              int bid, int tid) {
  int s0 = bid * 8;
  {
    const int r8 = tid & 7, kq = tid >> 3;
    cf a[9], b[9];
    const cf* pa = X2 + (size_t)kq * SEQ + s0 + r8;
    const cf* pb = X2 + (size_t)(512 - kq) * SEQ + s0 + r8;
    const cf* pb0 = (kq == 0) ? pa : pb;
#pragma unroll
    for (int i = 0; i < 8; ++i) {
      a[i] = ld_tag(pa + (size_t)32 * i * SEQ);
      b[i] = ld_tag((i ? pb : pb0) - (size_t)32 * i * SEQ);
    }
    a[8] = ld_tag(X2 + (size_t)256 * SEQ + s0 + r8);
    for (unsigned spins = 0; spins < (1u << 14); ++spins) {
      u32 ok = __float_as_uint(a[8].x);
#pragma unroll
      for (int i = 0; i < 8; ++i) ok &= __float_as_uint(a[i].x) & __float_as_uint(b[i].x);
      if (__all(ok & 1u)) break;
#pragma unroll
      for (int i = 0; i < 8; ++i) {
        if (!(__float_as_uint(a[i].x) & 1u)) a[i] = ld_tag(pa + (size_t)32 * i * SEQ);
        if (!(__float_as_uint(b[i].x) & 1u)) b[i] = ld_tag((i ? pb : pb0) - (size_t)32 * i * SEQ);
      }
      if (!(__float_as_uint(a[8].x) & 1u)) a[8] = ld_tag(X2 + (size_t)256 * SEQ + s0 + r8);
    }
    TSR(2);
    b[8] = a[8];
    cf* zr = sm + r8 * ZP;
#pragma unroll
    for (int i = 0; i < 9; ++i) {
      int k = kq + 32 * i;
      if (i < 8 || kq == 0) {
        cf aa = a[i], bb = b[i];
        if (i == 0 && k == 0) {
          zr[0] = conj_add_pi(aa, aa);
        } else {
          cf ev = addc(aa, bb);
          cf od = cmulc(subc(aa, bb), wk[i]);
          zr[k] = add_pi(ev, od);
          if (i < 8) zr[512 - k] = conj_add_pi(ev, od);
        }
      }
    }
  }
  __syncthreads();
  int row = tid >> 5, t = tid & 31;
  int k0 = t & 15, h = t >> 4;
  cf v[16];
  {
    const cf* zs = sm + row * ZP;
#pragma unroll
    for (int r = 0; r < 16; ++r) v[r] = zs[k0 + 16 * ((r & 7) + 8 * h + 16 * (r >> 3))];
  }
  __syncthreads();
  bool hi = h != 0;
  static_for<0, 8>([&](auto ic) {
    constexpr int j = decltype(ic)::value;
    cf a = cadd(v[j], v[j + 8]);
    cf d = csub(v[j], v[j + 8]);
    cf dr = cmk(hi ? -d.y : d.x, hi ? d.x : d.y);
    dr = tw64<2 * j, 1>(dr);
    swap16(a, dr);
    v[j] = a;
    v[j + 8] = dr;
  });
  fft_reg<16, 1>(v);
  cf* rb = sm + row * RRS;
  {
    float4* p = reinterpret_cast<float4*>(rb + k0 * RP + h * 16);
#pragma unroll
    for (int m = 0; m < 8; ++m) p[m] = make_float4(v[2 * m].x, v[2 * m].y, v[2 * m + 1].x, v[2 * m + 1].y);
  }
  wave_sync();
  {
    int pos = (t & 1) * 16 + (t >> 1);
#pragma unroll
    for (int kk = 0; kk < 16; ++kk) v[kk] = rb[kk * RP + pos];
  }
  apply_tw16<true>(v, tb);
  fft_reg<16, 1>(v);
  u32* orow = reinterpret_cast<u32*>(out + (size_t)(s0 + row) * (2 * DCH) + DCH);
#pragma unroll
  for (int a = 0; a < 16; ++a) orow[32 * a + t] = cvtpk(v[a].x, v[a].y);
}

__device__ __forceinline__ cf csel(bool c, cf a, cf b) { return cmk(c ? a.x : b.x, c ? a.y : b.y); }
__device__ __forceinline__ cf unpk(u32 w, float sc) { return cmk(bf2f(w & 0xffffu) * sc, bf2f(w >> 16) * sc); }

#define TSB(k)
template <bool SPECIAL>
__device__ __forceinline__ void bcol_block(cf* X1T, const u32* __restrict__ WT,
                                           const cf* __restrict__ TW, cf* buf, int col, int t, u32* flag) {
  cf v[16];
  cf* xc = X1T + (size_t)col * SEQ;
#pragma unroll
  for (int a = 0; a < 8; ++a) {
    float4 q = *reinterpret_cast<const float4*>(xc + 256 * a + 2 * t);
    v[2 * a] = cmk(q.x, q.y);
    v[2 * a + 1] = cmk(q.z, q.w);
  }
  FCtx f = make_fctx(TW, t);
  const float sc = 1.0f / (2048.0f * 1024.0f);
  const u32* wc = WT + (size_t)col * SEQ;
  u32 wq[16], wn[16];
#pragma unroll
  for (int j = 0; j < 8; ++j) {
    wq[j] = wc[f.kA + 256 * j];
    wq[8 + j] = wc[f.kB + 256 * j];
  }
  if (SPECIAL) {
    const u32* w1 = WT + (size_t)512 * SEQ;
#pragma unroll
    for (int j = 0; j < 8; ++j) {
      wn[j] = w1[f.kA + 256 * j];
      wn[8 + j] = w1[f.kB + 256 * j];
    }
  }
  fft_reg<16, -1>(v);
  TSB(5);
  fft2048_fwd_rest(v, buf, f);
  TSB(6);
  if (!SPECIAL || col != 0) {
#pragma unroll
    for (int r = 0; r < 16; ++r) v[r] = cmul(v[r], unpk(wq[r], sc));
  } else {
    const bool sp = (t == 0);
    cf oP[8], oQ[8];
    auto pair = [&](cf a, cf b, u32 pa, u32 pb, u32 qa, u32 qb, cf& hp, cf& hq) {
      cf A = cmk(0.5f * (a.x + b.x), 0.5f * (a.y - b.y));
      cf B = cmk(0.5f * (a.y + b.y), -0.5f * (a.x - b.x));
      cf p = unpk(pa, 1.f), q = unpk(pb, 1.f);
      cf wh0 = cmk(0.5f * sc * (p.x + q.x), 0.5f * sc * (p.y - q.y));
      p = unpk(qa, 1.f); q = unpk(qb, 1.f);
      cf wh1 = cmk(0.5f * sc * (p.x + q.x), 0.5f * sc * (p.y - q.y));
      cf h0 = cmul(A, wh0), h1 = cmul(B, wh1);
      hp = cmk(h0.x - h1.y, h0.y + h1.x);
      hq = cmk(h0.x + h1.y, -h0.y + h1.x);
    };
    static_for<0, 8>([&](auto ic) {
      constexpr int j = decltype(ic)::value;
      constexpr int ia = j, ib = 15 - j;
      constexpr int sa = (j < 4) ? j : 8 + j;
      constexpr int sb = (j == 0) ? 0 : (j < 4) ? 8 - j : 15 - j;
      pair(csel(sp, v[sa], v[ia]), csel(sp, v[sb], v[ib]), sp ? wq[sa] : wq[ia], sp ? wq[sb] : wq[ib],
           sp ? wn[sa] : wn[ia], sp ? wn[sb] : wn[ib], oP[j], oQ[j]);
    });
    cf s4, s4b;
    pair(v[4], v[4], wq[4], wq[4], wn[4], wn[4], s4, s4b);
    v[0] = oP[0];
    v[1] = oP[1];
    v[2] = oP[2];
    v[3] = oP[3];
    v[4] = csel(sp, s4, oP[4]);
    v[5] = csel(sp, oQ[3], oP[5]);
    v[6] = csel(sp, oQ[2], oP[6]);
    v[7] = csel(sp, oQ[1], oP[7]);
    v[8] = oQ[7];
    v[9] = oQ[6];
    v[10] = oQ[5];
    v[11] = oQ[4];
    v[12] = csel(sp, oP[4], oQ[3]);
    v[13] = csel(sp, oP[5], oQ[2]);
    v[14] = csel(sp, oP[6], oQ[1]);
    v[15] = csel(sp, oP[7], oQ[0]);
  }
  if (threadIdx.x == 0) __hip_atomic_store(flag, 1u, __ATOMIC_RELAXED, __HIP_MEMORY_SCOPE_AGENT);
  fft2048_inv_first(v, buf, f);
  fft_reg<16, 1>(v);
#pragma unroll
  for (int a = 0; a < 16; ++a) st_tag(xc + 128 * a + t, v[a]);
}

using f32x4v = __attribute__((ext_vector_type(4))) float;
typedef u32x4 u32x4a4 __attribute__((aligned(4)));

constexpr int rne_loc(int j, int s) { return s == 0 ? j : (j + (1 << (s - 1)) - 1 + ((j >> s) & 1)) >> s; }

__device__ __forceinline__ void kexpand(u32* DA, const u32* Pt, int t) {
  const int m = 127 - t;
  const int s = (m < 16) ? 0 : (m < 32) ? 1 : (m < 64) ? 2 : 3;
  const int base = 128 * s + ((16 * m) >> s);
  const int s8 = 8 * s;
  u32 pv[17];
  static_for<0, 16>([&](auto ic) {
    constexpr int j = decltype(ic)::value;
    constexpr u32 tab = rne_loc(j, 0) | (rne_loc(j, 1) << 8) | (rne_loc(j, 2) << 16) | (rne_loc(j, 3) << 24);
    pv[j + 1] = Pt[base + ((tab >> s8) & 0xffu)];
  });
  {
    const int m1 = m - 1;
    const int s1 = (m1 < 16) ? 0 : (m1 < 32) ? 1 : (m1 < 64) ? 2 : 3;
    constexpr u32 tab = rne_loc(15, 0) | (rne_loc(15, 1) << 8) | (rne_loc(15, 2) << 16) | (rne_loc(15, 3) << 24);
    int ix = 128 * s1 + ((16 * m1) >> s1) + ((tab >> (8 * s1)) & 0xffu);
    pv[0] = Pt[m == 0 ? 644 : ix];
  }
  u32 he[8], ho[8], le[8], lo[8];
#pragma unroll
  for (int ii = 0; ii < 8; ++ii) {
    u32 a = pv[16 - 2 * ii], b = pv[15 - 2 * ii], c = pv[14 - 2 * ii];
    ho[ii] = __builtin_amdgcn_perm(b, a, 0x05040100u);
    lo[ii] = __builtin_amdgcn_perm(b, a, 0x07060302u);
    he[ii] = __builtin_amdgcn_perm(c, b, 0x05040100u);
    le[ii] = __builtin_amdgcn_perm(c, b, 0x07060302u);
  }
  u32* q = DA + 8 * t;
#pragma unroll
  for (int h = 0; h < 2; ++h) {
    *reinterpret_cast<u32x4*>(q + 4 * h) = u32x4{he[4 * h], he[4 * h + 1], he[4 * h + 2], he[4 * h + 3]};
    *reinterpret_cast<u32x4*>(q + DLEN + 4 * h) = u32x4{ho[4 * h], ho[4 * h + 1], ho[4 * h + 2], ho[4 * h + 3]};
    *reinterpret_cast<u32x4*>(q + 2 * DLEN + 4 * h) = u32x4{le[4 * h], le[4 * h + 1], le[4 * h + 2], le[4 * h + 3]};
    *reinterpret_cast<u32x4*>(q + 3 * DLEN + 4 * h) = u32x4{lo[4 * h], lo[4 * h + 1], lo[4 * h + 2], lo[4 * h + 3]};
  }
  if (t < 8) *reinterpret_cast<u32x4*>(DA + (t >> 1) * DLEN + 1024 + 4 * (t & 1)) = u32x4{0u, 0u, 0u, 0u};
}

typedef const __attribute__((address_space(3))) u32* l3p;
typedef const __attribute__((address_space(3))) u32x4a4* l3a;
typedef const __attribute__((address_space(3))) u32x4* l3q;
constexpr int SPORD[8] = {0, 7, 1, 6, 2, 5, 3, 4};
__device__ __forceinline__ void conv_mfma(const u32* DA, const u32* U, int w2, int lane, f32x4v* acc) {
  const int e = lane & 15, g = lane >> 4;
  l3p da = (l3p)(DA + (e & 1) * DLEN + 4 * g - (e >> 1) + 7 - 64 * w2 + 1008 - 128 - 48);
  l3p ub = (l3p)(U + 120 + 8 * e + 4 * g - 64 * w2 - 48);
  l3p bh[4], bl[4];
#pragma unroll
  for (int q = 0; q < 4; ++q) {
    bh[q] = da - 256 * q;
    bl[q] = bh[q] + 2 * DLEN;
    asm volatile("" : "+v"(bh[q]), "+v"(bl[q]));
  }
#pragma unroll
  for (int i = 0; i < 8; ++i) acc[i] = f32x4v{0.f, 0.f, 0.f, 0.f};
  u32x4 B[2][8], Ah[3], Al[3];
  auto ldA = [&](auto ic) {
    constexpr int n = decltype(ic)::value;
    constexpr int rr = n >> 3, sp = SPORD[n & 7];
    constexpr int off = ((sp & 1) ? 0 : 128) + 48 - 16 * rr;
    Ah[n % 3] = *(l3a)(bh[sp >> 1] + off);
    Al[n % 3] = *(l3a)(bl[sp >> 1] + off);
  };
  ldA(std::integral_constant<int, 0>{});
#pragma unroll
  for (int k = 0; k < 8; ++k) B[0][k] = *(l3q)(ub + 128 * k + 48);
  ldA(std::integral_constant<int, 1>{});
  static_for<0, 32>([&](auto ic) {
    constexpr int n = decltype(ic)::value;
    constexpr int rr = n >> 3, sp = SPORD[n & 7];
    if constexpr (n + 2 < 32) ldA(std::integral_constant<int, n + 2>{});
    if constexpr (rr < 3) B[(rr + 1) & 1][n & 7] = *(l3q)(ub + 128 * (n & 7) + 48 - 16 * (rr + 1));
    __builtin_amdgcn_sched_barrier(0);
#pragma unroll
    for (int i = sp; i < 8; ++i)
      acc[i] = __builtin_amdgcn_mfma_f32_16x16x32_bf16(__builtin_bit_cast(bf16x8, Ah[n % 3]), __builtin_bit_cast(bf16x8, B[rr & 1][i - sp]), acc[i], 0, 0, 0);
#pragma unroll
    for (int i = sp; i < 8; ++i)
      acc[i] = __builtin_amdgcn_mfma_f32_16x16x32_bf16(__builtin_bit_cast(bf16x8, Al[n % 3]), __builtin_bit_cast(bf16x8, B[rr & 1][i - sp]), acc[i], 0, 0, 0);
    __builtin_amdgcn_sched_barrier(0);
  });
}

#define TS(k)
#define NSH 16
#define CSTR 64
typedef __attribute__((address_space(1))) unsigned gu32;

__device__ __forceinline__ u32 ld_u32_a2(const u16* p) {
  u32 w;
  __builtin_memcpy(&w, p, 4);
  return w;
}

#define FLG 4096
#define TSOFF 768
__global__ __launch_bounds__(256, 3) void k1(KParams P, u32* Pg, const u16* x, cf* X1T, const u16* wr,
                                            const u16* wi, u32* WT, cf* TW, const u16* u, u16* uT, u32* cnt) {
  __shared__ __attribute__((aligned(16))) u32 smw[4 * 32 * KREC];
  cf* sm = reinterpret_cast<cf*>(smw);
  int bb = blockIdx.x, tid = threadIdx.x;
  int b = bb / 3, r3 = bb % 3;
  TS(0);
  if (r3 < 2) {
    kgen_block(P, Pg, smw, b * 2 + r3, tid);
    TS(1);
  } else {
    u32 xw[16];
    {
      const u32* xr = reinterpret_cast<const u32*>(x + (size_t)pos2row(8 * b + (tid >> 5)) * DCH);
#pragma unroll
      for (int a = 0; a < 16; ++a) xw[a] = xr[32 * a + (tid & 31)];
    }
    uint4 uq[2][2];
    const int ur_ = tid >> 3, uc_ = tid & 7;
#pragma unroll
    for (int it = 0; it < 2; ++it) {
      int tl = b * 2 + it;
      int l0 = (tl >> 4) * 64, d0 = (tl & 15) * 64;
#pragma unroll
      for (int h = 0; h < 2; ++h)
        uq[it][h] = *reinterpret_cast<const uint4*>(u + (size_t)(l0 + ur_ + 32 * h) * DCH + d0 + 8 * uc_);
    }
    const int ws0 = (b >> 1) * 16, kb = (b & 1) * 257, nk = 257 - (b & 1);
    u32 wa[9], wb[9];
#pragma unroll
    for (int i = 0; i < 9; ++i) {
      int e = min(tid + 256 * i, 16 * 129 - 1);
      int r = e / 129, c = e - r * 129;
      size_t o = (size_t)(ws0 + r) * DF + kb + min(2 * c, nk - 2);
      wa[i] = ld_u32_a2(wr + o);
      wb[i] = ld_u32_a2(wi + o);
    }
    if (b < 8) {
      int j = b * 256 + tid;
      float s, c;
      sincospif(-(float)j / 1024.0f, &s, &c);
      TW[j] = cmk(c, s);
    }
    if (b == 8 && tid < NSH) cnt[tid * CSTR] = 0u;
    if (b == 9) cnt[FLG + 16 * tid] = 0u;
    const rsrc_t rws = mkrsrc(X1T);
    brow_block(xw, rws, sm, b, tid);
    TS(2);
    __syncthreads();
    u16* ut = reinterpret_cast<u16*>(smw);
    u32* wt = smw + 4608;
#pragma unroll
    for (int it = 0; it < 2; ++it) {
      u16* tl_ = ut + it * 64 * 72;
#pragma unroll
      for (int h = 0; h < 2; ++h) {
        u32 w[4] = {uq[it][h].x, uq[it][h].y, uq[it][h].z, uq[it][h].w};
#pragma unroll
        for (int j = 0; j < 4; ++j) {
          tl_[(8 * uc_ + 2 * j) * 72 + ur_ + 32 * h] = (u16)(w[j] & 0xffffu);
          tl_[(8 * uc_ + 2 * j + 1) * 72 + ur_ + 32 * h] = (u16)(w[j] >> 16);
        }
      }
    }
#pragma unroll
    for (int i = 0; i < 9; ++i) {
      int e = min(tid + 256 * i, 16 * 129 - 1);
      int r = e / 129, c = e - r * 129;
      int kk = min(2 * c, nk - 2);
      wt[kk * 17 + r] = (wa[i] & 0xffffu) | (wb[i] << 16);
      wt[(kk + 1) * 17 + r] = (wa[i] >> 16) | (wb[i] & 0xffff0000u);
    }
    __syncthreads();
#pragma unroll
    for (int it = 0; it < 2; ++it) {
      int tl = b * 2 + it;
      int l0 = (tl >> 4) * 64, d0 = (tl & 15) * 64;
      const u16* tl_ = ut + it * 64 * 72;
#pragma unroll
      for (int h = 0; h < 2; ++h) {
        uint4 o = *reinterpret_cast<const uint4*>(tl_ + (ur_ + 32 * h) * 72 + 8 * uc_);
        u32x4 oo = {o.x, o.y, o.z, o.w};
        st_wt(rws, (24 << 20) + ((d0 + ur_ + 32 * h) * SEQ + l0 + 8 * uc_) * 2, oo);
      }
    }
#pragma unroll
    for (int i = 0; i < 5; ++i) {
      int kk = (tid >> 2) + 64 * i, q = tid & 3;
      if (kk < nk) {
        const u32* p = wt + kk * 17 + 4 * q;
        u32x4 oo = {p[0], p[1], p[2], p[3]};
        st_wt(rws, (8 << 20) + ((kb + kk) * SEQ + ws0 + 4 * q) * 4, oo);
      }
    }
    TS(3);
  }
  TS(4);
}
#undef TSOFF
#define TSOFF 0

__device__ __forceinline__ void handoff(gu32* gc, int b, int tid) {
  asm volatile("s_waitcnt vmcnt(0)" ::: "memory");
  __syncthreads();
  if (tid < 64) {
    if (tid == 0) __hip_atomic_fetch_add(gc + (b & (NSH - 1)) * CSTR, 1u, __ATOMIC_RELAXED, __HIP_MEMORY_SCOPE_AGENT);
    for (unsigned spins = 0; spins < (1u << 16); ++spins) {
      unsigned c = (tid < NSH) ? __hip_atomic_load(gc + tid * CSTR, __ATOMIC_RELAXED, __HIP_MEMORY_SCOPE_AGENT) : (256u / NSH);
      if (__all(c == 256u / NSH)) break;
      __builtin_amdgcn_s_sleep(1);
    }
    __builtin_amdgcn_fence(__ATOMIC_ACQUIRE, "wavefront");
  }
  __syncthreads();
}

__global__ __launch_bounds__(256, 3) void k2(const u32* Pg, cf* X1T, const u32* WT, const u32* uT,
                                            const cf* TW, const u16* Dskip, u16* out, u32* cnt) {
  __shared__ __attribute__((aligned(16))) u32 smw[P_OFF + 2 * P_LEN];
  cf* sm = reinterpret_cast<cf*>(smw);
  int b = blockIdx.x, tid = threadIdx.x;
  int g = tid >> 7, t = tid & 127;
  TS(0);
  if (b < 256) {
    __builtin_amdgcn_s_setprio(3);
    if (b == 0) bcol_block<true>(X1T, WT, TW, sm + g * XBUF, 2 * b + g, t, cnt + FLG + 16 * b);
    else bcol_block<false>(X1T, WT, TW, sm + g * XBUF, 2 * b + g, t, cnt + FLG + 16 * b);
    Tw16 tb = load_tw16(TW, 4 * (tid & 31));
    cf wk[9];
#pragma unroll
    for (int i = 0; i < 9; ++i) wk[i] = TW[(2 * ((tid >> 3) + 32 * i)) & 2047];
    TS(1);
    __syncthreads();
    browinv_block(X1T, tb, wk, out, sm, b, tid);
    TS(3);
    TS(4);
  } else {
    int i = b - 256;
    int p = (i & 7) * 64 + (i >> 3);
    const int d = 2 * p + g, wave = tid >> 6, lane = tid & 63, w2 = wave & 1;
    u32x4 uq[2], pq[2];
    {
      const u32x4* pr = reinterpret_cast<const u32x4*>(Pg + (size_t)d * P_LEN);
      pq[0] = pr[t];
      pq[1] = pr[128 + min(t, P_LEN / 4 - 129)];
      const u32x4* ur = reinterpret_cast<const u32x4*>(uT + (size_t)d * (SEQ / 2) + 8 * t);
      uq[0] = ur[0];
      uq[1] = ur[1];
    }
    const float D = bf2f(Dskip[d]);
    u32* DA = smw + g * DA_CH;
    u32* U = smw + U_OFF + g * U_LEN;
    u32* Pt = smw + P_OFF + g * P_LEN;
    *reinterpret_cast<u32x4*>(Pt + 4 * t) = pq[0];
    *reinterpret_cast<u32x4*>(Pt + 4 * (128 + min(t, P_LEN / 4 - 129))) = pq[1];
    if (t < 32) *reinterpret_cast<u32x4*>(U + 4 * t) = u32x4{0u, 0u, 0u, 0u};
    __syncthreads();
    kexpand(DA, Pt, t);
    *reinterpret_cast<u32x4*>(U + 128 + 8 * t) = uq[0];
    *reinterpret_cast<u32x4*>(U + 128 + 8 * t + 4) = uq[1];
    TS(1);
#define GATEMIN 256
    if (tid == 0 && b >= GATEMIN) {
      gu32* fl = (gu32*)cnt + FLG + 16 * (b & 255);
      for (unsigned spins = 0; spins < 512u; ++spins) {
        if (__hip_atomic_load(fl, __ATOMIC_RELAXED, __HIP_MEMORY_SCOPE_AGENT) != 0u) break;
        __builtin_amdgcn_s_sleep(2);
      }
    }
    __syncthreads();
    TS(5);
    f32x4v acc[8];
    conv_mfma(DA, U, w2, lane, acc);
    TS(6);
    __syncthreads();
    {
      f32x4v* ex = reinterpret_cast<f32x4v*>(smw) + wave * 256;
      const f32x4v* exp_ = reinterpret_cast<const f32x4v*>(smw) + (wave ^ 1) * 256;
      f32x4v kp[4];
#pragma unroll
      for (int i = 0; i < 4; ++i) {
        f32x4v a0 = acc[i], a1 = acc[4 + i], gv;
#pragma unroll
        for (int j = 0; j < 4; ++j) {
          gv[j] = w2 ? a0[j] : a1[j];
          kp[i][j] = w2 ? a1[j] : a0[j];
        }
        ex[i * 64 + lane] = gv;
      }
      __syncthreads();
      const int q0 = 512 * w2 + 8 * (lane & 15) + 2 * (lane >> 4);
      u32* osc = smw + OST_OFF + g * 1024;
#pragma unroll
      for (int i = 0; i < 4; ++i) {
        f32x4v y = kp[i] + exp_[i * 64 + lane];
        u32x2 uw = *reinterpret_cast<const u32x2*>(U + 128 + q0 + 128 * i);
        u32x2 o;
        {
          u32 yb = cvtpk(y[0], y[1]);
          u32 sb = cvtpk(bf2f(uw.x & 0xffffu) * D, bf2f(uw.x >> 16) * D);
          o.x = cvtpk(bf2f(yb & 0xffffu) + bf2f(sb & 0xffffu), bf2f(yb >> 16) + bf2f(sb >> 16));
          yb = cvtpk(y[2], y[3]);
          sb = cvtpk(bf2f(uw.y & 0xffffu) * D, bf2f(uw.y >> 16) * D);
          o.y = cvtpk(bf2f(yb & 0xffffu) + bf2f(sb & 0xffffu), bf2f(yb >> 16) + bf2f(sb >> 16));
        }
        *reinterpret_cast<u32x2*>(osc + q0 + 128 * i) = o;
      }
    }
    __syncthreads();
    TS(2);
    const u32* ost = smw + OST_OFF;
    u32* op = reinterpret_cast<u32*>(out + 2 * p);
#pragma unroll
    for (int j = 0; j < 4; ++j) {
      int q = tid + 256 * j;
      u32 w0 = ost[q], w1 = ost[1024 + q];
      op[(size_t)(2 * q) * DCH] = (w0 & 0xffffu) | (w1 << 16);
      op[(size_t)(2 * q + 1) * DCH] = (w0 >> 16) | (w1 & 0xffff0000u);
    }
    TS(3);
    TS(4);
  }
}

extern "C" void kernel_launch(void* const* d_in, const int* in_sizes, int n_in, void* d_out, int out_size,
                              void* d_ws, size_t ws_size, hipStream_t stream) {
  const u16* u = (const u16*)d_in[0];
  KParams P;
  P.Are = (const u16*)d_in[1];
  P.Aim = (const u16*)d_in[2];
  P.Bre = (const u16*)d_in[3];
  P.Bim = (const u16*)d_in[4];
  P.Cre = (const u16*)d_in[5];
  P.Cim = (const u16*)d_in[6];
  P.logdt = (const u16*)d_in[7];
  const u16* Dskip = (const u16*)d_in[8];
  const u16* x = (const u16*)d_in[9];
  const u16* wr = (const u16*)d_in[10];
  const u16* wi = (const u16*)d_in[11];
  u16* out = (u16*)d_out;
  char* ws = (char*)d_ws;
  cf* X1T = (cf*)ws;
  u32* WT = (u32*)(ws + (size_t)8 * 1024 * 1024);
  cf* X2T = (cf*)(ws + (size_t)16 * 1024 * 1024);
  u16* uT = (u16*)(ws + (size_t)24 * 1024 * 1024);
  cf* TW = (cf*)(ws + (size_t)28 * 1024 * 1024);
  u32* cnt = (u32*)(ws + (size_t)29 * 1024 * 1024);
  u32* Pg = (u32*)(ws + (size_t)30 * 1024 * 1024);

  k1<<<768, 256, 0, stream>>>(P, Pg, x, X1T, wr, wi, WT, TW, u, uT, cnt);
  k2<<<768, 256, 0, stream>>>(Pg, X1T, WT, (const u32*)uT, TW, Dskip, out, cnt);
}
```

```cpp
#define NTW 0
#define TMODE 1
#define KFIRST 1
#define NTX 1
#define NTU 1
#define NTP 1
#define NTW2 0
#define WMAP 1
#define AUX_X 16
#define AUX_U 0
#define AUX_K 0
#define NTO 0
#define GATEPOS 5
#define XBAR 0
#define UPAT 1
#define WEARLY 0
#define OMAP 0
#define APRIO0 0
#define WORD 1
#define WGRP 2
#define GATEP1 GATEPOS
#define OSLP 1
#define OFS 4
#define CPRIO1 1
#define CPRIO2 1
#define CPOS 1
#define PSLEEP 16
#define GSLP 2
#define KPRIO 1
#define APRIO1 1
#include <hip/hip_runtime.h>
#include <hip/hip_bf16.h>
#include <type_traits>

#define SEQ 2048
#define DCH 1024
#define NST 128
#define DF 513

typedef __attribute__((ext_vector_type(2))) float cf;
typedef unsigned int u32;
typedef unsigned short u16;
using f32x16 = __attribute__((ext_vector_type(16))) float;

#define TSX(k)
#define TSINIT()
#define TSEND(base, slot)
template <int I, int N, class F>
__device__ __forceinline__ void static_for(F&& f) {
  if constexpr (I < N) {
    f(std::integral_constant<int, I>{});
    static_for<I + 1, N>(f);
  }
}

__device__ __forceinline__ cf cmk(float x, float y) { cf r; r.x = x; r.y = y; return r; }
__device__ __forceinline__ cf cmul(cf a, cf b) {
  cf t, r;
  asm("v_pk_mul_f32 %0, %1, %2 op_sel:[0,0] op_sel_hi:[0,1]" : "=v"(t) : "v"(a), "v"(b));
  asm("v_pk_fma_f32 %0, %1, %2, %3 op_sel:[1,1,0] op_sel_hi:[1,0,1] neg_lo:[0,1,0]" : "=v"(r) : "v"(a), "v"(b), "v"(t));
  return r;
}
__device__ __forceinline__ float bf2f(u32 h) { return __uint_as_float(h << 16); }
__device__ __forceinline__ u32 f2bf(float f) {
  __hip_bfloat16 b = __float2bfloat16(f);
  return (u32)(*reinterpret_cast<u16*>(&b));
}
__device__ __forceinline__ float bfround(float f) { return bf2f(f2bf(f)); }

__device__ __forceinline__ void wave_sync() {
  __builtin_amdgcn_fence(__ATOMIC_SEQ_CST, "wavefront");
  __builtin_amdgcn_wave_barrier();
  __builtin_amdgcn_fence(__ATOMIC_SEQ_CST, "wavefront");
}

__device__ __forceinline__ cf cis_rev(float rev) {
  float r = rev - __builtin_rintf(rev);
  cf w = cmk(__builtin_amdgcn_cosf(r), __builtin_amdgcn_sinf(r));
  asm("s_nop 1" : "+v"(w));
  return w;
}

typedef __attribute__((address_space(3))) unsigned l3u;
__device__ __forceinline__ void garrive(u32* c) {
  asm volatile("" ::: "memory");
  if ((threadIdx.x & 63) == 0) __hip_atomic_fetch_add((l3u*)c, 1u, __ATOMIC_RELAXED, __HIP_MEMORY_SCOPE_WORKGROUP);
  asm volatile("" ::: "memory");
}
template <int SLEEP>
__device__ __forceinline__ void gwait(u32* c, u32 target) {
  for (unsigned spins = 0; spins < (1u << 20); ++spins) {
    if ((u32)__builtin_amdgcn_readfirstlane((int)*(volatile l3u*)c) >= target) break;
    if (SLEEP) __builtin_amdgcn_s_sleep(SLEEP);
  }
  asm volatile("" ::: "memory");
}

__device__ __forceinline__ u32 wave_umax(u32 x) {
  auto dmax = [](u32 v, auto ctl, auto rm) {
    return max(v, (u32)__builtin_amdgcn_update_dpp(0, (int)v, decltype(ctl)::value, decltype(rm)::value, 0xf, true));
  };
  using std::integral_constant;
  x = dmax(x, integral_constant<int, 0x111>{}, integral_constant<int, 0xf>{});
  x = dmax(x, integral_constant<int, 0x112>{}, integral_constant<int, 0xf>{});
  x = dmax(x, integral_constant<int, 0x114>{}, integral_constant<int, 0xf>{});
  x = dmax(x, integral_constant<int, 0x118>{}, integral_constant<int, 0xf>{});
  x = dmax(x, integral_constant<int, 0x142>{}, integral_constant<int, 0xa>{});
  x = dmax(x, integral_constant<int, 0x143>{}, integral_constant<int, 0xc>{});
  return (u32)__builtin_amdgcn_readlane((int)x, 63);
}

#define DLEN 304
#define RT_OFF (2 * DLEN)
#define AREA 3616
#define PX_OFF 1152
#define CH_LDS (2 * AREA)
#define SPX_OFF 8192
#define CONV_OFF 9216
#define CH2 3592
#define U_OFF 1408
#define SUM0 (U_OFF + 128 + 768)
#define SUM1 (U_OFF + 128 + 1024)
#define CTR_OFF (CONV_OFF + 8 * CH2)
#define K2_LDS (CTR_OFF + 64)
#define KROW 68
#define C_TD 8
#define C_DS 32
#define C_SY 40
#define C_YR 48
using u32x4 = __attribute__((ext_vector_type(4))) unsigned;
using u32x2 = __attribute__((ext_vector_type(2))) unsigned;
using bf16x8 = __attribute__((ext_vector_type(8))) __bf16;
using f16x8 = __attribute__((ext_vector_type(8))) _Float16;
#define SB() __builtin_amdgcn_sched_barrier(0)
#define BAR() do { asm volatile("" ::: "memory"); SB(); __builtin_amdgcn_s_barrier(); SB(); asm volatile("" ::: "memory"); } while (0)

__device__ __forceinline__ cf cexp_scaled(float ar2, float aih, float ail, float sc) {
  float e = __builtin_amdgcn_exp2f(ar2 * sc);
  float h = aih * sc;
  float r = (h - __builtin_rintf(h)) + ail * sc;
  cf w = cis_rev(r);
  return cmk(e * w.x, e * w.y);
}
__device__ __forceinline__ float bfr(float a) {
  float r;
  asm("v_cvt_pk_bf16_f32 %0, 0, %1" : "=v"(r) : "v"(a));
  return r;
}
__device__ __forceinline__ u32 cvtpkh(float a, float b) {
  u32 r;
  asm("v_cvt_pk_f16_f32 %0, %1, %2" : "=v"(r) : "v"(a), "v"(b));
  return r;
}
#define PM(d, a, b) "v_pk_mul_f32 " d ", " a ", " b " op_sel:[0,0] op_sel_hi:[0,1]\n\t"
#define PF(d, a, b) "v_pk_fma_f32 " d ", " a ", " b ", " d " op_sel:[1,1,0] op_sel_hi:[1,0,1] neg_lo:[0,1,0]\n\t"
#define QM(d, a, b) "v_pk_mul_f32 " d ", " a ", " b " op_sel:[0,0] op_sel_hi:[1,0]\n\t"
#define QF(d, a, b) "v_pk_fma_f32 " d ", " a ", " b ", " d " op_sel:[1,1,0] op_sel_hi:[0,1,1] neg_hi:[1,0,0]\n\t"
template <bool CONJ>
__device__ __forceinline__ void cm4(cf& r0, cf& r1, cf& r2, cf& r3, cf a0, cf a1, cf a2, cf a3, cf b) {
  if constexpr (CONJ)
    asm(QM("%0", "%4", "%8") QM("%1", "%5", "%8") QM("%2", "%6", "%8") QM("%3", "%7", "%8")
        QF("%0", "%4", "%8") QF("%1", "%5", "%8") QF("%2", "%6", "%8") QF("%3", "%7", "%8")
        : "=&v"(r0), "=&v"(r1), "=&v"(r2), "=&v"(r3) : "v"(a0), "v"(a1), "v"(a2), "v"(a3), "v"(b));
  else
    asm(PM("%0", "%4", "%8") PM("%1", "%5", "%8") PM("%2", "%6", "%8") PM("%3", "%7", "%8")
        PF("%0", "%4", "%8") PF("%1", "%5", "%8") PF("%2", "%6", "%8") PF("%3", "%7", "%8")
        : "=&v"(r0), "=&v"(r1), "=&v"(r2), "=&v"(r3) : "v"(a0), "v"(a1), "v"(a2), "v"(a3), "v"(b));
}
__device__ __forceinline__ void cm3(cf& r0, cf& r1, cf& r2, cf a0, cf a1, cf a2, cf b) {
  asm(PM("%0", "%3", "%6") PM("%1", "%4", "%6") PM("%2", "%5", "%6") PF("%0", "%3", "%6") PF("%1", "%4", "%6") PF("%2", "%5", "%6")
      : "=&v"(r0), "=&v"(r1), "=&v"(r2) : "v"(a0), "v"(a1), "v"(a2), "v"(b));
}
__device__ __forceinline__ void cmx3(cf& r0, cf& r1, cf& r2, cf a0, cf b0, cf a1, cf b1, cf a2, cf b2) {
  asm(QM("%0", "%3", "%4") QM("%1", "%5", "%6") PM("%2", "%7", "%8") QF("%0", "%3", "%4") QF("%1", "%5", "%6") PF("%2", "%7", "%8")
      : "=&v"(r0), "=&v"(r1), "=&v"(r2) : "v"(a0), "v"(b0), "v"(a1), "v"(b1), "v"(a2), "v"(b2));
}
__device__ __forceinline__ void csq3(cf& r0, cf& r1, cf& r2, cf a0, cf a1, cf a2) {
  asm(PM("%0", "%3", "%3") PM("%1", "%4", "%4") PM("%2", "%5", "%5") PF("%0", "%3", "%3") PF("%1", "%4", "%4") PF("%2", "%5", "%5")
      : "=&v"(r0), "=&v"(r1), "=&v"(r2) : "v"(a0), "v"(a1), "v"(a2));
}
__device__ __forceinline__ void csq2(cf& r0, cf& r1, cf a0, cf a1) {
  asm(PM("%0", "%2", "%2") PM("%1", "%3", "%3") PF("%0", "%2", "%2") PF("%1", "%3", "%3") : "=&v"(r0), "=&v"(r1) : "v"(a0), "v"(a1));
}
__device__ __forceinline__ cf cmulc(cf a, cf b) {
  cf r;
  asm(QM("%0", "%1", "%2") QF("%0", "%1", "%2") : "=&v"(r) : "v"(a), "v"(b));
  return r;
}

template <int NT, class T>
__device__ __forceinline__ auto ldg(T p) {
  if constexpr (NT) return __builtin_nontemporal_load(p);
  else return *p;
}
template <int NT, class T, class V>
__device__ __forceinline__ void stg(T p, V v) {
  if constexpr (NT) __builtin_nontemporal_store(v, p);
  else *p = v;
}
struct KParams { const u16 *Are, *Aim, *Bre, *Bim, *Cre, *Cim, *logdt; };
struct KIn { u32 ld, are, aim, bre, bim, cre, cim; };
typedef __attribute__((address_space(1))) char gch;
__device__ __forceinline__ gch* sptr(gch* p) {
  asm("" : "+s"(p));
  return p;
}
__device__ __forceinline__ u32 vopq(u32 o) {
  asm volatile("" : "+v"(o));
  return o;
}
__device__ __forceinline__ KIn kgen_load(const KParams& P, int d, int n0, int lane) {
  typedef const __attribute__((address_space(1))) u16* g16;
  KIn k;
  const u32 so = 2u * (u32)(d * NST + n0), vo = vopq(2u * lane);
  auto ld = [&](const u16* a) { return (u32)ldg<NTP>((g16)(sptr((gch*)a + so) + vo)); };
  k.are = ld(P.Are);
  k.aim = ld(P.Aim);
  k.ld = P.logdt[d];
  k.bre = ld(P.Bre);
  k.bim = ld(P.Bim);
  k.cre = ld(P.Cre);
  k.cim = ld(P.Cim);
  return k;
}
template <class F>
__device__ __forceinline__ int kgen(const KIn& in, u32* scr, int lane, f32x16& acc, F&& hook) {
  const float dt = bfround(expf(bf2f(in.ld)));
  const float ar = dt * bf2f(in.are), ai = dt * bf2f(in.aim);
  const cf dB = cmk(dt * bf2f(in.bre), dt * bf2f(in.bim));
  const cf C = cmk(bf2f(in.cre), bf2f(in.cim));
  const cf c0 = cmul(C, dB);
  cf e[11];
  float gm, e0;
  int es;
  {
    const float L2E = 1.44269504088896341f, I2PH = 0.15915494309189535f, I2PL = 6.4206383e-09f;
    float ar2 = ar * L2E;
    float aih = ai * I2PH;
    float ail = fmaf(ai, I2PL, fmaf(ai, I2PH, -aih));
    e[0] = cexp_scaled(ar2, aih, ail, 1.0f);
    e[4] = cexp_scaled(ar2, aih, ail, 16.0f);
    e[8] = cexp_scaled(ar2, aih, ail, 256.0f);
    float xg = fmaxf(ar2, 0.f);
    float esf = fminf(__builtin_ceilf(248.f * xg), 120.f);
    es = (int)esf;
    e0 = __builtin_amdgcn_ldexpf(16.f, -es);
    gm = fmaxf(__builtin_fabsf(c0.x), __builtin_fabsf(c0.y)) * __builtin_amdgcn_exp2f(fmaf(1800.f, xg, esf));
  }
  const int eg = min(max((int)(wave_umax(__float_as_uint(gm)) >> 23), 16), 254);
  SB();
  hook(std::integral_constant<int, 0>{});
  SB();
  csq3(e[1], e[5], e[9], e[0], e[4], e[8]);
  csq3(e[2], e[6], e[10], e[1], e[5], e[9]);
  csq2(e[3], e[7], e[2], e[6]);
  typedef __attribute__((address_space(3))) u32* l3w;
  typedef const __attribute__((address_space(3))) u32x4* l3r;
  l3w w = (l3w)(scr + lane);
  auto put = [&](int row, cf v) { w[KROW * row] = cvtpkh(v.x, v.y); };
  cf G[21], E[32];
  G[0] = cmk(__builtin_amdgcn_ldexpf(c0.x, es + 130 - eg), __builtin_amdgcn_ldexpf(-c0.y, es + 130 - eg));
  E[0] = cmk(e0, 0.f);
  E[1] = e[3] * e0;
  E[2] = e[4] * e0;
  E[4] = e[5] * e0;
  E[8] = e[6] * e0;
  E[16] = e[7] * e0;
  cmx3(G[1], G[2], E[3], G[0], e[0], G[0], e[1], E[1], e[4]);
  put(0, G[0]); put(21, E[0]); put(22, E[1]); put(23, E[2]); put(25, E[4]); put(29, E[8]); put(37, E[16]);
  cm3(E[5], E[6], E[7], E[1], E[2], E[3], e[5]);
  G[3] = cmulc(G[1], e[1]);
  put(1, G[1]); put(2, G[2]); put(24, E[3]);
  SB();
  hook(std::integral_constant<int, 1>{});
  SB();
  cm4<false>(E[9], E[10], E[11], E[12], E[1], E[2], E[3], E[4], e[6]);
  put(26, E[5]); put(27, E[6]); put(28, E[7]); put(3, G[3]);
  cm3(E[13], E[14], E[15], E[5], E[6], E[7], e[6]);
  put(30, E[9]); put(31, E[10]); put(32, E[11]); put(33, E[12]);
  cm4<true>(G[4], G[5], G[6], G[7], G[0], G[1], G[2], G[3], e[2]);
  put(34, E[13]); put(35, E[14]); put(36, E[15]);
  SB();
  hook(std::integral_constant<int, 2>{});
  SB();
  cm4<false>(E[17], E[18], E[19], E[20], E[1], E[2], E[3], E[4], e[7]);
  put(4, G[4]); put(5, G[5]); put(6, G[6]); put(7, G[7]);
  cm4<true>(G[8], G[9], G[10], G[11], G[0], G[2], G[4], G[6], e[8]);
  put(38, E[17]); put(39, E[18]); put(40, E[19]); put(41, E[20]);
  SB();
  hook(std::integral_constant<int, 3>{});
  SB();
  cm4<false>(E[21], E[22], E[23], E[24], E[5], E[6], E[7], E[8], e[7]);
  put(8, G[8]); put(9, G[9]); put(10, G[10]); put(11, G[11]);
  cm4<true>(G[12], G[13], G[14], G[15], G[0], G[4], G[8], G[10], e[9]);
  put(42, E[21]); put(43, E[22]); put(44, E[23]); put(45, E[24]);
  SB();
  hook(std::integral_constant<int, 4>{});
  SB();
  cm4<false>(E[25], E[26], E[27], E[28], E[9], E[10], E[11], E[12], e[7]);
  put(12, G[12]); put(13, G[13]); put(14, G[14]); put(15, G[15]);
  cm4<true>(G[16], G[17], G[18], G[19], G[0], G[8], G[12], G[14], e[10]);
  put(46, E[25]); put(47, E[26]); put(48, E[27]); put(49, E[28]);
  SB();
  hook(std::integral_constant<int, 5>{});
  SB();
  cm3(E[29], E[30], E[31], E[13], E[14], E[15], e[7]);
  G[20] = cmulc(G[16], e[10]);
  put(16, G[16]); put(17, G[17]); put(18, G[18]); put(19, G[19]);
  put(50, E[29]); put(51, E[30]); put(52, E[31]); put(20, G[20]);
  wave_sync();
  SB();
  hook(std::integral_constant<int, 6>{});
  SB();
#pragma unroll
  for (int i = 0; i < 16; ++i) acc[i] = 0.f;
  {
    l3r r = (l3r)(scr + KROW * (lane & 31) + 4 * (lane >> 5));
    u32x4 A[8], B[8];
#pragma unroll
    for (int m = 0; m < 8; ++m) {
      A[m] = r[2 * m];
      B[m] = r[2 * m + 21 * KROW / 4];
    }
    SB();
#pragma unroll
    for (int m = 0; m < 8; ++m)
      acc = __builtin_amdgcn_mfma_f32_32x32x16_f16(__builtin_bit_cast(f16x8, A[m]), __builtin_bit_cast(f16x8, B[m]), acc, 0, 0, 0);
  }
  wave_sync();
  return eg;
}

#define TP 17408
#define MX_OFF 4608
#define K1_CTR 4624
#define K1_KG 4672
#define K1_LDS (K1_KG + 8 * AREA)
#define KV_G (20 << 20)
#define UT_G (24 << 20)
#define UE_OFF ((28 << 20) + 65536)
#define TA_OFF (28 << 20)
#define TB_OFF ((28 << 20) + 1024)
#define PX_G ((28 << 20) + 2048)
#define WC_OFF ((28 << 20) + 4096)
#define PERM(t) (4 * (((t) & 15) + 16 * ((t) >> 6)) + (((t) >> 4) & 3))

typedef __attribute__((ext_vector_type(4))) float f32x4;
using f32x4v = __attribute__((ext_vector_type(4))) float;
typedef __attribute__((ext_vector_type(2))) _Float16 f16x2;
typedef __attribute__((ext_vector_type(2))) __bf16 bf16x2;
typedef __amdgpu_buffer_rsrc_t rsrc_t;
__device__ __forceinline__ rsrc_t mkrsrc(const void* p) {
  return __builtin_amdgcn_make_buffer_rsrc(const_cast<void*>(p), 0, 32 << 20, 0x00020000);
}
template <int AUX>
__device__ __forceinline__ void st_wt(rsrc_t rs, int off, u32x4 q) { __builtin_amdgcn_raw_buffer_store_b128(q, rs, off, 0, AUX); }
typedef unsigned long long u64;
typedef __attribute__((address_space(1))) u64 gu64;
typedef __attribute__((address_space(1))) unsigned gu32;
__device__ __forceinline__ u32 ld_tag(gch* p) { return __hip_atomic_load((gu32*)p, __ATOMIC_RELAXED, __HIP_MEMORY_SCOPE_AGENT); }
#define TS(k)
typedef const __attribute__((address_space(3))) u32* l3p;
typedef const __attribute__((address_space(3))) u32x4* l3q;
typedef const __attribute__((address_space(3))) u32x2* l3d;
typedef __attribute__((address_space(3))) u32x4* l3wq;
typedef __attribute__((address_space(3))) u32x2* l3w2;

__device__ __forceinline__ f32x4v mma(u32x4 a, u32x4 b, f32x4v c) {
  return __builtin_amdgcn_mfma_f32_16x16x32_f16(__builtin_bit_cast(f16x8, a), __builtin_bit_cast(f16x8, b), c, 0, 0, 0);
}
__device__ __forceinline__ f32x4v mmb(u32x4 a, u32x4 b, f32x4v c) {
  return __builtin_amdgcn_mfma_f32_16x16x32_bf16(__builtin_bit_cast(bf16x8, a), __builtin_bit_cast(bf16x8, b), c, 0, 0, 0);
}
__device__ __forceinline__ u32 pkh(float a, float b) { return __builtin_bit_cast(u32, __builtin_convertvector(cmk(a, b), f16x2)); }
__device__ __forceinline__ u32 pkb(float a, float b) { return __builtin_bit_cast(u32, __builtin_convertvector(cmk(a, b), bf16x2)); }
template <int LOGN>
__device__ __forceinline__ cf cisn(int n) {
  const float r = (float)(n & ((1 << LOGN) - 1)) * (1.0f / (float)(1 << LOGN));
  return cmk(__builtin_amdgcn_cosf(r), __builtin_amdgcn_sinf(r));
}
struct CD { f32x4v re, im; };
struct MP { u32x4 re, im; };
struct TW { f32x4v c, s; };
#define Z4 (f32x4v{0.f, 0.f, 0.f, 0.f})
__device__ __forceinline__ CD mmA(u32x4 d, const MP& m) { return CD{mma(d, m.re, Z4), mma(d, m.im, Z4)}; }
__device__ __forceinline__ CD mmB(u32x4 d, const MP& m) { return CD{mma(m.re, d, Z4), mma(m.im, d, Z4)}; }
template <bool CONJ>
__device__ __forceinline__ CD twm(const CD& d, const TW& t) {
  if constexpr (CONJ) return CD{d.re * t.c - d.im * t.s, d.im * t.c + d.re * t.s};
  else return CD{d.re * t.c + d.im * t.s, d.im * t.c - d.re * t.s};
}
__device__ __forceinline__ u32x4 packh(const CD& d) {
  return u32x4{pkh(d.re[0], d.im[0]), pkh(d.re[1], d.im[1]), pkh(d.re[2], d.im[2]), pkh(d.re[3], d.im[3])};
}
__device__ __forceinline__ u32 cmh(u32 z, u32 t) {
  const f16x2 a = __builtin_bit_cast(f16x2, z), w = __builtin_bit_cast(f16x2, t);
  const f16x2 p = f16x2{a.x, a.x} * w;
  return __builtin_bit_cast(u32, __builtin_elementwise_fma(f16x2{a.y, a.y}, f16x2{-w.y, w.x}, p));
}
template <class T>
__device__ __forceinline__ void pin(T& a) { asm volatile("" : "+v"(a)); }
__device__ __forceinline__ void pin(MP& m) { asm volatile("" : "+v"(m.re), "+v"(m.im)); }
__device__ __forceinline__ void pin(TW& m) { asm volatile("" : "+v"(m.c), "+v"(m.s)); }
__device__ __forceinline__ int qk_q(int qk) { return (0x40332211u >> (4 * qk)) & 7; }

template <class LD>
__device__ __forceinline__ void brow_block(LD&& load, const void* xbase, u32* smw, u32* ctr, int t, int tid) {
  const int w = __builtin_amdgcn_readfirstlane(tid >> 6), lane = tid & 63, lo = lane & 15, g = lane >> 4;
  u32* mxs = smw + MX_OFF;
  MP Fh, Fl, F, BM, BP;
  TW t1, t2;
#pragma unroll
  for (int j = 0; j < 4; ++j) {
    const cf e = cisn<4>((4 * g + j) * lo);
    Fh.re[j] = pkb(e.x, e.y);
    Fh.im[j] = pkb(-e.y, e.x);
    const float rc = e.x - __uint_as_float(Fh.re[j] << 16), rs = e.y - __uint_as_float(Fh.re[j] & 0xffff0000u);
    Fl.re[j] = pkb(rc, rs);
    Fl.im[j] = pkb(-rs, rc);
    F.re[j] = pkh(e.x * 0.0625f, e.y * 0.0625f);
    F.im[j] = pkh(e.y * -0.0625f, e.x * 0.0625f);
    const cf a = cisn<8>((4 * g + j) * lo), b = cisn<9>(lo + 16 * (4 * g + j));
    t1.c[j] = a.x; t1.s[j] = a.y;
    t2.c[j] = b.x; t2.s[j] = b.y;
  }
  {
    const int qk = lo >> 1, ty = lo & 1, q = qk_q(qk), kc = (qk < 6 ? qk : 0) & 1;
    const float fm = ty ? 0.f : 0.0625f, fp = ty ? 0.0625f : 0.f;
    const u32 sg = (kc ^ ty) ? 0x80008000u : 0u;
#pragma unroll
    for (int j = 0; j < 2; ++j) {
      const cf e = cisn<11>(q * (256 * (2 * g + j) + t));
      BM.re[j] = pkh(e.x * fm, e.y * fm);
      BM.im[j] = pkh(-e.y * fm, e.x * fm);
      BP.re[j] = pkh(e.x * fp, -e.y * fp);
      BP.im[j] = pkh(-e.y * fp, -e.x * fp);
      BM.re[j + 2] = BM.re[j] ^ sg;
      BM.im[j + 2] = BM.im[j] ^ sg;
      BP.re[j + 2] = BP.re[j] ^ sg;
      BP.im[j + 2] = BP.im[j] ^ sg;
    }
  }
  pin(Fh); pin(Fl); pin(F); pin(BM); pin(BP); pin(t1); pin(t2);
  SB();
  u32x4 xin[4];
  load(xin);
  SB();
  const rsrc_t X1 = mkrsrc(xbase);
  CD d[4];
#pragma unroll
  for (int i = 0; i < 4; ++i) {
    d[i].re = mmb(xin[i], Fh.re, mmb(xin[i], Fl.re, Z4));
    d[i].im = mmb(xin[i], Fh.im, mmb(xin[i], Fl.im, Z4));
  }
  {
    typedef __attribute__((ext_vector_type(2))) unsigned short u16x2;
    u16x2 m = {0, 0};
#pragma unroll
    for (int i = 0; i < 4; ++i)
#pragma unroll
      for (int j = 0; j < 4; ++j) m = __builtin_elementwise_max(m, __builtin_bit_cast(u16x2, xin[i][j] & 0x7fff7fffu));
    const u32 wm = wave_umax(max((u32)m.x, (u32)m.y));
    if (lane == 0) mxs[w] = wm;
  }
  garrive(ctr);
  gwait<0>(ctr, 4u);
  const u32 ex = min(max(max(max(mxs[0], mxs[1]), max(mxs[2], mxs[3])) >> 7, 27u), 227u);
  const float s1 = __uint_as_float((263u - ex) << 23);
  if (tid == 0) __builtin_amdgcn_raw_buffer_store_b32(ex, X1, TA_OFF + 4 * PERM(t), 0, 16);
  t1.c *= s1;
  t1.s *= s1;
#pragma unroll
  for (int i = 0; i < 4; ++i) d[i] = mmB(packh(twm<false>(d[i], t1)), F);
  d[2] = twm<false>(d[2], t2);
  d[3] = twm<false>(d[3], t2);
  const int f = (lo >> 2) & 3;
  {
    l3wq lw = (l3wq)(smw + 16 * lo + 1024 * g + 4 * (w ^ f));
#pragma unroll
    for (int j = 0; j < 4; ++j) {
      const u32x4 o = {pkh(d[0].re[j], d[0].im[j]), pkh(d[1].re[j], d[1].im[j]), pkh(d[2].re[j], d[2].im[j]), pkh(d[3].re[j], d[3].im[j])};
      lw[64 * j] = o;
      if (j == 0 && lane == 0) lw[1024] = u32x4{o[0], o[1], o[2] ^ 0x80008000u, o[3] ^ 0x80008000u};
    }
  }
  garrive(ctr + 1);
  gwait<0>(ctr + 1, 4u);
  {
    l3q rm = (l3q)(smw + 16 * lo + 1024 * w + 4 * (g ^ f));
    const int kp = 256 - lo - 64 * w;
    l3q rp = (l3q)(smw + 16 * (kp - 48) + 4 * (g ^ ((kp >> 2) & 3)));
    u32x4 om[4], op[4];
#pragma unroll
    for (int i = 0; i < 4; ++i) {
      om[i] = rm[64 * i];
      op[i] = rp[64 * (3 - i)];
    }
    const int vo = 2048 * (lo >> 1) + 32 * (lo & 1) + 64 * (g >> 1) + 16 * (g & 1);
    const int so = t * TP + 512 * w;
#pragma unroll
    for (int i = 0; i < 4; ++i) {
      CD r;
      r.re = mma(op[i], BP.re, mma(om[i], BM.re, Z4));
      r.im = mma(op[i], BP.im, mma(om[i], BM.im, Z4));
      u32x4 o = packh(r) & ~1u;
      if (i == 0 && w == 0) {
        const CD x = mmA(om[0], BP);
        const u32 xo = pkh(x.re[0], x.im[0]) & ~1u;
        if (lane == 13 || lane == 15) {
          __builtin_amdgcn_raw_buffer_store_b32(o[0], X1, 512 * (lane - 13), PX_G + 4 * PERM(t), 16);
          o[0] = xo;
        }
      }
      __builtin_amdgcn_raw_buffer_store_b128(o, X1, vo + 128 * i, so, AUX_X);
    }
  }
}
#define GATE(pos) do { if (GATEPOS == pos) garrive(ctr + 4); if (GATEP1 == pos && GATEP1 != GATEPOS) garrive(ctr + 5); } while (0)
__device__ __forceinline__ void dot4(u32 wr, u32 wi, u32 k0, u32 k1, u32 k2, u32 k3, float& a, float& b, float& c, float& d) {
  asm("v_dot2_f32_bf16 %0, %4, %6, 0\n\tv_dot2_f32_bf16 %1, %5, %7, 0\n\tv_dot2_f32_bf16 %2, %4, %8, 0\n\tv_dot2_f32_bf16 %3, %5, %9, 0"
      : "=&v"(a), "=&v"(b), "=&v"(c), "=&v"(d) : "v"(wr), "v"(wi), "v"(k0), "v"(k1), "v"(k2), "v"(k3));
}
__device__ __forceinline__ void dot4a(u32 wr, u32 wi, u32 k0, u32 k1, u32 k2, u32 k3, float& a, float& b, float& c, float& d) {
  asm("v_dot2_f32_bf16 %0, %4, %6, %0\n\tv_dot2_f32_bf16 %1, %5, %7, %1\n\tv_dot2_f32_bf16 %2, %4, %8, %2\n\tv_dot2_f32_bf16 %3, %5, %9, %3"
      : "+v"(a), "+v"(b), "+v"(c), "+v"(d) : "v"(wr), "v"(wi), "v"(k0), "v"(k1), "v"(k2), "v"(k3));
}
__device__ __forceinline__ u32 pkmulh(u32 a, u32 b) {
  return __builtin_bit_cast(u32, __builtin_bit_cast(f16x2, a) * __builtin_bit_cast(f16x2, b));
}

__device__ __forceinline__ void wstage_store(const u32x4* s, u32* WT, int hw, int lane) {
  const int f = (lane >> 2) & 3;
  u32* row = WT + 16 * (64 * hw + lane);
#pragma unroll
  for (int w = 0; w < 4; ++w)
    *(l3wq)(row + 4 * (w ^ f)) = u32x4{__builtin_amdgcn_perm(s[2][3 - w], s[0][w], 0x07060100u), __builtin_amdgcn_perm(s[3][3 - w], s[1][w], 0x07060100u),
                                       __builtin_amdgcn_perm(s[2][3 - w], s[0][w], 0x05040302u), __builtin_amdgcn_perm(s[3][3 - w], s[1][w], 0x05040302u)};
}

__device__ __forceinline__ void bwaves(gch* X, u16* __restrict__ out, u32* XT, u32* WT, u32* SPX, u32* ctr, int b, int tid) {
  const int w = __builtin_amdgcn_readfirstlane(tid >> 6), lane = tid & 63, lo = lane & 15, g = lane >> 4;
  const int beta = ((b & 7) << 5) | (b >> 3);
  const int qk = beta >> 5, q = qk_q(qk), k0 = (qk < 6 ? (qk & 1) * 256 : 0) + 8 * (beta & 31);
  const bool sp1 = (k0 == 0) && (w == 0), sp2 = sp1 && (qk >= 6);
  typedef const __attribute__((address_space(1))) u32x4* g4p;
  u32x4 xs[4];
  const u32x4 ta = *(g4p)(sptr(X + TA_OFF) + 16u * lane), tb = *(g4p)(sptr(X + TA_OFF) + 16u * lane + (TB_OFF - TA_OFF));
  SB();
  {
    gch* xb = X + (size_t)(64 * w) * TP + 64 * beta;
    const u32 vo = (u32)((lane >> 2) * TP + 16 * (lane & 3));
#pragma unroll
    for (int i = 0; i < 4; ++i) xs[i] = *(g4p)(sptr(xb + (size_t)(16 * i) * TP) + vo);
  }
  SB();
  u32x4 wx[4];
  {
    const u32 so = vopq(sp1 ? 16u * lane : 0u);
    gch* wc = X + WC_OFF + 1024 * q;
    wx[0] = *(g4p)(sptr(wc) + so);
    wx[1] = *(g4p)(sptr(wc + 16384) + so);
    wx[2] = *(g4p)(sptr(wc + 8192) + so);
    wx[3] = *(g4p)(sptr(X + PX_G + 1024 * (qk & 1)) + so);
  }
  SB();
  __syncthreads();
  SB();
  MP H;
  u32x4 t2;
  u32 vo[4], v0[4];
  auto late = [&]() {
    SB();
  {
    const int a = 2 * (lo >> 2) + (lo & 1), sg = (lo >> 1) & 1, isp = g >> 1;
#pragma unroll
    for (int j = 0; j < 4; ++j) {
      const int qq = 4 * (g & 1) + j, kc = (qq < 6 ? qq : 0) & 1;
      const cf e = cisn<11>(qk_q(qq) * (256 * a + b));
      const float sc = (sg & (kc ^ isp)) ? -0.25f : 0.25f, sd = isp ? -sc : sc;
      H.re[j] = pkh(e.x * sc, -e.y * sc);
      H.im[j] = pkh(e.y * sd, e.x * sd);
      const cf c2 = cisn<9>(lo + 16 * (4 * w + j));
      t2[j] = pkh(c2.x, c2.y);
    }
  }
  pin(H); pin(t2);
#pragma unroll
  for (int i = 0; i < 4; ++i) {
    const int ka = lo + 16 * (4 * w + i), kp = 256 - ka;
    const int kk = (g >> 1) ? kp : ka;
    vo[i] = (u32)(4096 + 8192 * (g & 1) + 64 * (kk >> 3) + 4 * (kk & 7) + 32 * (g >> 1));
  }
  {
    const bool fx = (w == 0) && (lo == 0) && (g >= 2);
    v0[0] = vo[0];
    v0[1] = vo[0] - (fx ? 4096u : 0u);
    v0[2] = vo[0] - ((fx && g == 3) ? 2048u : 0u);
    v0[3] = vo[0] - (fx ? (g == 3 ? 2048u : 4096u) : 0u);
  }
  asm volatile("" : "+v"(vo[1]), "+v"(vo[2]), "+v"(vo[3]), "+v"(v0[0]), "+v"(v0[1]), "+v"(v0[2]), "+v"(v0[3]));
  SB();
  };
#define LATE(pos) do { if (CPOS == pos) { if (pos == 1) __builtin_amdgcn_s_setprio(0); late(); if (pos == 1) __builtin_amdgcn_s_setprio(3); } } while (0)
  MP F, G;
  TW tw;
#pragma unroll
  for (int j = 0; j < 4; ++j) {
    const cf e = cisn<4>((4 * g + j) * lo);
    F.re[j] = pkh(e.x * 0.0625f, e.y * 0.0625f);
    F.im[j] = pkh(e.y * -0.0625f, e.x * 0.0625f);
    G.re[j] = pkh(e.x * 0.25f, e.y * -0.25f);
    G.im[j] = pkh(e.y * 0.25f, e.x * 0.25f);
    const cf a = cisn<8>((4 * g + j) * lo);
    tw.c[j] = a.x;
    tw.s[j] = a.y;
  }
  cf cs0 = cisn<10>(k0 + 2 * w), cs1 = cisn<10>(k0 + 2 * w + 1);
  const int f = (lo >> 2) & 3;
  pin(F); pin(G); pin(tw); pin(cs0); pin(cs1);
  SB();
  LATE(0);
  LATE(1);
  const u32 em = wave_umax(max(max(ta[0], ta[1]), max(ta[2], ta[3])));
  const u32 ew = min(max(wave_umax(max(max(tb[0], tb[1]), max(tb[2], tb[3]))) >> 7, 27u), 227u);
  u32 dm[4];
#pragma unroll
  for (int j = 0; j < 4; ++j) {
    const float v = __uint_as_float((127u - min(em - ta[j], 40u)) << 23);
    dm[j] = pkh(v, v);
  }
  const u32 gb = (251u - ew) << 7;
  const u32 kpp = gb * 0x10001u;
  const int e2 = min(max((int)(em + ew) - 255, -126), 127), e2c = min(max(e2, -10), 15);
  {
    u32* xw = XT + 1024 * w + 16 * (lane >> 2) + 4 * ((lane & 3) ^ ((lane >> 4) & 3));
#pragma unroll
    for (int i = 0; i < 4; ++i) *(l3wq)(xw + 256 * i) = xs[i];
  }
  pin(dm[0]); pin(dm[1]); pin(dm[2]); pin(dm[3]);
  garrive(ctr);
  gwait<0>(ctr, 4u);
  TS(5);
  u32* xp[2];
  xp[0] = XT + 16 * lo + 1024 * g + 4 * ((w >> 1) ^ f) + 2 * (w & 1);
  xp[1] = XT + 16 * lo + 1024 * g + 4 * ((2 + (w >> 1)) ^ f) + 2 * (w & 1);
  u32x4 op[4];
#pragma unroll
  for (int j = 0; j < 4; ++j)
#pragma unroll
    for (int ty = 0; ty < 2; ++ty) {
      const u32x2 v = *(l3d)(xp[ty] + 256 * j);
      op[2 * ty][j] = pkmulh(v.x, dm[j]);
      op[2 * ty + 1][j] = pkmulh(v.y, dm[j]);
    }
  CD z[4];
#pragma unroll
  for (int c = 0; c < 4; ++c) z[c] = mmA(op[c], F);
#pragma unroll
  for (int c = 0; c < 4; ++c) z[c] = mmB(packh(twm<false>(z[c], tw)), F);
  GATE(1);
  CD y5;
  const u32x4 ex_ = wx[0], ey_ = wx[1], e5_ = wx[2];
  if (sp2) {
    u32x4 o5;
#pragma unroll
    for (int j = 0; j < 4; ++j) o5[j] = pkmulh(wx[3][j], dm[j]);
    y5 = z[2];
    z[2] = mmB(packh(twm<false>(mmA(o5, F), tw)), F);
  }
  LATE(2);
  gwait<0>(ctr + 3, 4u);
  TS(6);
  GATE(2);
  {
    l3q wp = (l3q)(WT + 16 * lo + 1024 * g + 4 * (w ^ f));
    u32x4 wq[4];
#pragma unroll
    for (int j = 0; j < 4; ++j) wq[j] = wp[64 * j];
#pragma unroll
    for (int u = 0; u < 2; ++u) {
      float sd[4][4];
      {
        const bool sp = (u == 0) && sp1;
        const u32 hpp = sp ? kpp - 0x00800080u : kpp, hpm = hpp | 0x80000000u;
#pragma unroll
        for (int j = 0; j < 4; ++j) dot4(wq[j][2 * u], wq[j][2 * u + 1], hpp, hpm, hpm, hpp, sd[0][j], sd[1][j], sd[2][j], sd[3][j]);
        if (sp) {
#pragma unroll
          for (int j = 0; j < 4; ++j) {
            const u32 er = __builtin_amdgcn_perm(ey_[j], ex_[j], 0x05040100u), ei = __builtin_amdgcn_perm(ey_[j], ex_[j], 0x07060302u);
            dot4a(er, ei, hpp, hpp | 0x8000u, hpm, hpm | 0x8000u, sd[0][j], sd[1][j], sd[2][j], sd[3][j]);
          }
        }
      }
      f32x4v sr = {sd[0][0], sd[0][1], sd[0][2], sd[0][3]}, si = {sd[1][0], sd[1][1], sd[1][2], sd[1][3]};
      f32x4v dr = {sd[2][0], sd[2][1], sd[2][2], sd[2][3]}, di = {sd[3][0], sd[3][1], sd[3][2], sd[3][3]};
      asm volatile("s_nop 2" : "+v"(sr), "+v"(si), "+v"(dr), "+v"(di));
      const cf cs = u ? cs1 : cs0;
      const f32x4v ar = sr - cs.y * dr, ai = si - cs.y * di, er = sr + cs.y * dr, ei = si + cs.y * di;
      const f32x4v br = cs.x * dr, bi = cs.x * di;
      const CD a = z[u], y = z[2 + u];
      z[u].re = ar * a.re - ai * a.im - br * y.im - bi * y.re;
      z[u].im = ar * a.im + ai * a.re + br * y.re - bi * y.im;
      z[2 + u].re = er * y.re - ei * y.im + br * a.im + bi * a.re;
      z[2 + u].im = er * y.im + ei * y.re - br * a.re + bi * a.im;
      if (u == 0 && sp2) {
        const float g2 = __uint_as_float((gb << 16) + 0x00800000u);
        f32x4v vr, vi;
#pragma unroll
        for (int j = 0; j < 4; ++j) {
          vr[j] = __uint_as_float(e5_[j] << 16) * g2;
          vi[j] = __uint_as_float(e5_[j] & 0xffff0000u) * g2;
        }
        z[2].re = vr * y5.re - vi * y5.im;
        z[2].im = vr * y5.im + vi * y5.re;
      }
    }
  }
  GATE(3);
#pragma unroll
  for (int c = 0; c < 4; ++c) z[c] = mmA(packh(z[c]), G);
#pragma unroll
  for (int c = 0; c < 4; ++c) z[c] = mmB(packh(twm<true>(z[c], tw)), G);
  GATE(5);
#pragma unroll
  for (int j = 0; j < 4; ++j)
#pragma unroll
    for (int ty = 0; ty < 2; ++ty)
      *(l3w2)(xp[ty] + 256 * j) = u32x2{pkh(z[2 * ty].re[j], z[2 * ty].im[j]) | 1u, pkh(z[2 * ty + 1].re[j], z[2 * ty + 1].im[j]) | 1u};
  garrive(ctr + 1);
  gwait<0>(ctr + 1, 4u);
  GATE(6);
  {
    gch* xb = X + (size_t)(64 * w) * TP + 64 * beta;
    const u32 vo = (u32)((lane >> 3) * TP + 8 * (lane & 7));
    u32x2 v[8];
#pragma unroll
    for (int i = 0; i < 8; ++i) {
      const int r = 8 * i + (lane >> 3);
      v[i] = *(l3d)(XT + 1024 * w + 16 * r + 4 * (((lane >> 1) & 3) ^ ((r >> 2) & 3)) + 2 * (lane & 1));
    }
#pragma unroll
    for (int i = 0; i < 8; ++i)
      __hip_atomic_store((gu64*)(sptr(xb + (size_t)(8 * i) * TP) + vo), __builtin_bit_cast(u64, v[i]), __ATOMIC_RELAXED, __HIP_MEMORY_SCOPE_AGENT);
  }
  GATE(7);
  TS(1);
  u32x4 th;
  MP G2;
  {
    const u32 sh = (u32)(e2c + 15) * 0x04000400u;
#pragma unroll
    for (int j = 0; j < 4; ++j) {
      th[j] = pkh(tw.c[j], tw.s[j]);
      G2.re[j] = pkmulh(G.re[j], sh);
      G2.im[j] = pkmulh(G.im[j], sh);
    }
    pin(th); pin(G2);
  }
  __builtin_amdgcn_s_sleep(PSLEEP);
  LATE(3);
  u32x4 hv[4];
  {
    gch* xr = X + (size_t)b * TP;
    gch* xq[4];
#pragma unroll
    for (int j = 0; j < 4; ++j) xq[j] = sptr(xr + (2048 * j - 4096));
#pragma unroll
    for (int i = 0; i < 4; ++i)
#pragma unroll
      for (int j = 0; j < 4; ++j) hv[i][j] = ld_tag(xq[j] + (i ? vo[i] : v0[j]));
    for (unsigned spins = 0; spins < (1u << 14); ++spins) {
      const u32x4 an = hv[0] & hv[1] & hv[2] & hv[3];
      if (__all(an[0] & an[1] & an[2] & an[3] & 1u)) break;
#pragma unroll
      for (int i = 0; i < 4; ++i)
#pragma unroll
        for (int j = 0; j < 4; ++j)
          if (!(hv[i][j] & 1u)) hv[i][j] = ld_tag(xq[j] + vopq(i ? vo[i] : v0[j]));
    }
  }
  TS(2);
#pragma unroll
  for (int i = 0; i < 4; ++i) {
    op[i] = packh(mmB(hv[i], H));
    op[i][2] = cmh(op[i][2], t2[i]);
    op[i][3] = cmh(op[i][3], t2[i]);
  }
  {
    l3wq lw = (l3wq)(WT + 1024 * g + 16 * lo + 4 * (w ^ f));
#pragma unroll
    for (int j = 0; j < 4; ++j) lw[64 * j] = u32x4{op[0][j], op[1][j], op[2][j], op[3][j]};
  }
  garrive(ctr + 2);
  gwait<0>(ctr + 2, 4u);
  {
    l3q lr = (l3q)(WT + 1024 * w + 16 * lo + 4 * (g ^ f));
#pragma unroll
    for (int i = 0; i < 4; ++i) op[i] = lr[64 * i];
  }
#pragma unroll
  for (int i = 0; i < 4; ++i) {
    op[i] = packh(mmA(op[i], G));
#pragma unroll
    for (int j = 0; j < 4; ++j) op[i][j] = cmh(op[i][j], th[j]);
  }
#pragma unroll
  for (int i = 0; i < 4; ++i) z[i] = mmB(op[i], G2);
  if (__builtin_expect(e2 != e2c, 0)) {
    const float s2 = __uint_as_float((u32)(e2 - e2c + 127) << 23);
#pragma unroll
    for (int i = 0; i < 4; ++i) z[i] = CD{z[i].re * s2, z[i].im * s2};
  }
  {
    typedef __attribute__((address_space(1))) u32x2 gu2;
    const u32 vo = (u32)(8 * lo + 512 * g);
#pragma unroll
    for (int i = 0; i < 2; ++i) {
      gch* ob = sptr((gch*)out + (size_t)(b + 256 * (2 * w + i)) * (4 * DCH) + 2 * DCH);
#pragma unroll
      for (int j = 0; j < 4; ++j) stg<(NTO >> 1)>((gu2*)(ob + vo + 128 * j), u32x2{pkb(z[i].re[j], z[i].im[j]), pkb(z[i + 2].re[j], z[i + 2].im[j])});
    }
  }
}

typedef u32x4 u32x4a4 __attribute__((aligned(4)));
typedef const __attribute__((address_space(3))) u32x4a4* l3a;
typedef __attribute__((address_space(3))) u16* l3h;
typedef __attribute__((address_space(3))) u32* l3w1;
typedef __attribute__((address_space(3))) char* l3c;
typedef const volatile __attribute__((address_space(3))) u32x4* l3vq;

struct KAddr { u32 z0, z1, a, b, t[4]; };
__device__ __forceinline__ KAddr kaddr(u32 base, int lane) {
  KAddr k;
  const int col = lane & 31, hh = lane >> 5;
  {
    const int h = lane >= 31 ? 1 : 0, q = lane - 31 * h;
    int r = 60 * h + (q < 15 ? q : q + 29);
    if (lane >= 62) r = 58 + lane;
    k.z0 = base + 4 * RT_OFF + 16 * r;
    int o = 4 * RT_OFF + 16 * (122 + min(lane, 12));
    if (lane >= 13) o = 4 * (268 + 4 * (lane - 13));
    if (lane >= 16) o = 4 * (DLEN + 272 + 4 * (lane & 1));
    if (lane >= 18) o = 4 * DLEN;
    k.z1 = base + o;
  }
  k.a = base + 2 * (539 - 8 * col - 4 * hh);
  k.b = base + 560 - 16 * min(lane, 35);
  {
    const int r0 = 16 * (132 + ((col - 1) >> 1)) + 8 * (1 - (col & 1));
    const int m = 12 + (col >> 1);
    const int s0 = (col & 1) ? 16 * m + 8 : 16 * (60 + m - 1) + 8;
    const int s1 = (col & 1) ? 16 * (60 + m) : 16 * m;
    const u32 rb = base + 4 * RT_OFF;
    k.t[0] = rb + (hh ? (col ? s0 : 16 * 195 + 8) : (col ? r0 : 16 * 103 + 8));
    k.t[1] = rb + (hh ? s1 : r0 + 256);
    k.t[2] = rb + (hh ? s0 + 256 : r0 + 512);
    k.t[3] = rb + (hh ? s1 + 256 : r0 + 768);
  }
  return k;
}
__device__ __forceinline__ void kpack(const float* kv, int lane, u32x4& q0, u32x4& q1) {
  const float v0 = lane <= 32 ? kv[4] : 0.f, v1 = lane < 32 ? kv[5] : v0, v2 = lane < 32 ? kv[6] : 0.f, v3 = lane < 32 ? kv[7] : 0.f;
  const bool hi = lane >= 32;
  q0 = u32x4{pkh(kv[3], kv[2]), pkh(kv[1], kv[0]), pkh(v3, v2), pkh(v1, v0)};
  q1[0] = pkh(lane > 32 ? kv[4] : kv[8], hi ? kv[5] : kv[9]);
  q1[1] = pkh(hi ? kv[6] : kv[10], hi ? kv[7] : kv[11]);
}
__device__ __forceinline__ void kzero(const KAddr& k) {
  *(l3wq)(size_t)k.z0 = u32x4{0u, 0u, 0u, 0u};
  *(l3wq)(size_t)k.z1 = u32x4{0u, 0u, 0u, 0u};
  asm volatile("" ::: "memory");
}
__device__ __forceinline__ void kimage(const KAddr& k, u32x4 q0, u32x4 q1, int lane) {
  {
    const u32 a = q0[2], c = q0[3];
    const u32 b = __builtin_amdgcn_perm(a, a, 0x03020302u), d = __builtin_amdgcn_perm(c, c, 0x03020302u), e = __builtin_amdgcn_alignbit(c, a, 16);
    if (lane < 36) {
      l3c p = (l3c)(size_t)k.b;
      *(l3wq)p = u32x4{a, b, c, d};
      *(l3h)(p + 4 * DLEN + 2) = (u16)a;
      *(l3w1)(p + 4 * DLEN + 4) = b;
      *(l3w2)(p + 4 * DLEN + 8) = u32x2{e, d};
      *(l3h)(p + 4 * DLEN + 16) = (u16)d;
    }
  }
  asm volatile("" ::: "memory");
  {
    const u32 p32 = q0[0], p10 = q0[1];
    l3c p = (l3c)(size_t)k.a;
    *(l3h)p = (u16)p32;
    *(l3w1)(p + 2) = __builtin_amdgcn_alignbit(p10, p32, 16);
    *(l3h)(p + 6) = (u16)(p10 >> 16);
    *(l3w2)(p + 4 * DLEN + 2) = u32x2{p32, p10};
  }
  {
    const u32 c01 = q1[0], c23 = q1[1];
#pragma unroll
    for (int s = 0; s < 4; ++s) {
      *(l3h)((l3c)(size_t)k.t[0] + 18 * s) = (u16)c01;
      *(l3h)((l3c)(size_t)k.t[1] + 18 * s) = (u16)(c01 >> 16);
      *(l3h)((l3c)(size_t)k.t[2] + 18 * s) = (u16)c23;
      *(l3h)((l3c)(size_t)k.t[3] + 18 * s) = (u16)(c23 >> 16);
    }
  }
}
__device__ __forceinline__ void wpatterns(int lane, u32x4* pat) {
  u32 b0 = (u32)(8 * (lane >> 4) - (lane & 15) + 0x4002) * 0x10001u + 0x10000u, cx[17];
#pragma unroll
  for (int x = 0; x < 17; ++x) {
    asm("" : "+v"(b0));
    cx[x] = b0 - (u32)x * 0x10001u;
    asm("" : "+v"(cx[x]));
  }
#pragma unroll
  for (int w = 0; w < 4; ++w) {
    auto th = [&](int t) { return cx[t + 2 - 2 * w]; };
    pat[0][w] = th(14) & ~th(11) & 0x3c003c00u;
    pat[1][w] = th(11) & ~th(6) & 0x3c003c00u;
    pat[2][w] = th(12) & ~th(5) & 0x3c003c00u;
    pat[3][w] = th(13) & ~th(4) & 0x3c003c00u;
    pat[4][w] = th(14) & ~th(12) & 0x3c003c00u;
  }
}
__device__ __forceinline__ u32x4 zpack(f32x4v a, f32x4v b) { return u32x4{pkh(a[0], a[1]), pkh(a[2], a[3]), pkh(b[0], b[1]), pkh(b[2], b[3])}; }

__device__ __forceinline__ void skip2(float& a, float& b, u32 uu, float ds) {
  asm("v_fma_mix_f32 %0, %2, %3, 0 op_sel_hi:[1,0,0]\n\tv_fma_mix_f32 %1, %2, %3, 0 op_sel:[1,0,0] op_sel_hi:[1,0,0]\n\t"
      "v_cvt_pk_bf16_f32 %0, 0, %0\n\tv_cvt_pk_bf16_f32 %1, 0, %1"
      : "=&v"(a), "=&v"(b) : "v"(uu), "v"(ds));
}
__device__ __forceinline__ void fin2(float& a, float& b, float& y0, float& y1, float s0, float s1) {
  asm("v_cvt_pk_bf16_f32 %2, 0, %2\n\tv_cvt_pk_bf16_f32 %3, 0, %3\n\tv_fmac_f32 %0, %4, %2\n\tv_fmac_f32 %1, %5, %3"
      : "+v"(a), "+v"(b), "+v"(y0), "+v"(y1) : "v"(s0), "v"(s1));
}
struct CPtr { l3p da, ub, rt, pe, pu, pc; l3wq st; u32 vo; };
__device__ __forceinline__ CPtr conv_ptrs(u32* conv, u32* ctr, int w, int lane) {
  CPtr q;
  u32* AA = conv + w * CH2;
  const int e = lane & 15, g = lane >> 4, rp = lane >> 2, cp = lane & 3;
  q.da = (l3p)(AA + (e & 1) * DLEN + 4 * g - (e >> 1) + 7);
  q.ub = (l3p)(AA + U_OFF + 8 * e + 4 * g);
  q.rt = (l3p)(AA + RT_OFF + 4 * (e - 4 * g + 12));
  q.st = (l3wq)(AA + 16 * e + 4 * (g ^ ((e >> 1) & 3)));
  q.pe = (l3p)(conv + 2 * cp * CH2 + 16 * (2 * w + (rp >> 3)) + 4 * (((rp >> 1) & 3) ^ (w & 3)) + 2 * (rp & 1));
  q.pu = (l3p)(conv + 2 * cp * CH2 + U_OFF + 128 + 16 * w + rp);
  q.pc = (l3p)(ctr + 2 * cp);
  q.vo = (u32)(32 * w + 2 * rp) * 4096u + 4u * cp;
  asm volatile("" : "+v"(q.da), "+v"(q.ub), "+v"(q.rt), "+v"(q.st), "+v"(q.pe), "+v"(q.pu), "+v"(q.pc), "+v"(q.vo));
  return q;
}
#define IN(j) ((j) >= S0 && (j) < S1)
template <int TY>
__device__ __forceinline__ void conv8(const CPtr& q, u32* AA, int w, const u32x4* pat, u32* ctr, gch* op) {
  using std::integral_constant;
  constexpr int S0 = TY ? 5 : 0, S1 = TY ? 8 : 5, SUM = TY ? SUM1 : SUM0, WS = S1 - 2, SB0 = TY ? 4 : 0, N = 8 * (S1 - SB0);
  const l3p da = q.da, ub = q.ub, rt = q.rt;
  volatile __attribute__((address_space(3))) u16* pw = (volatile __attribute__((address_space(3))) u16*)(ctr + C_TD) + w;
  f32x4v acc[8];
#pragma unroll
  for (int j = S0; j < S1; ++j) acc[j] = Z4;
  {
    u32x4 Us[2], Vs[2], T2[2][3], T3[4], A16, Z[3], pk;
    auto gen = [&](auto sc) {
      constexpr int s = decltype(sc)::value;
      constexpr bool n01 = IN(s + 2) || IN(s + 3) || IN(s + 4), n2 = s < 4 && (IN(s + 4) || TY);
      f32x4v z0, z1, z2, z3, z4, z5;
      if constexpr (n01) {
        z0 = mma(Us[s & 1], pat[0], Z4);
        z1 = mma(Us[s & 1], pat[1], Z4);
        z2 = mma(Vs[s & 1], pat[0], Z4);
        z3 = mma(Vs[s & 1], pat[1], Z4);
      }
      if constexpr (n2) {
        z4 = mma(Us[s & 1], pat[2], Z4);
        z5 = mma(Vs[s & 1], pat[3], Z4);
      }
      if constexpr (n01) {
        Z[0] = zpack(z0, z1);
        Z[1] = zpack(z2, z3);
      }
      if constexpr (n2) Z[2] = zpack(z4, z5);
    };
    Us[0] = *(l3q)(ub + 120);
    Vs[0] = *(l3q)(ub + 116);
    Us[1] = *(l3q)(ub + 120 + 128);
    Vs[1] = *(l3q)(ub + 116 + 128);
    {
      const u32 kc = (u32)*(const __attribute__((address_space(3))) u16*)((l3c)(AA + RT_OFF) + 16 * 103 + 8) * 0x10001u;
      A16 = *(l3a)(da);
#pragma unroll
      for (int k = 0; k < 3; ++k) {
        T2[0][k] = *(l3q)(rt + 4 * (16 * k));
        T2[1][k] = *(l3q)(rt + 4 * (60 + 16 * k));
        pin(T2[0][k]);
        pin(T2[1][k]);
      }
#pragma unroll
      for (int k = 0; k < (TY ? 4 : 1); ++k) {
        T3[k] = *(l3q)(rt + 4 * (120 + 16 * k));
        pin(T3[k]);
      }
#pragma unroll
      for (int k = 0; k < 4; ++k) pk[k] = pkmulh(pat[4][k], kc);
      pin(pk);
      pin(A16);
      pin(Us[0]); pin(Vs[0]); pin(Us[1]); pin(Vs[1]);
    }
    TS(2);
    gwait<GSLP>(ctr + (w >= 4 || GATEP1 == GATEPOS ? 4 : GATEP1 < 0 ? C_TD : 5), 4u);
    TS(5);
    SB();
    gen(integral_constant<int, 0>{});
    static_for<0, WS>([&](auto sc) {
      constexpr int s = decltype(sc)::value;
      SB();
      if constexpr (IN(s + 2)) acc[s + 2] = mma(A16, Us[s & 1], acc[s + 2]);
      if constexpr (s < 4 && IN(s + 4)) acc[s + 4] = mma(pk, Us[s & 1], acc[s + 4]);
      SB();
      if constexpr (s + 2 < WS) {
        Us[s & 1] = *(l3q)(ub + 120 + 128 * (s + 2));
        Vs[s & 1] = *(l3q)(ub + 116 + 128 * (s + 2));
      }
      SB();
      static_for<0, 2>([&](auto hc) {
        constexpr int h = decltype(hc)::value;
        static_for<2, 5>([&](auto dc) {
          constexpr int dl = decltype(dc)::value;
          if constexpr (IN(s + dl)) acc[s + dl] = mma(Z[h], T2[h][dl - 2], acc[s + dl]);
        });
      });
      if constexpr (s < 4) {
        static_for<4, 8 - s>([&](auto dc) {
          constexpr int dl = decltype(dc)::value;
          if constexpr (IN(s + dl)) acc[s + dl] = mma(Z[2], T3[dl - 4], acc[s + dl]);
        });
      }
      SB();
      if constexpr (s + 1 < WS) gen(integral_constant<int, s + 1>{});
      SB();
    });
  }
  TS(4);
  constexpr int NB = 6, PD = 5;
  u32x4 At[16], Ub[NB], fl;
  u32x2 ya, yb, qd, qs, qr;
  u32 ua, ub2;
  float a0, b0, a1, b1, y00, y01, y10, y11;
  bool slow;
  auto ldU = [&](auto nc) {
    constexpr int n = decltype(nc)::value;
    if constexpr (n < N) Ub[n % NB] = *(l3q)(ub + 120 + 128 * (SB0 + (n >> 3)) - 16 * (n & 7));
  };
  auto ldA = [&](auto rc) {
    constexpr int r = decltype(rc)::value;
    if constexpr (TY) At[8 + r] = *(l3a)(da + 128 - 16 * r);
    At[r] = *(l3a)(da + 256 - 16 * r);
    if constexpr (!TY) At[8 + r] = *(l3a)(da + 128 - 16 * r);
  };
  auto fin = [&](auto ic) {
    constexpr int i = decltype(ic)::value;
    q.st[(SUM + 256 * (i - S0)) / 4] = __builtin_bit_cast(u32x4, acc[i]);
    *pw = (u16)(0xffffu << (i + 1));
  };
  auto outp = [&](auto ic, auto kc) {
    constexpr int i = decltype(ic)::value, k = decltype(kc)::value;
    if constexpr (k == 0) fl = *(l3vq)(ctr + C_TD);
    if constexpr (k == 2) {
      auto done = [&]() { return ((u32)__builtin_amdgcn_readfirstlane((int)(fl[0] | fl[1] | fl[2] | fl[3])) & (0x10001u << i)) == 0u; };
      if (__builtin_expect(!done(), 0)) {
#pragma nounroll
        for (unsigned spins = 0; spins < (1u << 20); ++spins) {
          if (OSLP) __builtin_amdgcn_s_sleep(OSLP);
          fl = *(l3vq)(ctr + C_TD);
          if (done()) break;
        }
      }
      asm volatile("" ::: "memory");
      if constexpr (i == S0) {
        qd = *(l3d)(q.pc + C_DS);
        qs = *(l3d)(q.pc + C_SY);
        qr = *(l3d)(q.pc + C_YR);
      }
      ua = q.pu[128 * i];
      ub2 = q.pu[128 * i + CH2];
      ya = *(l3d)(q.pe + SUM + 256 * (i - S0));
      yb = *(l3d)(q.pe + SUM + 256 * (i - S0) + CH2);
    }
    if constexpr (k == 4) skip2(a0, b0, ua, __uint_as_float(qd[0]));
    if constexpr (k == 5) {
      skip2(a1, b1, ub2, __uint_as_float(qd[1]));
      y00 = __uint_as_float(ya[0]);
      y01 = __uint_as_float(ya[1]);
      y10 = __uint_as_float(yb[0]);
      y11 = __uint_as_float(yb[1]);
      if constexpr (i == S0) slow = __builtin_amdgcn_ballot_w64((qr[0] | qr[1]) != 0u) != 0ull;
      if (__builtin_expect(slow, 0)) {
        y00 = __builtin_amdgcn_ldexpf(y00, (int)qr[0]);
        y01 = __builtin_amdgcn_ldexpf(y01, (int)qr[0]);
        y10 = __builtin_amdgcn_ldexpf(y10, (int)qr[1]);
        y11 = __builtin_amdgcn_ldexpf(y11, (int)qr[1]);
      }
    }
    if constexpr (k == 6) {
      fin2(a0, a1, y00, y10, __uint_as_float(qs[0]), __uint_as_float(qs[1]));
      stg<(NTO & 1)>((gu32*)(sptr(op + (size_t)i * (256 * 4096)) + q.vo), pkb(a0, a1));
    }
    if constexpr (k == 7) {
      fin2(b0, b1, y01, y11, __uint_as_float(qs[0]), __uint_as_float(qs[1]));
      stg<(NTO & 1)>((gu32*)(sptr(op + (size_t)i * (256 * 4096) + 4096) + q.vo), pkb(b0, b1));
    }
  };
  auto ostep = [&](auto nc) {
    constexpr int m = decltype(nc)::value - 9 - OFS, i = SB0 + (m >> 3);
    if constexpr (m >= 0 && IN(i) && i < S1 - 1) outp(integral_constant<int, i>{}, integral_constant<int, m & 7>{});
  };
  ldU(integral_constant<int, 0>{});
  ldA(integral_constant<int, 0>{});
  ldU(integral_constant<int, 1>{});
  ldA(integral_constant<int, 1>{});
  ldU(integral_constant<int, 2>{});
  ldA(integral_constant<int, 2>{});
  ldU(integral_constant<int, 3>{});
  ldU(integral_constant<int, 4>{});
  SB();
  static_for<0, N>([&](auto nc) {
    constexpr int n = decltype(nc)::value, s = SB0 + (n >> 3), rr = n & 7;
    constexpr bool lo = IN(s), hi = IN(s + 1), sw = rr == 7 || !lo;
    if constexpr (sw && hi) acc[s + 1] = mma(At[8 + rr], Ub[n % NB], acc[s + 1]);
    else acc[s] = mma(At[rr], Ub[n % NB], acc[s]);
    SB();
    if constexpr (n < 5) ldA(integral_constant<int, n + 3>{});
    if constexpr (rr == 1 && IN(s - 1)) fin(integral_constant<int, s - 1>{});
    SB();
    if constexpr (sw && hi && lo) acc[s] = mma(At[rr], Ub[n % NB], acc[s]);
    else if constexpr (!sw && hi) acc[s + 1] = mma(At[8 + rr], Ub[n % NB], acc[s + 1]);
    SB();
    ldU(integral_constant<int, n + PD>{});
    ostep(nc);
    SB();
  });
  fin(integral_constant<int, S1 - 1>{});
  TS(3);
  static_for<N, N + 8 + OFS>([&](auto nc) { ostep(nc); });
  static_for<0, 8>([&](auto kc) { outp(integral_constant<int, S1 - 1>{}, kc); });
}

#define NTOUCH 242
__device__ __forceinline__ u32 touch_code() {
  unsigned long long pc;
  asm volatile("s_getpc_b64 %0" : "=s"(pc));
  const u32 line = min(16u * (blockIdx.x >> 3) + 2u * ((threadIdx.x >> 6) & 7u) + (threadIdx.x & 1u), (u32)(NTOUCH - 1));
  return *(const __attribute__((address_space(1))) u32*)(pc + 128u * line);
}
#define K1END() __builtin_amdgcn_endpgm()
__global__ __launch_bounds__(1024, 1) void k1(KParams P, const u16* x, cf* X1T, const u16* u, const u16* wr, const u16* wi, u32* cnt) {
  __shared__ __attribute__((aligned(256))) u32 smw[K1_LDS];
  u32* ctr = smw + K1_CTR;
  const int b = blockIdx.x, tid = threadIdx.x & 255;
  const int grp = __builtin_amdgcn_readfirstlane(threadIdx.x >> 8);
  TSINIT();
  TS(0);
  if (threadIdx.x - 512u < 32u) ctr[threadIdx.x - 512u] = 0u;
  if (grp == 0) {
    {
    brow_block([&](u32x4* xw) {
      typedef const __attribute__((address_space(1))) u32x2* g2p;
      const int w = __builtin_amdgcn_readfirstlane(tid >> 6);
      const u32 vo = 8u * (tid & 15) + 512u * ((tid >> 4) & 3);
      TS(1);
#pragma unroll
      for (int i = 0; i < 2; ++i) {
        gch* xr = sptr((gch*)x + (size_t)(b + 256 * (2 * w + i)) * (2 * DCH));
#pragma unroll
        for (int j = 0; j < 4; ++j) {
          const u32x2 v = ldg<NTX>((g2p)(xr + vo + 128 * j));
          xw[i][j] = v.x;
          xw[i + 2][j] = v.y;
        }
      }
      SB();
      TS(5);
      __builtin_amdgcn_s_setprio(3);
      BAR();
    }, X1T, smw, ctr, b, tid);
    TS(2);
    K1END();
    }
  } else if (grp == 1) {
    typedef const __attribute__((address_space(1))) u32x4* g4p;
    const int w = __builtin_amdgcn_readfirstlane(tid >> 6), lane = tid & 63, lo = lane & 15, g = lane >> 4;
    const int tile = b >> 3, l0 = 64 * tile, d0 = 128 * (b & 7) + 32 * w;
    u32x4 ua[4];
    {
      gch* ub = sptr((gch*)u + (size_t)l0 * (2 * DCH) + 2 * d0);
      const u32 vo = (u32)(((UPAT ? 8 : 16) * (lo >> 2) + (lo & 3)) * (2 * DCH) + 16 * g);
#pragma unroll
      for (int a = 0; a < 4; ++a) ua[a] = ldg<NTU>((g4p)(sptr(ub + (UPAT ? 4 * (a & 1) + 32 * (a >> 1) : 4 * a) * (2 * DCH)) + vo));
    }
    SB();
    TS(5);
    BAR();
    {
    __builtin_amdgcn_s_setprio(2);
    const rsrc_t rws = mkrsrc(X1T);
    u32x4 wq[5];
    {
      typedef u32x4 u32x4a2 __attribute__((aligned(2)));
      typedef const __attribute__((address_space(1))) u32x4a2* g4u;
      const int qk = b & 7, k0 = (qk < 6 ? (qk & 1) * 256 : 0) + 8 * (tid & 31);
      const int sp = qk_q(qk) + 8 * (8 * (b >> 3) + (tid >> 5)), sn = (2048 - sp) & 2047;
      const u32 o1 = 2u * (sp * DF + k0), o2 = 2u * (sn * DF + 505 - k0);
      wq[0] = ldg<NTW>((g4u)((gch*)wr + o1));
      wq[1] = ldg<NTW>((g4u)((gch*)wi + o1));
      wq[2] = ldg<NTW>((g4u)((gch*)wr + o2));
      wq[3] = ldg<NTW>((g4u)((gch*)wi + o2));
    }
    const int wix = (8 * b + (tid & 7)) * DF + 256 * min(tid >> 3, 2);
    const u32 w5a = wr[wix], w5b = wi[wix];
    u32x4 sel[2];
#pragma unroll
    for (int h = 0; h < 2; ++h)
#pragma unroll
      for (int j = 0; j < 4; ++j) {
        const int k = 8 * g + 2 * j - 16 * h - lo;
        sel[h][j] = k == 0 ? 0x3f80u : (k == -1 ? 0x3f800000u : 0u);
      }
    pin(sel[0]); pin(sel[1]);
    SB();
#pragma unroll
    for (int h = 0; h < 2; ++h) {
      f32x4v v[4];
#pragma unroll
      for (int a = 0; a < 4; ++a) v[a] = mmb(ua[a], sel[h], Z4);
      float m = 0.f;
#pragma unroll
      for (int a = 0; a < 4; ++a) {
        m = fmaxf(fmaxf(__builtin_fabsf(v[a][0]), __builtin_fabsf(v[a][1])), m);
        m = fmaxf(fmaxf(__builtin_fabsf(v[a][2]), __builtin_fabsf(v[a][3])), m);
      }
      u32 mm = __float_as_uint(m);
      {
        auto r = __builtin_amdgcn_permlane16_swap(mm, mm, false, false);
        mm = max(r[0], r[1]);
        r = __builtin_amdgcn_permlane32_swap(mm, mm, false, false);
        mm = max(r[0], r[1]);
      }
      const u32 eu = min(max(mm >> 23, 16u), 254u);
      const float usc = __uint_as_float((265u - eu) << 23);
      const int d = d0 + 16 * h + lo;
#pragma unroll
      for (int q = 0; q < 2; ++q) {
        const f32x4v x0 = v[2 * q] * usc, x1 = v[2 * q + 1] * usc;
        st_wt<AUX_U>(rws, UT_G + (d * SEQ + l0 + (UPAT ? 8 * g + 32 * q : 16 * g + 8 * q)) * 2, u32x4{pkh(x0[0], x0[1]), pkh(x0[2], x0[3]), pkh(x1[0], x1[1]), pkh(x1[2], x1[3])});
      }
      if (g == 0) __builtin_amdgcn_raw_buffer_store_b32(eu, rws, UE_OFF + 128 * d + 16 * (tile & 7) + 4 * (tile >> 3), 0, 16);
    }
    TS(2);
    SB();
    const u32 w5 = w5a | (w5b << 16);
    wq[4] = u32x4{w5, 0u, 0u, 0u};
    {
      typedef __attribute__((ext_vector_type(2))) unsigned short u16x2;
      u16x2 m = {0, 0};
#pragma unroll
      for (int i = 0; i < 5; ++i)
#pragma unroll
        for (int j = 0; j < 4; ++j) m = __builtin_elementwise_max(m, __builtin_bit_cast(u16x2, wq[i][j] & 0x7fff7fffu));
      const u32 wm = wave_umax(max((u32)m.x, (u32)m.y));
      if (lane == 0) {
        __hip_atomic_fetch_max((l3u*)(ctr + 2), wm, __ATOMIC_RELAXED, __HIP_MEMORY_SCOPE_WORKGROUP);
        asm volatile("" ::: "memory");
        if (__hip_atomic_fetch_add((l3u*)(ctr + 3), 1u, __ATOMIC_RELAXED, __HIP_MEMORY_SCOPE_WORKGROUP) == 3u) {
          asm volatile("" ::: "memory");
          __builtin_amdgcn_raw_buffer_store_b32(*(volatile l3u*)(ctr + 2), rws, TB_OFF + 4 * b, 0, 16);
        }
      }
    }
    if (tid < 24) {
      const int sr = 8 * b + (tid & 7), n = tid < 8 ? (2048 - sr) & 2047 : sr;
      __builtin_amdgcn_raw_buffer_store_b32(w5, rws, WC_OFF + 1024 * ((tid & 24) + (n & 7)) + 4 * PERM(n >> 3), 0, 16);
    }
    TS(3);
    K1END();
    }
  } else {
    const int p = (b & 7) * 32 + (b >> 3);
    const int c = __builtin_amdgcn_readfirstlane(tid >> 6), lane = tid & 63, d = 4 * p + c;
    u32 tch = touch_code();
    SB();
    __builtin_amdgcn_s_setprio(3);
    KIn kin = kgen_load(P, d, 64 * (grp - 2), lane);
    u32* AA = smw + K1_KG + c * CH_LDS;
    SB();
    TS(5);
    asm volatile("s_waitcnt lgkmcnt(0)" ::: "memory");
    BAR();
    {
    if (grp == 3) __builtin_amdgcn_s_setprio(KPRIO);
    else __builtin_amdgcn_s_setprio(0);
    TS(1);
    f32x16 kacc;
    const int eg = kgen(kin, AA + (grp - 2) * AREA, lane, kacc, [&](auto) {});
    pin(tch);
    TS(2);
    if (grp == 3) {
      l3wq px = (l3wq)(AA + AREA + PX_OFF + 4 * lane);
#pragma unroll
      for (int i = 0; i < 3; ++i)
        px[64 * i] = u32x4{__float_as_uint(kacc[4 * i]), __float_as_uint(kacc[4 * i + 1]), __float_as_uint(kacc[4 * i + 2]), __float_as_uint(kacc[4 * i + 3])};
      *(volatile l3u*)(ctr + 8 + c) = (u32)eg;
    } else {
      gwait<1>(ctr + 8 + c, 1u);
      const int eh = __builtin_amdgcn_readfirstlane((int)*(volatile l3u*)(ctr + 8 + c));
      const int em = max(eg, eh);
      const float ra = __uint_as_float((u32)max(127 - (em - eg), 0) << 23), rh = __uint_as_float((u32)max(127 - (em - eh), 0) << 23);
      l3q px = (l3q)(AA + AREA + PX_OFF + 4 * lane);
      float kv[12];
#pragma unroll
      for (int i = 0; i < 3; ++i) {
        const u32x4 q = px[64 * i];
#pragma unroll
        for (int j = 0; j < 4; ++j) kv[4 * i + j] = fmaf(__uint_as_float(q[j]), rh, kacc[4 * i + j] * ra);
      }
      u32x4 q0, q1;
      const rsrc_t rws = mkrsrc(X1T);
      kpack(kv, lane, q0, q1);
      q1[2] = (u32)em;
      q1[3] = 0u;
      st_wt<AUX_K>(rws, KV_G + 2048 * d + 16 * lane, q0);
      st_wt<AUX_K>(rws, KV_G + 2048 * d + 1024 + 16 * lane, q1);
    }
    TS(3);
    K1END();
    }
  }
}

__global__ __launch_bounds__(768, 1) void k2(cf* X1T, const u16* wr, const u16* wi, const u16* Dskip, u16* out, u32* cnt) {
  __shared__ __attribute__((aligned(256))) u32 smw_all[K2_LDS];
  u32* ctr = smw_all + CTR_OFF;
  const int b = blockIdx.x;
  const int grp = __builtin_amdgcn_readfirstlane(threadIdx.x >> 8);
  const int tid = threadIdx.x & 255;
  gch* X = (gch*)X1T;
  TSINIT();
  TS(0);
  if (threadIdx.x < 32) ctr[threadIdx.x] = (threadIdx.x & 28) == C_TD ? ~0u : 0u;
  if (grp == 0) {
    __builtin_amdgcn_s_setprio(3);
    bwaves(X, out, smw_all, smw_all + 4096, smw_all + SPX_OFF, ctr, b, tid);
    TS(3);
    TS(4);
    TSEND(cnt, blockIdx.x * 4 + (tid >> 6));
    return;
  }
  const int ty = (b >> 3) & 1, oct = OMAP ? 8 * ((b & 7) + 8 * (b >> 7)) + ((b >> 4) & 7) : 16 * (b & 7) + (b >> 4);
  const int w = __builtin_amdgcn_readfirstlane((threadIdx.x >> 6) - 4), lane = tid & 63, d = 8 * oct + w;
  u32* conv = smw_all + CONV_OFF;
  u32* AA = conv + w * CH2;
  typedef const __attribute__((address_space(1))) u32x4* g4p;
  __syncthreads();
  __builtin_amdgcn_s_setprio(APRIO0);
  SB();
  u32x4 ws[4], kq[2], uq[4], ue, pat[5];
  auto wload = [&]() {
    if (grp == WGRP) {
      typedef u32x4 u32x4a2 __attribute__((aligned(2)));
      typedef const __attribute__((address_space(1))) u32x4a2* g4u;
      const int p = (b & 7) * 32 + (b >> 3);
      const int qk = p >> 5, k0 = (qk < 6 ? (qk & 1) * 256 : 0) + 8 * (p & 31);
      const int sp = qk_q(qk) + 8 * (64 * (w & 3) + lane), sn = (2048 - sp) & 2047;
      const u32 o1 = 2u * (sp * DF + k0), o2 = 2u * (sn * DF + 505 - k0);
      ws[0] = ldg<NTW2>((g4u)((gch*)wr + o1));
      ws[1] = ldg<NTW2>((g4u)((gch*)wi + o1));
      ws[2] = ldg<NTW2>((g4u)((gch*)wr + o2));
      ws[3] = ldg<NTW2>((g4u)((gch*)wi + o2));
    }
    SB();
  };
  auto wstage = [&]() {
    if (grp == WGRP) {
      __builtin_amdgcn_s_setprio(3);
      wstage_store(ws, smw_all + 4096, w & 3, lane);
      garrive(ctr + 3);
    }
    __builtin_amdgcn_s_setprio(APRIO1);
    SB();
  };
  {
    gch* kd = sptr(X + KV_G + 2048 * d);
    kq[0] = *(g4p)(kd + 16u * lane);
    kq[1] = *(g4p)(kd + 16u * lane + 1024);
  }
  SB();
  if (WORD == 0) wload();
  gch* ud = sptr(X + UT_G + (size_t)d * (2 * SEQ));
  ue = *(g4p)(sptr(X + UE_OFF + 128 * d) + 16u * (lane >> 3));
  const u32 dsk = Dskip[d];
#pragma unroll
  for (int j = 0; j < 3; ++j) uq[j] = *(g4p)(ud + 16u * lane + 1024 * j);
  uq[3] = *(g4p)(ud + 16u * lane + (ty ? 3072 : 2048));
  SB();
  if (WORD) wload();
  KAddr ka = kaddr((u32)(size_t)(l3w1)AA, lane);
  asm volatile("" : "+v"(ka.z0), "+v"(ka.z1), "+v"(ka.a), "+v"(ka.b), "+v"(ka.t[0]), "+v"(ka.t[1]), "+v"(ka.t[2]), "+v"(ka.t[3]));
  kzero(ka);
  if (lane < 32) *(l3wq)(AA + U_OFF + 4 * lane) = u32x4{0u, 0u, 0u, 0u};
  SB();
  wpatterns(lane, pat);
  asm volatile("" : "+v"(pat[0]), "+v"(pat[1]), "+v"(pat[2]), "+v"(pat[3]), "+v"(pat[4]));
  const CPtr cq = conv_ptrs(conv, ctr, w, lane);
  SB();
  if (WORD < 2) wstage();
  pin(kq[1]);
  kimage(ka, kq[0], kq[1], lane);
  const int ek = __builtin_amdgcn_readfirstlane((int)kq[1][2]);
  TS(1);
  SB();
  if (WORD == 3) wstage();
  {
    const u32 em = wave_umax(max(max(ue[0], ue[1]), max(ue[2], ue[3])));
    u32 dm[4];
#pragma unroll
    for (int j = 0; j < 4; ++j) {
      const float v = __uint_as_float((127u - min(em - ue[j], 40u)) << 23);
      dm[j] = pkh(v, v);
    }
    const int yexp = ek + (int)em - 272;
    const int ycl = min(max(yexp, -126), 127);
    *(volatile l3u*)(ctr + C_SY + w) = (u32)(ycl + 127) << 23;
    *(volatile l3u*)(ctr + C_YR + w) = (u32)(yexp - ycl);
    *(volatile l3u*)(ctr + C_DS + w) = __float_as_uint(bf2f(dsk) * __uint_as_float((em - 11u) << 23));
    pin(dm[0]); pin(dm[1]); pin(dm[2]); pin(dm[3]);
    SB();
#pragma unroll
    for (int j = 0; j < 4; ++j)
      if (j < 3 || ty)
        *(l3wq)(AA + U_OFF + 128 + 4 * (lane + 64 * j)) = u32x4{pkmulh(uq[j][0], dm[j]), pkmulh(uq[j][1], dm[j]), pkmulh(uq[j][2], dm[j]), pkmulh(uq[j][3], dm[j])};
    SB();
  }
  if (WORD == 2) wstage();
  if (grp == 1) __builtin_amdgcn_s_setprio(CPRIO1);
  else __builtin_amdgcn_s_setprio(CPRIO2);
  if (ty) conv8<1>(cq, AA, w, pat, ctr, (gch*)(out + 8 * oct));
  else conv8<0>(cq, AA, w, pat, ctr, (gch*)(out + 8 * oct));
  TS(6);
  TS(0);
  TSEND(cnt, 1024 * grp + blockIdx.x * 4 + (w & 3));
}

extern "C" void kernel_launch(void* const* d_in, const int* in_sizes, int n_in, void* d_out, int out_size,
                              void* d_ws, size_t ws_size, hipStream_t stream) {
  const u16* u = (const u16*)d_in[0];
  KParams P;
  P.Are = (const u16*)d_in[1];
  P.Aim = (const u16*)d_in[2];
  P.Bre = (const u16*)d_in[3];
  P.Bim = (const u16*)d_in[4];
  P.Cre = (const u16*)d_in[5];
  P.Cim = (const u16*)d_in[6];
  P.logdt = (const u16*)d_in[7];
  const u16* Dskip = (const u16*)d_in[8];
  const u16* x = (const u16*)d_in[9];
  const u16* wr = (const u16*)d_in[10];
  const u16* wi = (const u16*)d_in[11];
  u16* out = (u16*)d_out;
  char* ws = (char*)d_ws;
  cf* X1T = (cf*)ws;
  u32* cnt = (u32*)(ws + (size_t)29 * 1024 * 1024);

  k1<<<256, 1024, 0, stream>>>(P, x, X1T, u, wr, wi, cnt);
  k2<<<256, 768, 0, stream>>>(X1T, wr, wi, Dskip, out, cnt);
}
```
